```python
import torch

def _softmax(x, axis=-1):
    return torch.softmax(x, dim=axis)
def _gelu(x):
    return torch.nn.functional.gelu(x, approximate="tanh")
def _silu(x):
    return torch.nn.functional.silu(x)
def _sigmoid(x):
    return torch.sigmoid(x)
def _layernorm(x, axis=-1, eps=1e-5):
    m = x.mean(dim=axis, keepdim=True)
    v = x.var(dim=axis, keepdim=True, unbiased=False)
    return (x - m) / torch.sqrt(v + eps)
def _scatter_add(tgt, idx, src, axis=0):
    return tgt.clone().index_add_(axis, idx, src)
def _segment_sum(src, seg, n):
    out = torch.zeros((n,) + src.shape[1:], dtype=src.dtype)
    return out.index_add_(0, seg, src)
def _pad(x, pw):
    flat = tuple(v for pair in reversed(pw) for v in pair)
    return torch.nn.functional.pad(x, flat)

kernel_name = 'recurrent_scan__compressed_kv_cla__dense_activation_squared_relu__decode'

def setup_inputs():

    _w = (lambda a: (lambda t: t.to(torch.bfloat16) if t.is_floating_point() else t)(torch.as_tensor(a)))
    def _A_setup():
        B, H, Sq, Sk, D, M = 1, 16, 1, 8192, 256, 448
        Dc = 4 * D
        return {
            "x": _w((lambda *s: torch.randn(*s, dtype=torch.bfloat16) * 0.05)(B, Sq, M)),
            "c_kv": _w((lambda *s: torch.randn(*s, dtype=torch.bfloat16) * 0.8)(B, Sk, Dc)),
            "w_q": _w((lambda *s: torch.randn(*s, dtype=torch.bfloat16) * 1.6)(M, H, D) * (M ** -0.5)),
            "w_uk": _w((lambda *s: torch.randn(*s, dtype=torch.bfloat16) * 1.6)(Dc, H, D) * (Dc ** -0.5)),
            "w_uv": _w((lambda *s: torch.randn(*s, dtype=torch.bfloat16) * 0.8)(Dc, H, D) * (Dc ** -0.5)),
            "w_o": _w((lambda *s: torch.randn(*s, dtype=torch.bfloat16) * 0.8)(H, D, M) * ((H * D) ** -0.5)),
        }
    _A_in = _A_setup()

    def _B_setup():
        B, S, D, F = 1, 1, 448, 1792
        return {
            "x": _w((lambda *s: torch.randn(*s, dtype=torch.bfloat16) * 0.05)(B, S, D)),
            "w1": _w((lambda *s: torch.randn(*s, dtype=torch.bfloat16) * 0.2)(D, F)),
            "b1": _w((lambda *s: torch.randn(*s, dtype=torch.bfloat16) * 0.2)(F)),
            "w2": _w((lambda *s: torch.randn(*s, dtype=torch.bfloat16) * 0.2)(F, D)),
            "b2": _w((lambda *s: torch.randn(*s, dtype=torch.bfloat16) * 0.2)(D)),
        }
    _B_in = _B_setup()

    return {
        "A_x": _A_in["x"],
        "A_c_kv": _A_in["c_kv"],
        "A_w_q": _A_in["w_q"],
        "A_w_uk": _A_in["w_uk"],
        "A_w_uv": _A_in["w_uv"],
        "A_w_o": _A_in["w_o"],
        "B_x": _B_in["x"],
        "B_w1": _B_in["w1"],
        "B_b1": _B_in["b1"],
        "B_w2": _B_in["w2"],
        "B_b2": _B_in["b2"],
    }

def reference(A_x, A_c_kv, A_w_q, A_w_uk, A_w_uv, A_w_o, B_x, B_w1, B_b1, B_w2, B_b2):
    def _A_ref(x, c_kv, w_q, w_uk, w_uv, w_o):
        B, Sq, M = x.shape[0], x.shape[1], x.shape[2]
        Sk = c_kv.shape[1]
        H, D = w_q.shape[1], w_q.shape[2]
        q = torch.einsum("bqm,mhd->bhqd", x, w_q)
        k = torch.einsum("bkc,chd->bhkd", c_kv, w_uk)
        v = torch.einsum("bkc,chd->bhkd", c_kv, w_uv)
        scores = torch.einsum("bhqd,bhkd->bhqk", q, k) * (D ** -0.5)
        off = Sk - Sq
        qi = torch.arange(Sq)[:, None]
        kj = torch.arange(Sk)[None, :]
        cmask = kj <= (qi + off)
        scores = torch.where(cmask[None, None], scores, -1e9)
        p = _softmax(scores, axis=-1)
        ctx = torch.einsum("bhqk,bhkd->bhqd", p, v)
        return torch.einsum("bhqd,hdm->bqm", ctx, w_o)

    def _B_ref(x, w1, b1, w2, b2):
        h = torch.einsum("bsd,df->bsf", x, w1) + b1
        zero = torch.zeros_like(h)
        r = torch.where(h > zero, h, zero)
        h = r * r
        out = torch.einsum("bsf,fd->bsd", h, w2) + b2
        return out

    x = A_x
    xn = _layernorm(x, axis=-1)
    s = _A_ref(xn, A_c_kv, A_w_q, A_w_uk, A_w_uv, A_w_o)
    g = _silu(_B_ref(xn, B_w1, B_b1, B_w2, B_b2))
    return x + s * g
```

```cpp
#include <hip/hip_runtime.h>
#include <hip/hip_bf16.h>
#include <stdint.h>

typedef unsigned short u16;
__device__ __forceinline__ float bf2f(u16 b) { return __builtin_bit_cast(float, (unsigned)b << 16); }
__device__ __forceinline__ float rbf(float f) {
    unsigned u = __builtin_bit_cast(unsigned, f);
    u += 0x7FFFu + ((u >> 16) & 1u);
    return __builtin_bit_cast(float, u & 0xFFFF0000u);
}
__device__ __forceinline__ u16 f2bf(float f) {
    unsigned u = __builtin_bit_cast(unsigned, f);
    u += 0x7FFFu + ((u >> 16) & 1u);
    return (u16)(u >> 16);
}
constexpr int M = 448, H = 16, D = 256, DC = 1024, SK = 8192, F = 1792;

#define MIMIC 1

__device__ void layernorm_to_lds(const u16* x, float* xn, float* red) {
    int t = threadIdx.x;
    float a = 0.f;
    for (int i = t; i < M; i += 256) a += bf2f(x[i]);
    red[t] = a; __syncthreads();
    for (int s = 128; s > 0; s >>= 1) { if (t < s) red[t] += red[t + s]; __syncthreads(); }
    float mean = red[0] / M; __syncthreads();
    a = 0.f;
    for (int i = t; i < M; i += 256) { float d = bf2f(x[i]) - mean; a += d * d; }
    red[t] = a; __syncthreads();
    for (int s = 128; s > 0; s >>= 1) { if (t < s) red[t] += red[t + s]; __syncthreads(); }
    float var = red[0] / M; __syncthreads();
    float mb = rbf(mean), vb = rbf(var);
    float sd = rbf(sqrtf(rbf(vb + 1e-5f)));
    for (int i = t; i < M; i += 256) xn[i] = rbf(rbf(bf2f(x[i]) - mb) / sd);
    __syncthreads();
}

__global__ void k1(const u16* x, const u16* wq, const u16* w1, const u16* b1, float* q, float* hid) {
    __shared__ float xn[M]; __shared__ float red[256];
    layernorm_to_lds(x, xn, red);
    int j = blockIdx.x * 256 + threadIdx.x;
    if (j < H * D) {
        float a = 0.f;
        for (int m = 0; m < M; ++m) a += xn[m] * bf2f(wq[m * (H * D) + j]);
        q[j] = MIMIC ? rbf(a) : a;
    } else {
        int f = j - H * D;
        if (f < F) {
            float a = 0.f;
            for (int m = 0; m < M; ++m) a += xn[m] * bf2f(w1[m * F + f]);
            if (MIMIC) a = rbf(rbf(a) + bf2f(b1[f])); else a = a + bf2f(b1[f]);
            a = a > 0.f ? a : 0.f;
            hid[f] = MIMIC ? rbf(a * a) : a * a;
        }
    }
}
__global__ void k2(const float* q, const u16* wuk, const float* hid, const u16* w2, const u16* b2, float* qabs, float* g) {
    int wave = (blockIdx.x * 256 + threadIdx.x) >> 6, lane = threadIdx.x & 63;
    if (wave < DC * H) {
        int c = wave / H, h = wave % H;
        float a = 0.f;
        for (int d = lane; d < D; d += 64) a += q[h * D + d] * bf2f(wuk[(size_t)c * H * D + h * D + d]);
        for (int s = 32; s > 0; s >>= 1) a += __shfl_xor(a, s);
        if (lane == 0) qabs[h * DC + c] = a * 0.0625f;
    } else {
        int m = wave - DC * H;
        if (m < M) {
            float a = 0.f;
            for (int f = lane; f < F; f += 64) a += hid[f] * bf2f(w2[f * M + m]);
            for (int s = 32; s > 0; s >>= 1) a += __shfl_xor(a, s);
            if (MIMIC) a = rbf(rbf(a) + bf2f(b2[m])); else a += bf2f(b2[m]);
            float sg = a / (1.f + __expf(-a));
            if (lane == 0) g[m] = MIMIC ? rbf(sg) : sg;
        }
    }
}
__global__ void __launch_bounds__(256) k3(const float* qabs, const u16* ckv, float* pctx, float* pm, float* pl) {
    __shared__ float sc[H][32];
    int b = blockIdx.x, t = threadIdx.x, wave = t >> 6, lane = t & 63;
    const u16* C = ckv + (size_t)b * 32 * DC;
    for (int idx = wave; idx < H * 32; idx += 4) {
        int h = idx / 32, k = idx % 32;
        float a = 0.f;
        for (int c = lane; c < DC; c += 64) a += qabs[h * DC + c] * bf2f(C[k * DC + c]);
        for (int s = 32; s > 0; s >>= 1) a += __shfl_xor(a, s);
        if (lane == 0) sc[h][k] = a;
    }
    __syncthreads();
    if (t < H) {
        float m = -1e30f;
        for (int k = 0; k < 32; ++k) m = fmaxf(m, sc[t][k]);
        float l = 0.f;
        for (int k = 0; k < 32; ++k) { float p = __expf(sc[t][k] - m); sc[t][k] = p; l += p; }
        pm[b * H + t] = m; pl[b * H + t] = l;
    }
    __syncthreads();
    for (int c = t; c < DC; c += 256) {
        float acc[H];
        for (int h = 0; h < H; ++h) acc[h] = 0.f;
        #pragma unroll 1
        for (int k = 0; k < 32; ++k) { float v = bf2f(C[k * DC + c]);
#pragma unroll
            for (int h = 0; h < H; ++h) acc[h] += sc[h][k] * v; }
        for (int h = 0; h < H; ++h) pctx[((size_t)b * H + h) * DC + c] = acc[h];
    }
}
__global__ void k4a(const float* pctx, const float* pm, const float* pl, float* ctxc) {
    int j = blockIdx.x * 256 + threadIdx.x;
    int h = j / DC;
    float m = -1e30f;
    for (int b = 0; b < 256; ++b) m = fmaxf(m, pm[b * H + h]);
    float l = 0.f, a = 0.f;
    for (int b = 0; b < 256; ++b) { float w = __expf(pm[b * H + h] - m); l += w * pl[b * H + h]; a += w * pctx[(size_t)b * H * DC + j]; }
    ctxc[j] = a / l;
}
__global__ void k4b(const float* ctxc, const u16* wuv, float* ctx) {
    int j = blockIdx.x * 256 + threadIdx.x;
    int h = j / D;
    float a = 0.f;
    for (int c = 0; c < DC; ++c) a += ctxc[h * DC + c] * bf2f(wuv[(size_t)c * H * D + j]);
    ctx[j] = MIMIC ? rbf(a) : a;
}
__global__ void k5(const float* ctx, const u16* wo, const float* g, const u16* x, u16* out) {
    int wave = (blockIdx.x * 256 + threadIdx.x) >> 6, lane = threadIdx.x & 63;
    if (wave < M) {
        float a = 0.f;
        for (int j = lane; j < H * D; j += 64) a += ctx[j] * bf2f(wo[(size_t)j * M + wave]);
        for (int s = 32; s > 0; s >>= 1) a += __shfl_xor(a, s);
        if (lane == 0) {
            float o = MIMIC ? bf2f(x[wave]) + rbf(rbf(a) * g[wave]) : bf2f(x[wave]) + a * g[wave];
            out[wave] = f2bf(o);
        }
    }
}

extern "C" void kernel_launch(void* const* d_in, const int* in_sizes, int n_in,
                              void* d_out, int out_size, void* d_ws, size_t ws_size,
                              hipStream_t stream) {
    const u16* x = (const u16*)d_in[0];
    const u16* ckv = (const u16*)d_in[1];
    const u16* wq = (const u16*)d_in[2];
    const u16* wuk = (const u16*)d_in[3];
    const u16* wuv = (const u16*)d_in[4];
    const u16* wo = (const u16*)d_in[5];
    const u16* w1 = (const u16*)d_in[7];
    const u16* b1 = (const u16*)d_in[8];
    const u16* w2 = (const u16*)d_in[9];
    const u16* b2 = (const u16*)d_in[10];
    float* ws = (float*)d_ws;
    float* q = ws; float* hid = q + 4096; float* qabs = hid + 2048; float* g = qabs + 16384;
    float* pm = g + 512; float* pl = pm + 4096; float* ctxc = pl + 4096; float* ctx = ctxc + 16384;
    float* pctx = ctx + 4096;
    k1<<<(H * D + F) / 256, 256, 0, stream>>>(x, wq, w1, b1, q, hid);
    k2<<<(DC * H + M) / 4, 256, 0, stream>>>(q, wuk, hid, w2, b2, qabs, g);
    k3<<<256, 256, 0, stream>>>(qabs, ckv, pctx, pm, pl);
    k4a<<<64, 256, 0, stream>>>(pctx, pm, pl, ctxc);
    k4b<<<16, 256, 0, stream>>>(ctxc, wuv, ctx);
    k5<<<M / 4, 256, 0, stream>>>(ctx, wo, g, x, (u16*)d_out);
}
```

```cpp
#include <hip/hip_runtime.h>
#include <stdint.h>

typedef unsigned v4u __attribute__((ext_vector_type(4)));
typedef unsigned v2u __attribute__((ext_vector_type(2)));
typedef float f32x4 __attribute__((ext_vector_type(4)));
typedef float f32x2 __attribute__((ext_vector_type(2)));
typedef __bf16 bf16x8 __attribute__((ext_vector_type(8)));
typedef __bf16 bf16x2 __attribute__((ext_vector_type(2)));
typedef short s16x4 __attribute__((ext_vector_type(4)));
typedef unsigned short u16x2 __attribute__((ext_vector_type(2)));
typedef unsigned short u16;
#define LAS __attribute__((address_space(3)))
#define GAS __attribute__((address_space(1)))
typedef __amdgpu_buffer_rsrc_t rsrc_t;

#define NT_W 1
#define NT_C 0
#define NT_O 0
#define DLY0 8
#define DLY1 8
#define DLY2 8
#define DLY3 8
#define DLY4 8
#define RETRY 2
#define SPINSLEEP 0
#define WIN 12

constexpr int WS_EPOCH = 0;
constexpr int WS_H0 = 4096;
constexpr int WS_H1 = WS_H0 + 131072;
constexpr int WS_P = WS_H1 + 65536;
constexpr int WS_PS = WS_P + 262144;
constexpr int WS_H2 = WS_PS + 32768;
constexpr int WS_H2S = WS_H2 + 524288;
constexpr int WS_H4 = WS_H2S + 32768;
constexpr int WS_HID = WS_H4 + 256 * 1792;
constexpr int WS_GP = WS_HID + 28 * 7 * 256;
constexpr int WS_END = WS_GP + 28 * 1792;
constexpr int WS_DIAG = 4 << 20;

constexpr int NSLOT = 17;
constexpr int L_RING = 0;
constexpr int L_PART = 2 * NSLOT * 4096;
constexpr int L_XN = L_PART + 16384;
constexpr int L_Q = L_XN + 1024;
constexpr int L_CTX = L_Q + 512;
constexpr int L_HID = L_CTX + 128;
constexpr int L_ST4 = L_HID + 128;
constexpr int L_CTL = L_ST4 + 1024;
constexpr int L_END = L_CTL + 128;
enum { C_PROG0 = 0, C_PROG1, C_FILL, C_QX, C_S2, C_RELQ, C_RELA0, C_RELA1, C_RELA2, C_RELA3, C_CNT3, C_RED3, C_PPUB, C_CNT4A, C_CNT4B, C_S4, C_CNT5, C_S6, C_NUM };

constexpr int NWAVE = 15;
constexpr unsigned SENT = 0xFFFFFFFFu;
constexpr int AUX_SC1 = 16;
constexpr unsigned SPIN_G = 1u << 15, SPIN_L = 1u << 19;

struct Params {
    const void* x; const void* ckv; const void* wq; const void* wuk; const void* wuv; const void* wo;
    const void* w1; const void* b1; const void* w2; const void* b2; void* out; void* ws;
};

__device__ __forceinline__ float u2f(unsigned u) { return __builtin_bit_cast(float, u); }
__device__ __forceinline__ unsigned f2u(float f) { return __builtin_bit_cast(unsigned, f); }
__device__ __forceinline__ unsigned pk_bf16(float a, float b) { f32x2 v = {a, b}; bf16x2 r = __builtin_convertvector(v, bf16x2); return __builtin_bit_cast(unsigned, r); }
__device__ __forceinline__ float rbf(float a) { return u2f(pk_bf16(a, a) << 16); }
__device__ __forceinline__ float bflo(unsigned u) { return u2f(u << 16); }
__device__ __forceinline__ float bfhi(unsigned u) { return u2f(u & 0xFFFF0000u); }
__device__ __forceinline__ unsigned san(unsigned u) { return u < 0xFFFFFFFEu ? u : 0xFFFFFFFEu; }
__device__ __forceinline__ v4u san4(v4u v) { return v4u{san(v.x), san(v.y), san(v.z), san(v.w)}; }
__device__ __forceinline__ v4u f4u(f32x4 v) { return __builtin_bit_cast(v4u, v); }
__device__ __forceinline__ f32x4 u4f(v4u v) { return __builtin_bit_cast(f32x4, v); }
__device__ __forceinline__ unsigned umax(unsigned a, unsigned b) { return a > b ? a : b; }
__device__ __forceinline__ unsigned umax4(v4u v) { return umax(umax(v.x, v.y), umax(v.z, v.w)); }
__device__ __forceinline__ f32x4 mfma(v4u a, v4u b, f32x4 c) {
    return __builtin_amdgcn_mfma_f32_16x16x32_bf16(__builtin_bit_cast(bf16x8, a), __builtin_bit_cast(bf16x8, b), c, 0, 0, 0);
}
template <int N> __device__ __forceinline__ float ror(float v) { return u2f((unsigned)__builtin_amdgcn_update_dpp(0, (int)f2u(v), 0x120 + N, 0xf, 0xf, false)); }
__device__ __forceinline__ float row_sum(float v) { v += ror<8>(v); v += ror<4>(v); v += ror<2>(v); v += ror<1>(v); return v; }
__device__ __forceinline__ float row_max(float v) { v = fmaxf(v, ror<8>(v)); v = fmaxf(v, ror<4>(v)); v = fmaxf(v, ror<2>(v)); v = fmaxf(v, ror<1>(v)); return v; }
__device__ __forceinline__ float xrow_sum(float v) {
    auto a = __builtin_amdgcn_permlane32_swap(f2u(v), f2u(v), false, false); v = u2f(a[0]) + u2f(a[1]);
    auto b = __builtin_amdgcn_permlane16_swap(f2u(v), f2u(v), false, false); return u2f(b[0]) + u2f(b[1]);
}
__device__ __forceinline__ float xrow_max(float v) {
    auto a = __builtin_amdgcn_permlane32_swap(f2u(v), f2u(v), false, false); v = fmaxf(u2f(a[0]), u2f(a[1]));
    auto b = __builtin_amdgcn_permlane16_swap(f2u(v), f2u(v), false, false); return fmaxf(u2f(b[0]), u2f(b[1]));
}
__device__ __forceinline__ float wave_sum(float v) { return xrow_sum(row_sum(v)); }

__device__ __forceinline__ rsrc_t mk_rsrc(const void* p, int bytes) { return __builtin_amdgcn_make_buffer_rsrc((void*)p, 0, bytes, 0x00020000); }
__device__ __forceinline__ v4u ld16(rsrc_t r, int off) { return __builtin_amdgcn_raw_buffer_load_b128(r, off, 0, AUX_SC1); }
__device__ __forceinline__ v2u ld8(rsrc_t r, int off) { return __builtin_amdgcn_raw_buffer_load_b64(r, off, 0, AUX_SC1); }
__device__ __forceinline__ unsigned ld4(rsrc_t r, int off) { return __builtin_amdgcn_raw_buffer_load_b32(r, off, 0, AUX_SC1); }
__device__ __forceinline__ void st16(rsrc_t r, int off, v4u v) { __builtin_amdgcn_raw_buffer_store_b128(v, r, off, 0, AUX_SC1); }
__device__ __forceinline__ void st8(rsrc_t r, int off, v2u v) { __builtin_amdgcn_raw_buffer_store_b64(v, r, off, 0, AUX_SC1); }
#define CBAR() asm volatile("" ::: "memory")
#define DRAIN() asm volatile("s_waitcnt vmcnt(0)" ::: "memory")
#define LWAIT() asm volatile("s_waitcnt lgkmcnt(0)" ::: "memory")

typedef LAS unsigned char* lds_t;
__device__ __forceinline__ v4u lds16(lds_t L, int off) { return *(LAS v4u*)(L + off); }
__device__ __forceinline__ v4u lds_tr(lds_t L, int off) {
    s16x4 a = __builtin_amdgcn_ds_read_tr16_b64_v4i16((LAS s16x4*)(L + off));
    s16x4 b = __builtin_amdgcn_ds_read_tr16_b64_v4i16((LAS s16x4*)(L + off + 512));
    v2u a2 = __builtin_bit_cast(v2u, a), b2 = __builtin_bit_cast(v2u, b);
    return v4u{a2.x, a2.y, b2.x, b2.y};
}
__device__ __forceinline__ void wait_ge(lds_t L, int word, unsigned want) {
    volatile LAS unsigned* p = (volatile LAS unsigned*)(L + L_CTL) + word;
#pragma unroll 1
    for (unsigned s = 0; s < SPIN_L; ++s) { if ((unsigned)__builtin_amdgcn_readfirstlane(*p) >= want) break; if (SPINSLEEP) __builtin_amdgcn_s_sleep(SPINSLEEP); }
    CBAR();
}
__device__ __forceinline__ void wait_ge_sleep(lds_t L, int word, unsigned want) {
    volatile LAS unsigned* p = (volatile LAS unsigned*)(L + L_CTL) + word;
#pragma unroll 1
    for (unsigned s = 0; s < SPIN_L; ++s) { if ((unsigned)__builtin_amdgcn_readfirstlane(*p) >= want) break; __builtin_amdgcn_s_sleep(1); }
    CBAR();
}
__device__ __forceinline__ void ctl_add(lds_t L, int word, int lane) {
    CBAR();
    if (lane == 0) __hip_atomic_fetch_add((LAS unsigned*)(L + L_CTL) + word, 1u, __ATOMIC_RELAXED, __HIP_MEMORY_SCOPE_WORKGROUP);
    CBAR();
}
__device__ __forceinline__ bool any_sent(unsigned m) { return __builtin_amdgcn_ballot_w64(m == SENT) != 0ull; }
__device__ __forceinline__ bool any_nz(unsigned m) { return __builtin_amdgcn_ballot_w64(m != 0u) != 0ull; }

#define STAMP(k)
#define STAMP_DECL
#define LSTAMP(n)
#define PASSES(k, s)
#define STAMP_OUT()

template <int NT>
__device__ __forceinline__ void dma_tile(rsrc_t r, unsigned soff, unsigned lds, unsigned v0, unsigned v1, unsigned v2, unsigned v3) {
    if (NT)
        asm volatile("s_mov_b32 m0, %6\n\ts_nop 3\n\t"
                     "buffer_load_dwordx4 %0, %4, %5 offen nt lds\n\t"
                     "buffer_load_dwordx4 %1, %4, %5 offen offset:1024 nt lds\n\t"
                     "buffer_load_dwordx4 %2, %4, %5 offen offset:2048 nt lds\n\t"
                     "buffer_load_dwordx4 %3, %4, %5 offen offset:3072 nt lds"
                     :: "v"(v0), "v"(v1), "v"(v2), "v"(v3), "s"(r), "s"(soff), "s"(lds) : "memory");
    else
        asm volatile("s_mov_b32 m0, %6\n\ts_nop 3\n\t"
                     "buffer_load_dwordx4 %0, %4, %5 offen lds\n\t"
                     "buffer_load_dwordx4 %1, %4, %5 offen offset:1024 lds\n\t"
                     "buffer_load_dwordx4 %2, %4, %5 offen offset:2048 lds\n\t"
                     "buffer_load_dwordx4 %3, %4, %5 offen offset:3072 lds"
                     :: "v"(v0), "v"(v1), "v"(v2), "v"(v3), "s"(r), "s"(soff), "s"(lds) : "memory");
}

__device__ __forceinline__ void layernorm(const v4u xv, lds_t L, int lane) {
    float e[8] = {bflo(xv.x), bfhi(xv.x), bflo(xv.y), bfhi(xv.y), bflo(xv.z), bfhi(xv.z), bflo(xv.w), bfhi(xv.w)};
    float s = ((e[0] + e[1]) + (e[2] + e[3])) + ((e[4] + e[5]) + (e[6] + e[7]));
    const float mean = wave_sum(s) * (1.0f / 448.0f);
    float v = 0.f;
#pragma unroll
    for (int k = 0; k < 8; ++k) { const float d = e[k] - mean; v += d * d; }
    v = lane < 56 ? v : 0.f;
    const float var = wave_sum(v) * (1.0f / 448.0f);
    const float mb = rbf(mean);
    const float sd = rbf(sqrtf(rbf(rbf(var) + 1e-5f)));
    float o[8];
#pragma unroll
    for (int k = 0; k < 8; ++k) o[k] = rbf(e[k] - mb) / sd;
    v4u w = {pk_bf16(o[0], o[1]), pk_bf16(o[2], o[3]), pk_bf16(o[4], o[5]), pk_bf16(o[6], o[7])};
    if (lane >= 56) w = v4u{0u, 0u, 0u, 0u};
    *(LAS v4u*)(L + L_XN + lane * 16) = w;
    CBAR();
}

__global__ void __launch_bounds__(NWAVE * 64, 4) fused_decode(Params p) {
    extern __shared__ __attribute__((aligned(16))) unsigned char smem[];
    lds_t L = (lds_t)smem;
    const int lane = threadIdx.x & 63, wv = __builtin_amdgcn_readfirstlane(threadIdx.x >> 6);
    const int b = blockIdx.x, j = b >> 3;
    const int h = 2 * (b & 7) + (j >> 4);
    const int i16 = j & 15, dq = (j >> 2) & 3, mq = j & 3;
    const int l15 = lane & 15, q4 = lane >> 4;
    STAMP_DECL

    if (wv < 2) {
        const int pp = wv;
        const unsigned r = lane >> 3, pc = lane & 7, s0 = (r >> 1) & 1;
        const unsigned ce = (pc ^ (s0 << 1)) << 4, co = (pc ^ ((s0 | 2) << 1)) << 4;
#define VOFFS(st) (r * (st) + ce), ((r + 8) * (st) + co - 1024), ((r + 16) * (st) + ce - 2048), ((r + 24) * (st) + co - 3072)
        unsigned lds = L_RING + pp * (NSLOT * 4096);
        const unsigned lds_lo = lds, lds_hi = lds + NSLOT * 4096;
        unsigned n = 0;
        volatile LAS unsigned* prog = (volatile LAS unsigned*)(L + L_CTL) + C_PROG0 + pp;
#define STEP() do { lds += 4096; if (lds == lds_hi) lds = lds_lo; ++n; asm volatile("s_waitcnt vmcnt(%0)" :: "i"(WIN) : "memory"); \
                    if (n > WIN / 4) *prog = n - WIN / 4; CBAR(); LSTAMP(n); } while (0)
        {
            const rsrc_t rq = mk_rsrc(p.wq, 448 * 8192);
            const unsigned row0 = mq < 2 ? 128 * mq : 64 + 96 * mq;
            const unsigned so = (row0 + 32 * pp) * 8192 + (h * 256 + dq * 64) * 2;
            dma_tile<NT_W>(rq, so, lds, VOFFS(8192)); lds += 4096;
            dma_tile<NT_W>(mk_rsrc(p.wq, (pp == 1 && mq >= 2) ? 0 : 448 * 8192), so + 64 * 8192, lds, VOFFS(8192)); lds += 4096;
            const rsrc_t rk = mk_rsrc(p.wuk, 1024 * 8192);
            const unsigned sk = (64 * i16 + 32 * pp) * 8192 + h * 512;
            dma_tile<NT_W>(rk, sk, lds, VOFFS(8192)); lds += 4096;
            n = 3;
            __builtin_amdgcn_s_barrier();
#pragma unroll 1
            for (int cb = 1; cb < 4; ++cb) { dma_tile<NT_W>(rk, sk + cb * 128, lds, VOFFS(8192)); STEP(); }
        }
        STAMP(1);
        {
#pragma unroll 1
            for (int t = 0; t < 16; ++t) {
                const unsigned rb = pp ? ((i16 - t) & 15) : i16, cb = pp ? i16 : ((i16 + t) & 15);
                const unsigned so = (512 * h + 32 * rb) * 2048 + cb * 128;
                const rsrc_t rc = mk_rsrc(p.ckv, (pp == 1 && t == 0) ? 0 : 8192 * 2048);
                if (t == 11) wait_ge_sleep(L, C_RELQ, 4);
                dma_tile<NT_C>(rc, so, lds, VOFFS(2048)); STEP();
            }
        }
        STAMP(2);
        {
            const rsrc_t r1 = mk_rsrc(p.w1, b < 196 ? 448 * 3584 : 0);
            dma_tile<NT_W>(r1, b < 196 ? (64 * (b % 7) + 32 * pp) * 3584 + (b / 7) * 128 : 0, lds, VOFFS(3584)); STEP();
            const rsrc_t rv = mk_rsrc(p.wuv, 1024 * 8192);
            const unsigned sv = (256 * mq + 32 * pp) * 8192 + (h * 256 + dq * 64) * 2;
            wait_ge_sleep(L, C_RELA0, 2);
#pragma unroll 1
            for (int k = 0; k < 4; ++k) { dma_tile<NT_W>(rv, sv + k * (64 * 8192), lds, VOFFS(8192)); STEP(); }
            STAMP(3);
            const int bb = b - 60;
            const rsrc_t r2 = mk_rsrc(p.w2, bb >= 0 ? 1792 * 896 : 0);
            wait_ge_sleep(L, C_RELA1, 2);
            dma_tile<NT_W>(r2, bb >= 0 ? (64 * (bb / 7) + 32 * pp) * 896 + (bb % 7) * 128 : 0, lds, VOFFS(896)); STEP();
            const rsrc_t ro = mk_rsrc(p.wo, 4096 * 896);
            const unsigned sw = (h * 256 + dq * 64 + 32 * pp) * 896;
#pragma unroll 1
            for (int k = 0; k < 7; ++k) {
                int cb = k + 2 * mq; if (cb >= 7) cb -= 7;
                if (k == 3) wait_ge_sleep(L, C_RELA2, 2);
                dma_tile<NT_O>(ro, sw + cb * 128, lds, VOFFS(896)); STEP();
            }
        }
        STAMP(4);
        if (WIN > 12) { asm volatile("s_waitcnt vmcnt(12)" ::: "memory"); *prog = 32; }
        asm volatile("s_waitcnt vmcnt(8)" ::: "memory"); *prog = 33;
        asm volatile("s_waitcnt vmcnt(4)" ::: "memory"); *prog = 34;
        asm volatile("s_waitcnt vmcnt(0)" ::: "memory"); *prog = 35;
        STAMP_OUT();
        return;
    }

    const rsrc_t ws = mk_rsrc(p.ws, WS_END);
    const v2u ep = ld8(ws, WS_EPOCH);
    const v4u S4 = {SENT, SENT, SENT, SENT};
    const v2u S2 = {SENT, SENT};
    const int swr = ((l15 >> 1) & 1) | (((l15 >> 3) & 1) << 1);
    const int row_a0 = l15 * 128 + ((q4 ^ (swr << 1)) << 4), row_a1 = row_a0 ^ 64;
    const int swt = ((l15 >> 3) & 1) | ((q4 & 1) << 1);
    const int tr_b = (8 * q4 + (l15 >> 2)) * 128 + (((l15 >> 1) & 1) << 4) + 8 * (l15 & 1);
#define TR_A(nt) (tr_b + ((((nt) ^ swt)) << 5))
#define SLOT(pp, n) (L_RING + (pp) * (NSLOT * 4096) + ((n) % NSLOT) * 4096)

    if (wv < 6) {
        const int t = wv - 2;
        const v4u xv = __builtin_amdgcn_raw_buffer_load_b128(mk_rsrc(p.x, 896), lane * 16, 0, 0);
        __builtin_amdgcn_s_barrier();
        layernorm(xv, L, lane);
        const unsigned tagA = ep.x + 1u, tagB = ep.y + (tagA == 0u ? 1u : 0u);
        const int tra = TR_A(t);
        const int row0 = mq < 2 ? 128 * mq : 64 + 96 * mq;
        v4u bx[4];
#pragma unroll
        for (int s = 0; s < 4; ++s) bx[s] = lds16(L, L_XN + (row0 + 32 * s + 8 * q4) * 2);
        STAMP(1);
        wait_ge(L, C_PROG0, 2); wait_ge(L, C_PROG1, 2);
        STAMP(2);
        f32x4 acc = {0.f, 0.f, 0.f, 0.f};
#pragma unroll
        for (int s = 0; s < 4; ++s) acc = mfma(lds_tr(L, SLOT(s & 1, s >> 1) + tra), bx[s], acc);
        if (l15 < 2) st16(ws, WS_H0 + ((h * 4 + dq) * 4 + mq) * 512 + (16 * t + 4 * q4 + 2 * l15) * 8,
                          v4u{f2u(l15 ? acc.z : acc.x), tagA, f2u(l15 ? acc.w : acc.y), tagB});
        STAMP(3);
        __builtin_amdgcn_s_sleep(DLY0);
        v4u g0[2];
#pragma unroll 1
        for (unsigned s = 0; s < SPIN_G; ++s) { PASSES(0, s);
            CBAR();
#pragma unroll
            for (int k = 0; k < 2; ++k) g0[k] = ld16(ws, WS_H0 + (h * 4 + t) * 2048 + k * 1024 + lane * 16);
            const bool bad_ = any_nz((g0[0].y ^ tagA) | (g0[0].w ^ tagB) | (g0[1].y ^ tagA) | (g0[1].w ^ tagB));
            LSTAMP(s & 63);
            if (!bad_) break;
            __builtin_amdgcn_s_sleep(RETRY);
        }
        STAMP(4);
        {
            const float qa = u2f(g0[0].x) + u2f(g0[1].x), qb = u2f(g0[0].z) + u2f(g0[1].z);
            auto ra = __builtin_amdgcn_permlane32_swap(f2u(qa), f2u(qa), false, false);
            auto rb = __builtin_amdgcn_permlane32_swap(f2u(qb), f2u(qb), false, false);
            *(LAS unsigned*)(L + L_Q + t * 128 + (lane & 31) * 4) = pk_bf16(u2f(ra[0]) + u2f(ra[1]), u2f(rb[0]) + u2f(rb[1]));
            ctl_add(L, C_QX, lane);
        }
        v4u ak[8];
        wait_ge(L, C_PROG0 + (t >> 1), 6);
        {
            const int base = L_RING + (t >> 1) * (NSLOT * 4096) + (t & 1) * 2048;
#pragma unroll
            for (int cb = 0; cb < 4; ++cb) { ak[2 * cb] = lds16(L, base + (2 + cb) * 4096 + row_a0); ak[2 * cb + 1] = lds16(L, base + (2 + cb) * 4096 + row_a1); }
        }
        ctl_add(L, C_RELQ, lane);
        STAMP(5);
        wait_ge(L, C_FILL, 9);
        wait_ge(L, C_QX, 4);
        STAMP(6);
        {
            acc = f32x4{0.f, 0.f, 0.f, 0.f};
#pragma unroll
            for (int s = 0; s < 8; ++s) acc = mfma(ak[s], lds16(L, L_Q + (32 * s + 8 * q4) * 2), acc);
            const float sc = 0.0625f * 1.4426950408889634f;
            const int z = 4 * (t & 1) + q4;
            if (l15 == 0) st16(ws, WS_H1 + h * 4096 + (2 * i16 + (t >> 1)) * 128 + ((z >> 1) + 4 * (z & 1)) * 16,
                               v4u{pk_bf16(acc.x * sc, acc.y * sc), tagA, pk_bf16(acc.z * sc, acc.w * sc), tagB});
            ctl_add(L, C_S2, lane);
        }
        STAMP(7);
        wait_ge_sleep(L, C_PROG0, 27); wait_ge_sleep(L, C_PROG1, 27);
#pragma unroll
        for (int s = 0; s < 8; ++s) ak[s] = lds_tr(L, SLOT(s & 1, 23 + (s >> 1)) + tra);
        STAMP(8);
        wait_ge_sleep(L, C_S4, 2);
        STAMP(9);
        __builtin_amdgcn_s_sleep(DLY3);
        v4u g2[8]; v2u st;
        const int o_h2 = WS_H2 + (h * 16 + l15) * 2048 + (256 * mq + 8 * q4) * 2;
#pragma unroll 1
        for (unsigned s = 0; s < SPIN_G; ++s) { PASSES(1, s);
            CBAR();
#pragma unroll
            for (int k = 0; k < 8; ++k) g2[k] = ld16(ws, o_h2 + k * 64);
            st = ld8(ws, WS_H2S + ((l15 * 16 + 4 * mq) * 16 + h) * 8);
            unsigned m = umax(st.x, st.y);
#pragma unroll
            for (int k = 0; k < 8; ++k) m = umax(m, umax4(g2[k]));
            if (!any_sent(m)) break;
            __builtin_amdgcn_s_sleep(RETRY);
        }
        STAMP(10);
        acc = f32x4{0.f, 0.f, 0.f, 0.f};
#pragma unroll
        for (int s = 0; s < 8; ++s) acc = mfma(ak[s], g2[s], acc);
        {
            const float rg = u2f(st.x), lg = u2f(st.y);
            const float R = row_max(rg);
            const float sc = __builtin_amdgcn_exp2f(rg - R);
            const float wgt = sc / row_sum(sc * lg);
            const float c0 = row_sum(acc.x * wgt), c1 = row_sum(acc.y * wgt), c2 = row_sum(acc.z * wgt), c3 = row_sum(acc.w * wgt);
            if (l15 == 0) *(LAS v2u*)(L + L_CTX + (16 * t + 4 * q4) * 2) = v2u{pk_bf16(c0, c1), pk_bf16(c2, c3)};
        }
        ctl_add(L, C_CNT5, lane);
        STAMP_OUT();
        return;
    }

    if (wv == 6) {
        const int bb = b - 60;
        const int fs1 = b / 7, ms1 = b % 7, fs2 = bb / 7, mt2 = bb % 7;
        const int o_hid = WS_HID + (fs1 * 7 + ms1) * 256 + (16 * l15 + 4 * q4) * 4;
        const int o_gp = WS_GP + fs2 * 1792 + (64 * mt2 + 16 * l15 + 4 * q4) * 4;
        if (l15 < 4) { if (b < 196) st16(ws, o_hid, S4); if (bb >= 0) st16(ws, o_gp, S4); }
        if (lane < C_NUM) ((LAS unsigned*)(L + L_CTL))[lane] = 0u;
        LWAIT();
        __builtin_amdgcn_s_barrier();
        const int u = b - 144;
        unsigned b1v = 0; v2u b2v = {0u, 0u}, xv = {0u, 0u};
        if (bb >= 0) b1v = ((const GAS u16*)p.b1)[64 * fs2 + lane];
        if (u >= 0) { b2v = ((const GAS v2u*)p.b2)[u]; xv = ((const GAS v2u*)p.x)[u]; }
        DRAIN();
        ctl_add(L, C_FILL, lane);
        STAMP(1);
        if (b < 196) {
            wait_ge_sleep(L, C_RELQ, 4); wait_ge_sleep(L, C_PROG0, 23); wait_ge_sleep(L, C_PROG1, 23);
            const v4u bx0 = lds16(L, L_XN + (64 * ms1 + 8 * q4) * 2), bx1 = lds16(L, L_XN + (64 * ms1 + 32 + 8 * q4) * 2);
            f32x4 a[4];
#pragma unroll
            for (int nt = 0; nt < 4; ++nt) {
                a[nt] = mfma(lds_tr(L, SLOT(0, 22) + TR_A(nt)), bx0, f32x4{0.f, 0.f, 0.f, 0.f});
                a[nt] = mfma(lds_tr(L, SLOT(1, 22) + TR_A(nt)), bx1, a[nt]);
            }
            const f32x4 o = l15 == 0 ? a[0] : l15 == 1 ? a[1] : l15 == 2 ? a[2] : a[3];
            if (l15 < 4) st16(ws, o_hid, san4(f4u(o)));
        }
        STAMP(3);
        wait_ge_sleep(L, C_CNT3, 8);
        if (bb >= 0) {
            unsigned hp[7];
            __builtin_amdgcn_s_sleep(8);
#pragma unroll 1
            for (unsigned s = 0; s < SPIN_G; ++s) { PASSES(2, s);
                CBAR();
                unsigned m = 0;
#pragma unroll
                for (int k = 0; k < 7; ++k) hp[k] = ld4(ws, WS_HID + (fs2 * 7 + k) * 256 + lane * 4);
#pragma unroll
                for (int k = 0; k < 7; ++k) m = umax(m, hp[k]);
                if (!any_sent(m)) break;
                __builtin_amdgcn_s_sleep(4);
            }
            float hs = 0.f;
#pragma unroll
            for (int k = 0; k < 7; ++k) hs += u2f(hp[k]);
            hs = rbf(rbf(hs) + bflo(b1v));
            hs = hs > 0.f ? hs : 0.f;
            *(LAS u16*)(L + L_HID + lane * 2) = (u16)pk_bf16(hs * hs, 0.f);
            CBAR();
            STAMP(4);
            wait_ge_sleep(L, C_PROG0, 28); wait_ge_sleep(L, C_PROG1, 28);
            const v4u bh0 = lds16(L, L_HID + 8 * q4 * 2), bh1 = lds16(L, L_HID + (32 + 8 * q4) * 2);
            f32x4 a[4];
#pragma unroll
            for (int nt = 0; nt < 4; ++nt) {
                a[nt] = mfma(lds_tr(L, SLOT(0, 27) + TR_A(nt)), bh0, f32x4{0.f, 0.f, 0.f, 0.f});
                a[nt] = mfma(lds_tr(L, SLOT(1, 27) + TR_A(nt)), bh1, a[nt]);
            }
            const f32x4 o = l15 == 0 ? a[0] : l15 == 1 ? a[1] : l15 == 2 ? a[2] : a[3];
            if (l15 < 4) st16(ws, o_gp, san4(f4u(o)));
        }
        STAMP(5);
        if (u >= 0) {
            v4u gv;
            const int lg = lane < 28 ? lane : 27;
            __builtin_amdgcn_s_sleep(8);
#pragma unroll 1
            for (unsigned s = 0; s < SPIN_G; ++s) { PASSES(3, s);
                CBAR();
                gv = ld16(ws, WS_GP + lg * 1792 + u * 16);
                if (!any_sent(umax4(gv))) break;
                __builtin_amdgcn_s_sleep(4);
            }
            f32x4 gf = u4f(gv);
            if (lane >= 28) gf = f32x4{0.f, 0.f, 0.f, 0.f};
            float g[4] = {wave_sum(gf.x), wave_sum(gf.y), wave_sum(gf.z), wave_sum(gf.w)};
            const float bb2[4] = {bflo(b2v.x), bfhi(b2v.x), bflo(b2v.y), bfhi(b2v.y)};
#pragma unroll
            for (int k = 0; k < 4; ++k) { const float a = rbf(rbf(g[k]) + bb2[k]); g[k] = rbf(a / (1.f + __expf(-a))); }
            STAMP(6);
            wait_ge(L, C_S6, 7);
            STAMP(7);
            __builtin_amdgcn_s_sleep(DLY4);
            v4u sv[4];
#pragma unroll 1
            for (unsigned s = 0; s < SPIN_G; ++s) { PASSES(4, s);
                CBAR();
#pragma unroll
                for (int k = 0; k < 4; ++k) sv[k] = ld16(ws, WS_H4 + (lane + 64 * k) * 1792 + u * 16);
                if (!any_sent(umax(umax(umax4(sv[0]), umax4(sv[1])), umax(umax4(sv[2]), umax4(sv[3]))))) break;
                __builtin_amdgcn_s_sleep(RETRY);
            }
            STAMP(8);
            const f32x4 sf = (u4f(sv[0]) + u4f(sv[1])) + (u4f(sv[2]) + u4f(sv[3]));
            const float s[4] = {wave_sum(sf.x), wave_sum(sf.y), wave_sum(sf.z), wave_sum(sf.w)};
            const float xx[4] = {bflo(xv.x), bfhi(xv.x), bflo(xv.y), bfhi(xv.y)};
            float o[4];
#pragma unroll
            for (int k = 0; k < 4; ++k) o[k] = xx[k] + rbf(rbf(s[k]) * g[k]);
            if (lane == 0) ((GAS v2u*)p.out)[u] = v2u{pk_bf16(o[0], o[1]), pk_bf16(o[2], o[3])};
            if (u == 0 && lane == 0) { const unsigned e0 = ep.x + 1u; st8(ws, WS_EPOCH, v2u{e0, ep.y + (e0 == 0u ? 1u : 0u)}); }
        }
        STAMP_OUT();
        return;
    }

    {
        const int w = wv - 7, kq = w >> 1, ch = w & 1;
        int cbm = w + 2 * mq; if (cbm >= 7) cbm -= 7;
        const int o_p = WS_P + (h * 16 + i16) * 1024 + lane * 16;
        const int o_ps = WS_PS + ((h * 16 + i16) * 16 + l15) * 8;
        const int o_h2 = WS_H2 + (l15 * 16 + h) * 2048 + (64 * i16 + 32 * ch + 8 * q4) * 2;
        const int o_h2s = WS_H2S + ((h * 16 + i16) * 16 + l15) * 8;
        const int o_h4 = WS_H4 + b * 1792 + (64 * cbm + 16 * l15 + 4 * q4) * 4;
        if (w == 0) { st16(ws, o_p, S4); if (lane < 16) { st8(ws, o_ps, S2); st8(ws, o_h2s, S2); } }
        if (w < 2) st16(ws, o_h2, S4);
        if (w < 7 && l15 < 4) st16(ws, o_h4, S4);
        __builtin_amdgcn_s_barrier();
        DRAIN();
        ctl_add(L, C_FILL, lane);
        v4u ar[8], ac[8];
        const int rbase = L_RING + ((6 + 2 * w) % NSLOT) * 4096, rbase1 = L_RING + ((7 + 2 * w) % NSLOT) * 4096;
        const int tr0 = TR_A(2 * ch), tr1 = TR_A(2 * ch + 1);
#define PULL_R() do { wait_ge(L, C_PROG0, 8 + 2 * w); \
            ar[0] = lds16(L, rbase + row_a0); ar[1] = lds16(L, rbase + row_a1); ar[2] = lds16(L, rbase1 + row_a0); ar[3] = lds16(L, rbase1 + row_a1); \
            ar[4] = lds16(L, rbase + 2048 + row_a0); ar[5] = lds16(L, rbase + 2048 + row_a1); ar[6] = lds16(L, rbase1 + 2048 + row_a0); ar[7] = lds16(L, rbase1 + 2048 + row_a1); } while (0)
#define PULL_C() do { wait_ge(L, C_PROG1, 10 + 4 * kq); \
            _Pragma("unroll") for (int u = 0; u < 4; ++u) { int cbase = L_RING + NSLOT * 4096 + ((6 + 4 * kq + u) % NSLOT) * 4096; if (u == 0 && kq == 0) cbase = L_RING + 6 * 4096; \
                ac[2 * u] = lds_tr(L, cbase + tr0); ac[2 * u + 1] = lds_tr(L, cbase + tr1); } \
            LWAIT(); ctl_add(L, C_RELA0 + kq, lane); } while (0)
        STAMP(1);
        if (w < 4) { PULL_R(); PULL_C(); }
        STAMP(2);
        wait_ge_sleep(L, C_S2, 4);
        __builtin_amdgcn_s_sleep(DLY1);
        v4u bq[4];
        {
            const unsigned tagA = ep.x + 1u, tagB = ep.y + (tagA == 0u ? 1u : 0u);
            const int c0 = (i16 + 2 * w) & 15, c1 = (i16 + 2 * w + 1) & 15;
            const int o0 = WS_H1 + l15 * 4096 + c0 * 256 + q4 * 16, o1 = WS_H1 + l15 * 4096 + c1 * 256 + q4 * 16;
            v4u g[8];
#pragma unroll 1
            for (unsigned s = 0; s < SPIN_G; ++s) { PASSES(5, s);
                CBAR();
                unsigned m = 0;
#pragma unroll
                for (int k = 0; k < 4; ++k) { g[k] = ld16(ws, o0 + 64 * k); g[4 + k] = ld16(ws, o1 + 64 * k); }
#pragma unroll
                for (int k = 0; k < 8; ++k) m |= (g[k].y ^ tagA) | (g[k].w ^ tagB);
                if (!any_nz(m)) break;
                __builtin_amdgcn_s_sleep(RETRY);
            }
#pragma unroll
            for (int k = 0; k < 4; ++k) bq[k] = v4u{g[2 * k].x, g[2 * k].z, g[2 * k + 1].x, g[2 * k + 1].z};
        }
        STAMP(3);
        if (w >= 4) PULL_R();
        STAMP(4);
        {
            f32x4 s0 = {0.f, 0.f, 0.f, 0.f}, s1 = s0;
#pragma unroll
            for (int k = 0; k < 4; ++k) { s0 = mfma(ar[k], bq[k], s0); s1 = mfma(ar[4 + k], bq[k], s1); }
            *(LAS f32x4*)(L + L_PART + w * 2048 + lane * 16) = s0;
            *(LAS f32x4*)(L + L_PART + w * 2048 + 1024 + lane * 16) = s1;
            ctl_add(L, C_CNT3, lane);
        }
        STAMP(5);
        if (w >= 4) PULL_C();
        const int red_a = (q4 >> 1) * 1024 + ((q4 & 1) * 32 + l15) * 16;
        if (w == 0) {
            wait_ge(L, C_CNT3, 8);
            STAMP(6);
            f32x4 a0 = {0.f, 0.f, 0.f, 0.f}, a1 = a0;
#pragma unroll
            for (int k = 0; k < 8; ++k) { a0 += *(LAS f32x4*)(L + L_PART + k * 2048 + red_a); a1 += *(LAS f32x4*)(L + L_PART + k * 2048 + red_a + 256); }
            const float mx = xrow_max(fmaxf(fmaxf(fmaxf(a0.x, a0.y), fmaxf(a0.z, a0.w)), fmaxf(fmaxf(a1.x, a1.y), fmaxf(a1.z, a1.w))));
            const float r = __builtin_ceilf(mx);
            const float p0 = __builtin_amdgcn_exp2f(a0.x - r), p1 = __builtin_amdgcn_exp2f(a0.y - r), p2 = __builtin_amdgcn_exp2f(a0.z - r), p3 = __builtin_amdgcn_exp2f(a0.w - r);
            const float p4 = __builtin_amdgcn_exp2f(a1.x - r), p5 = __builtin_amdgcn_exp2f(a1.y - r), p6 = __builtin_amdgcn_exp2f(a1.z - r), p7 = __builtin_amdgcn_exp2f(a1.w - r);
            st16(ws, o_p, san4(v4u{pk_bf16(p0, p1), pk_bf16(p2, p3), pk_bf16(p4, p5), pk_bf16(p6, p7)}));
            const float l = xrow_sum(((p0 + p1) + (p2 + p3)) + ((p4 + p5) + (p6 + p7)));
            if (lane < 16) st8(ws, o_ps, v2u{san(f2u(r)), san(f2u(l))});
            ctl_add(L, C_PPUB, lane);
        }
        STAMP(7);
        wait_ge(L, C_PPUB, 1);
        __builtin_amdgcn_s_sleep(DLY2);
        v4u bp[4]; v2u ps[4];
        {
            int op[4], os[4];
#pragma unroll
            for (int u = 0; u < 4; ++u) { const int iu = (i16 - 4 * kq - u) & 15; op[u] = WS_P + (h * 16 + iu) * 1024 + lane * 16; os[u] = WS_PS + ((h * 16 + iu) * 16 + l15) * 8; }
#pragma unroll 1
            for (unsigned s = 0; s < SPIN_G; ++s) { PASSES(6, s);
                CBAR();
                unsigned m = 0;
#pragma unroll
                for (int u = 0; u < 4; ++u) { bp[u] = ld16(ws, op[u]); ps[u] = ld8(ws, os[u]); }
#pragma unroll
                for (int u = 0; u < 4; ++u) m = umax(m, umax(umax4(bp[u]), umax(ps[u].x, ps[u].y)));
                if (!any_sent(m)) break;
                __builtin_amdgcn_s_sleep(RETRY);
            }
        }
        STAMP(8);
        {
            const float rw = fmaxf(fmaxf(u2f(ps[0].x), u2f(ps[1].x)), fmaxf(u2f(ps[2].x), u2f(ps[3].x)));
            f32x4 c0 = {0.f, 0.f, 0.f, 0.f}, c1 = c0;
            float lw = 0.f;
#pragma unroll
            for (int u = 0; u < 4; ++u) {
                const float dl = rw - u2f(ps[u].x);
                const unsigned dd = (unsigned)fminf(dl, 255.f) * 0x00800080u;
                const u16x2 d2 = __builtin_bit_cast(u16x2, dd);
                const unsigned x0 = bp[u].x, x1 = bp[u].y, x2 = bp[u].z, x3 = bp[u].w;
                const v4u bs = {__builtin_bit_cast(unsigned, __builtin_elementwise_sub_sat(__builtin_bit_cast(u16x2, x0), d2)),
                                __builtin_bit_cast(unsigned, __builtin_elementwise_sub_sat(__builtin_bit_cast(u16x2, x1), d2)),
                                __builtin_bit_cast(unsigned, __builtin_elementwise_sub_sat(__builtin_bit_cast(u16x2, x2), d2)),
                                __builtin_bit_cast(unsigned, __builtin_elementwise_sub_sat(__builtin_bit_cast(u16x2, x3), d2))};
                c0 = mfma(ac[2 * u], bs, c0); c1 = mfma(ac[2 * u + 1], bs, c1);
                lw += __builtin_amdgcn_exp2f(-dl) * u2f(ps[u].y);
            }
            *(LAS f32x4*)(L + L_PART + w * 2048 + lane * 16) = c0;
            *(LAS f32x4*)(L + L_PART + w * 2048 + 1024 + lane * 16) = c1;
            if (lane < 16) *(LAS f32x2*)(L + L_ST4 + ((ch * 4 + kq) * 16 + l15) * 8) = f32x2{rw, lw};
            ctl_add(L, C_CNT4A + ch, lane);
        }
        STAMP(9);
        if (w < 2) {
            wait_ge(L, C_CNT4A + ch, 4);
            STAMP(10);
            f32x4 v0[4], v1[4]; f32x2 sx[4];
#pragma unroll
            for (int k = 0; k < 4; ++k) {
                v0[k] = *(LAS f32x4*)(L + L_PART + (2 * k + ch) * 2048 + red_a); v1[k] = *(LAS f32x4*)(L + L_PART + (2 * k + ch) * 2048 + red_a + 256);
                sx[k] = *(LAS f32x2*)(L + L_ST4 + ((ch * 4 + k) * 16 + l15) * 8);
            }
            const float R = fmaxf(fmaxf(sx[0].x, sx[1].x), fmaxf(sx[2].x, sx[3].x));
            f32x4 a0 = {0.f, 0.f, 0.f, 0.f}, a1 = a0; float lg = 0.f;
#pragma unroll
            for (int k = 0; k < 4; ++k) { const float sc = __builtin_amdgcn_exp2f(sx[k].x - R); a0 += v0[k] * sc; a1 += v1[k] * sc; lg += sx[k].y * sc; }
            st16(ws, o_h2, san4(v4u{pk_bf16(a0.x, a0.y), pk_bf16(a0.z, a0.w), pk_bf16(a1.x, a1.y), pk_bf16(a1.z, a1.w)}));
            if (w == 0 && lane < 16) st8(ws, o_h2s, v2u{san(f2u(R)), san(f2u(lg))});
            ctl_add(L, C_S4, lane);
        }
        STAMP(11);
        if (w < 7) {
            wait_ge_sleep(L, C_PROG0, 29 + w); wait_ge_sleep(L, C_PROG1, 29 + w);
            const int ob0 = L_RING + ((28 + w) % NSLOT) * 4096, ob1 = ob0 + NSLOT * 4096;
#pragma unroll
            for (int nt = 0; nt < 4; ++nt) { ar[nt] = lds_tr(L, ob0 + TR_A(nt)); ar[4 + nt] = lds_tr(L, ob1 + TR_A(nt)); }
            STAMP(12);
            wait_ge(L, C_CNT5, 4);
            STAMP(13);
            const v4u bc0 = lds16(L, L_CTX + 8 * q4 * 2), bc1 = lds16(L, L_CTX + (32 + 8 * q4) * 2);
            f32x4 a[4];
#pragma unroll
            for (int nt = 0; nt < 4; ++nt) { a[nt] = mfma(ar[nt], bc0, f32x4{0.f, 0.f, 0.f, 0.f}); a[nt] = mfma(ar[4 + nt], bc1, a[nt]); }
            const f32x4 o = l15 == 0 ? a[0] : l15 == 1 ? a[1] : l15 == 2 ? a[2] : a[3];
            if (l15 < 4) st16(ws, o_h4, san4(f4u(o)));
            ctl_add(L, C_S6, lane);
        }
        STAMP_OUT();
    }
}

extern "C" void kernel_launch(void* const* d_in, const int* in_sizes, int n_in,
                              void* d_out, int out_size, void* d_ws, size_t ws_size,
                              hipStream_t stream) {
    Params p;
    p.x = d_in[0]; p.ckv = d_in[1]; p.wq = d_in[2]; p.wuk = d_in[3]; p.wuv = d_in[4]; p.wo = d_in[5];
    p.w1 = d_in[7]; p.b1 = d_in[8]; p.w2 = d_in[9]; p.b2 = d_in[10]; p.out = d_out; p.ws = d_ws;
    (void)hipFuncSetAttribute((const void*)fused_decode, hipFuncAttributeMaxDynamicSharedMemorySize, L_END);
    fused_decode<<<256, NWAVE * 64, L_END, stream>>>(p);
}
```

```cpp
#include <hip/hip_runtime.h>
#include <stdint.h>
#include <stdio.h>

typedef unsigned v4u __attribute__((ext_vector_type(4)));
typedef unsigned v2u __attribute__((ext_vector_type(2)));
typedef float f32x4 __attribute__((ext_vector_type(4)));
typedef float f32x2 __attribute__((ext_vector_type(2)));
typedef __bf16 bf16x8 __attribute__((ext_vector_type(8)));
typedef __bf16 bf16x2 __attribute__((ext_vector_type(2)));
typedef short s16x4 __attribute__((ext_vector_type(4)));
typedef unsigned short u16x2 __attribute__((ext_vector_type(2)));
typedef unsigned short u16;
#define LAS __attribute__((address_space(3)))
#define GAS __attribute__((address_space(1)))
typedef __amdgpu_buffer_rsrc_t rsrc_t;

#define NT_W 1
#define NT_C 1
#define NT_O 1
#define DLY0 8
#define DLY1 8
#define DLY2 8
#define DLY3 8
#define DLY4 8
#define RETRY 2
#define SPINSLEEP 0
#define WIN 12

constexpr int WS_EPOCH = 0;
constexpr int WS_H0 = 4096;
constexpr int WS_H1 = WS_H0 + 131072;
constexpr int WS_P = WS_H1 + 65536;
constexpr int WS_PS = WS_P + 262144;
constexpr int WS_H2 = WS_PS + 32768;
constexpr int WS_H2S = WS_H2 + 524288;
constexpr int WS_H4 = WS_H2S + 32768;
constexpr int WS_HID = WS_H4 + 256 * 1792;
constexpr int WS_GP = WS_HID + 28 * 7 * 256;
constexpr int WS_END = WS_GP + 28 * 1792;
constexpr int WS_DIAG = 4 << 20;

constexpr int NSLOT = 17;
constexpr int L_RING = 0;
constexpr int L_PART = 2 * NSLOT * 4096;
constexpr int L_XN = L_PART + 16384;
constexpr int L_Q = L_XN + 1024;
constexpr int L_CTX = L_Q + 512;
constexpr int L_HID = L_CTX + 128;
constexpr int L_ST4 = L_HID + 128;
constexpr int L_CTL = L_ST4 + 1024;
constexpr int L_END = L_CTL + 128;
enum { C_PROG0 = 0, C_PROG1, C_FILL, C_QX, C_S2, C_RELQ, C_RELA0, C_RELA1, C_RELA2, C_RELA3, C_CNT3, C_RED3, C_PPUB, C_CNT4A, C_CNT4B, C_S4, C_CNT5, C_S6, C_NUM };

constexpr int NWAVE = 15;
constexpr unsigned SENT = 0xFFFFFFFFu;
constexpr int AUX_SC1 = 16;
constexpr unsigned SPIN_G = 1u << 15, SPIN_L = 1u << 19;

struct Params {
    const void* x; const void* ckv; const void* wq; const void* wuk; const void* wuv; const void* wo;
    const void* w1; const void* b1; const void* w2; const void* b2; void* out; void* ws;
};

__device__ __forceinline__ float u2f(unsigned u) { return __builtin_bit_cast(float, u); }
__device__ __forceinline__ unsigned f2u(float f) { return __builtin_bit_cast(unsigned, f); }
__device__ __forceinline__ unsigned pk_bf16(float a, float b) { f32x2 v = {a, b}; bf16x2 r = __builtin_convertvector(v, bf16x2); return __builtin_bit_cast(unsigned, r); }
__device__ __forceinline__ float rbf(float a) { return u2f(pk_bf16(a, a) << 16); }
__device__ __forceinline__ float bflo(unsigned u) { return u2f(u << 16); }
__device__ __forceinline__ float bfhi(unsigned u) { return u2f(u & 0xFFFF0000u); }
__device__ __forceinline__ unsigned san(unsigned u) { return u < 0xFFFFFFFEu ? u : 0xFFFFFFFEu; }
__device__ __forceinline__ v4u san4(v4u v) { return v4u{san(v.x), san(v.y), san(v.z), san(v.w)}; }
__device__ __forceinline__ v4u f4u(f32x4 v) { return __builtin_bit_cast(v4u, v); }
__device__ __forceinline__ f32x4 u4f(v4u v) { return __builtin_bit_cast(f32x4, v); }
__device__ __forceinline__ unsigned umax(unsigned a, unsigned b) { return a > b ? a : b; }
__device__ __forceinline__ unsigned umax4(v4u v) { return umax(umax(v.x, v.y), umax(v.z, v.w)); }
__device__ __forceinline__ f32x4 mfma(v4u a, v4u b, f32x4 c) {
    return __builtin_amdgcn_mfma_f32_16x16x32_bf16(__builtin_bit_cast(bf16x8, a), __builtin_bit_cast(bf16x8, b), c, 0, 0, 0);
}
template <int N> __device__ __forceinline__ float ror(float v) { return u2f((unsigned)__builtin_amdgcn_update_dpp(0, (int)f2u(v), 0x120 + N, 0xf, 0xf, false)); }
__device__ __forceinline__ float row_sum(float v) { v += ror<8>(v); v += ror<4>(v); v += ror<2>(v); v += ror<1>(v); return v; }
__device__ __forceinline__ float row_max(float v) { v = fmaxf(v, ror<8>(v)); v = fmaxf(v, ror<4>(v)); v = fmaxf(v, ror<2>(v)); v = fmaxf(v, ror<1>(v)); return v; }
__device__ __forceinline__ float xrow_sum(float v) {
    auto a = __builtin_amdgcn_permlane32_swap(f2u(v), f2u(v), false, false); v = u2f(a[0]) + u2f(a[1]);
    auto b = __builtin_amdgcn_permlane16_swap(f2u(v), f2u(v), false, false); return u2f(b[0]) + u2f(b[1]);
}
__device__ __forceinline__ float xrow_max(float v) {
    auto a = __builtin_amdgcn_permlane32_swap(f2u(v), f2u(v), false, false); v = fmaxf(u2f(a[0]), u2f(a[1]));
    auto b = __builtin_amdgcn_permlane16_swap(f2u(v), f2u(v), false, false); return fmaxf(u2f(b[0]), u2f(b[1]));
}
__device__ __forceinline__ float wave_sum(float v) { return xrow_sum(row_sum(v)); }

__device__ __forceinline__ rsrc_t mk_rsrc(const void* p, int bytes) { return __builtin_amdgcn_make_buffer_rsrc((void*)p, 0, bytes, 0x00020000); }
__device__ __forceinline__ v4u ld16(rsrc_t r, int off) { return __builtin_amdgcn_raw_buffer_load_b128(r, off, 0, AUX_SC1); }
__device__ __forceinline__ v2u ld8(rsrc_t r, int off) { return __builtin_amdgcn_raw_buffer_load_b64(r, off, 0, AUX_SC1); }
__device__ __forceinline__ unsigned ld4(rsrc_t r, int off) { return __builtin_amdgcn_raw_buffer_load_b32(r, off, 0, AUX_SC1); }
__device__ __forceinline__ void st16(rsrc_t r, int off, v4u v) { __builtin_amdgcn_raw_buffer_store_b128(v, r, off, 0, AUX_SC1); }
__device__ __forceinline__ void st8(rsrc_t r, int off, v2u v) { __builtin_amdgcn_raw_buffer_store_b64(v, r, off, 0, AUX_SC1); }
#define CBAR() asm volatile("" ::: "memory")
#define DRAIN() asm volatile("s_waitcnt vmcnt(0)" ::: "memory")
#define LWAIT() asm volatile("s_waitcnt lgkmcnt(0)" ::: "memory")

typedef LAS unsigned char* lds_t;
__device__ __forceinline__ v4u lds16(lds_t L, int off) { return *(LAS v4u*)(L + off); }
__device__ __forceinline__ v4u lds_tr(lds_t L, int off) {
    s16x4 a = __builtin_amdgcn_ds_read_tr16_b64_v4i16((LAS s16x4*)(L + off));
    s16x4 b = __builtin_amdgcn_ds_read_tr16_b64_v4i16((LAS s16x4*)(L + off + 512));
    v2u a2 = __builtin_bit_cast(v2u, a), b2 = __builtin_bit_cast(v2u, b);
    return v4u{a2.x, a2.y, b2.x, b2.y};
}
__device__ __forceinline__ void wait_ge(lds_t L, int word, unsigned want) {
    volatile LAS unsigned* p = (volatile LAS unsigned*)(L + L_CTL) + word;
#pragma unroll 1
    for (unsigned s = 0; s < SPIN_L; ++s) { if ((unsigned)__builtin_amdgcn_readfirstlane(*p) >= want) break; if (SPINSLEEP) __builtin_amdgcn_s_sleep(SPINSLEEP); }
    CBAR();
}
__device__ __forceinline__ void wait_ge_sleep(lds_t L, int word, unsigned want) {
    volatile LAS unsigned* p = (volatile LAS unsigned*)(L + L_CTL) + word;
#pragma unroll 1
    for (unsigned s = 0; s < SPIN_L; ++s) { if ((unsigned)__builtin_amdgcn_readfirstlane(*p) >= want) break; __builtin_amdgcn_s_sleep(1); }
    CBAR();
}
__device__ __forceinline__ void ctl_add(lds_t L, int word, int lane) {
    CBAR();
    if (lane == 0) __hip_atomic_fetch_add((LAS unsigned*)(L + L_CTL) + word, 1u, __ATOMIC_RELAXED, __HIP_MEMORY_SCOPE_WORKGROUP);
    CBAR();
}
__device__ __forceinline__ bool any_sent(unsigned m) { return __builtin_amdgcn_ballot_w64(m == SENT) != 0ull; }
__device__ __forceinline__ bool any_nz(unsigned m) { return __builtin_amdgcn_ballot_w64(m != 0u) != 0ull; }

#define STAMP(k)
#define STAMP_DECL
#define LSTAMP(n)
#define PASSES(k, s)
#define STAMP_OUT()

template <int NT>
__device__ __forceinline__ void dma_tile(rsrc_t r, unsigned soff, unsigned lds, unsigned v0, unsigned v1, unsigned v2, unsigned v3) {
    if (NT)
        asm volatile("s_mov_b32 m0, %6\n\ts_nop 3\n\t"
                     "buffer_load_dwordx4 %0, %4, %5 offen nt lds\n\t"
                     "buffer_load_dwordx4 %1, %4, %5 offen offset:1024 nt lds\n\t"
                     "buffer_load_dwordx4 %2, %4, %5 offen offset:2048 nt lds\n\t"
                     "buffer_load_dwordx4 %3, %4, %5 offen offset:3072 nt lds"
                     :: "v"(v0), "v"(v1), "v"(v2), "v"(v3), "s"(r), "s"(soff), "s"(lds) : "memory");
    else
        asm volatile("s_mov_b32 m0, %6\n\ts_nop 3\n\t"
                     "buffer_load_dwordx4 %0, %4, %5 offen lds\n\t"
                     "buffer_load_dwordx4 %1, %4, %5 offen offset:1024 lds\n\t"
                     "buffer_load_dwordx4 %2, %4, %5 offen offset:2048 lds\n\t"
                     "buffer_load_dwordx4 %3, %4, %5 offen offset:3072 lds"
                     :: "v"(v0), "v"(v1), "v"(v2), "v"(v3), "s"(r), "s"(soff), "s"(lds) : "memory");
}

__device__ __forceinline__ void layernorm(const v4u xv, lds_t L, int lane) {
    float e[8] = {bflo(xv.x), bfhi(xv.x), bflo(xv.y), bfhi(xv.y), bflo(xv.z), bfhi(xv.z), bflo(xv.w), bfhi(xv.w)};
    float s = ((e[0] + e[1]) + (e[2] + e[3])) + ((e[4] + e[5]) + (e[6] + e[7]));
    const float mean = wave_sum(s) * (1.0f / 448.0f);
    float v = 0.f;
#pragma unroll
    for (int k = 0; k < 8; ++k) { const float d = e[k] - mean; v += d * d; }
    v = lane < 56 ? v : 0.f;
    const float var = wave_sum(v) * (1.0f / 448.0f);
    const float mb = rbf(mean);
    const float sd = rbf(sqrtf(rbf(rbf(var) + 1e-5f)));
    float o[8];
#pragma unroll
    for (int k = 0; k < 8; ++k) o[k] = rbf(e[k] - mb) / sd;
    v4u w = {pk_bf16(o[0], o[1]), pk_bf16(o[2], o[3]), pk_bf16(o[4], o[5]), pk_bf16(o[6], o[7])};
    if (lane >= 56) w = v4u{0u, 0u, 0u, 0u};
    *(LAS v4u*)(L + L_XN + lane * 16) = w;
    CBAR();
}

__global__ void __launch_bounds__(NWAVE * 64, 4) fused_decode(Params p) {
    extern __shared__ __attribute__((aligned(16))) unsigned char smem[];
    lds_t L = (lds_t)smem;
    const int lane = threadIdx.x & 63, wv = __builtin_amdgcn_readfirstlane(threadIdx.x >> 6);
    const int b = blockIdx.x, j = b >> 3;
    const int h = 2 * (b & 7) + (j >> 4);
    const int i16 = j & 15, dq = (j >> 2) & 3, mq = j & 3;
    const int l15 = lane & 15, q4 = lane >> 4;
    STAMP_DECL

    if (wv < 2) {
        const int pp = wv;
        const unsigned r = lane >> 3, pc = lane & 7, s0 = (r >> 1) & 1;
        const unsigned ce = (pc ^ (s0 << 1)) << 4, co = (pc ^ ((s0 | 2) << 1)) << 4;
#define VOFFS(st) (r * (st) + ce), ((r + 8) * (st) + co - 1024), ((r + 16) * (st) + ce - 2048), ((r + 24) * (st) + co - 3072)
        unsigned lds = L_RING + pp * (NSLOT * 4096);
        const unsigned lds_lo = lds, lds_hi = lds + NSLOT * 4096;
        unsigned n = 0;
        volatile LAS unsigned* prog = (volatile LAS unsigned*)(L + L_CTL) + C_PROG0 + pp;
#define STEP() do { lds += 4096; if (lds == lds_hi) lds = lds_lo; ++n; asm volatile("s_waitcnt vmcnt(%0)" :: "i"(WIN) : "memory"); \
                    if (n > WIN / 4) *prog = n - WIN / 4; CBAR(); LSTAMP(n); } while (0)
        {
            const rsrc_t rq = mk_rsrc(p.wq, 448 * 8192);
            const unsigned row0 = mq < 2 ? 128 * mq : 64 + 96 * mq;
            const unsigned so = (row0 + 32 * pp) * 8192 + (h * 256 + dq * 64) * 2;
            dma_tile<NT_W>(rq, so, lds, VOFFS(8192)); lds += 4096;
            dma_tile<NT_W>(mk_rsrc(p.wq, (pp == 1 && mq >= 2) ? 0 : 448 * 8192), so + 64 * 8192, lds, VOFFS(8192)); lds += 4096;
            const rsrc_t rk = mk_rsrc(p.wuk, 1024 * 8192);
            const unsigned sk = (64 * i16 + 32 * pp) * 8192 + h * 512;
            dma_tile<NT_W>(rk, sk, lds, VOFFS(8192)); lds += 4096;
            n = 3;
            __builtin_amdgcn_s_barrier();
#pragma unroll 1
            for (int cb = 1; cb < 4; ++cb) { dma_tile<NT_W>(rk, sk + cb * 128, lds, VOFFS(8192)); STEP(); }
        }
        STAMP(1);
        {
#pragma unroll 1
            for (int t = 0; t < 16; ++t) {
                const unsigned rb = pp ? ((i16 - t) & 15) : i16, cb = pp ? i16 : ((i16 + t) & 15);
                const unsigned so = (512 * h + 32 * rb) * 2048 + cb * 128;
                const rsrc_t rc = mk_rsrc(p.ckv, (pp == 1 && t == 0) ? 0 : 8192 * 2048);
                if (t == 11) wait_ge_sleep(L, C_RELQ, 4);
                dma_tile<NT_C>(rc, so, lds, VOFFS(2048)); STEP();
            }
        }
        STAMP(2);
        {
            const rsrc_t r1 = mk_rsrc(p.w1, b < 196 ? 448 * 3584 : 0);
            dma_tile<NT_W>(r1, b < 196 ? (64 * (b % 7) + 32 * pp) * 3584 + (b / 7) * 128 : 0, lds, VOFFS(3584)); STEP();
            const rsrc_t rv = mk_rsrc(p.wuv, 1024 * 8192);
            const unsigned sv = (256 * mq + 32 * pp) * 8192 + (h * 256 + dq * 64) * 2;
            wait_ge_sleep(L, C_RELA0, 2);
#pragma unroll 1
            for (int k = 0; k < 4; ++k) { dma_tile<NT_W>(rv, sv + k * (64 * 8192), lds, VOFFS(8192)); STEP(); }
            STAMP(3);
            const int bb = b - 60;
            const rsrc_t r2 = mk_rsrc(p.w2, bb >= 0 ? 1792 * 896 : 0);
            wait_ge_sleep(L, C_RELA1, 2);
            dma_tile<NT_W>(r2, bb >= 0 ? (64 * (bb / 7) + 32 * pp) * 896 + (bb % 7) * 128 : 0, lds, VOFFS(896)); STEP();
            const rsrc_t ro = mk_rsrc(p.wo, 4096 * 896);
            const unsigned sw = (h * 256 + dq * 64 + 32 * pp) * 896;
#pragma unroll 1
            for (int k = 0; k < 7; ++k) {
                int cb = k + 2 * mq; if (cb >= 7) cb -= 7;
                if (k == 3) wait_ge_sleep(L, C_RELA2, 2);
                dma_tile<NT_O>(ro, sw + cb * 128, lds, VOFFS(896)); STEP();
            }
        }
        STAMP(4);
        if (WIN > 12) { asm volatile("s_waitcnt vmcnt(12)" ::: "memory"); *prog = 32; }
        asm volatile("s_waitcnt vmcnt(8)" ::: "memory"); *prog = 33;
        asm volatile("s_waitcnt vmcnt(4)" ::: "memory"); *prog = 34;
        asm volatile("s_waitcnt vmcnt(0)" ::: "memory"); *prog = 35;
        STAMP_OUT();
        return;
    }

    const rsrc_t ws = mk_rsrc(p.ws, WS_END);
    const v2u ep = ld8(ws, WS_EPOCH);
    const v4u S4 = {SENT, SENT, SENT, SENT};
    const v2u S2 = {SENT, SENT};
    const int swr = ((l15 >> 1) & 1) | (((l15 >> 3) & 1) << 1);
    const int row_a0 = l15 * 128 + ((q4 ^ (swr << 1)) << 4), row_a1 = row_a0 ^ 64;
    const int swt = ((l15 >> 3) & 1) | ((q4 & 1) << 1);
    const int tr_b = (8 * q4 + (l15 >> 2)) * 128 + (((l15 >> 1) & 1) << 4) + 8 * (l15 & 1);
#define TR_A(nt) (tr_b + ((((nt) ^ swt)) << 5))
#define SLOT(pp, n) (L_RING + (pp) * (NSLOT * 4096) + ((n) % NSLOT) * 4096)

    if (wv < 6) {
        const int t = wv - 2;
        const v4u xv = __builtin_amdgcn_raw_buffer_load_b128(mk_rsrc(p.x, 896), lane * 16, 0, 0);
        __builtin_amdgcn_s_barrier();
        layernorm(xv, L, lane);
        const unsigned tagA = ep.x + 1u, tagB = ep.y + (tagA == 0u ? 1u : 0u);
        const int tra = TR_A(t);
        const int row0 = mq < 2 ? 128 * mq : 64 + 96 * mq;
        v4u bx[4];
#pragma unroll
        for (int s = 0; s < 4; ++s) bx[s] = lds16(L, L_XN + (row0 + 32 * s + 8 * q4) * 2);
        STAMP(1);
        wait_ge(L, C_PROG0, 2); wait_ge(L, C_PROG1, 2);
        STAMP(2);
        f32x4 acc = {0.f, 0.f, 0.f, 0.f};
#pragma unroll
        for (int s = 0; s < 4; ++s) acc = mfma(lds_tr(L, SLOT(s & 1, s >> 1) + tra), bx[s], acc);
        if (l15 < 2) st16(ws, WS_H0 + ((h * 4 + dq) * 4 + mq) * 512 + (16 * t + 4 * q4 + 2 * l15) * 8,
                          v4u{f2u(l15 ? acc.z : acc.x), tagA, f2u(l15 ? acc.w : acc.y), tagB});
        STAMP(3);
        __builtin_amdgcn_s_sleep(DLY0);
        v4u g0[2];
#pragma unroll 1
        for (unsigned s = 0; s < SPIN_G; ++s) { PASSES(0, s);
            CBAR();
#pragma unroll
            for (int k = 0; k < 2; ++k) g0[k] = ld16(ws, WS_H0 + (h * 4 + t) * 2048 + k * 1024 + lane * 16);
            const bool bad_ = any_nz((g0[0].y ^ tagA) | (g0[0].w ^ tagB) | (g0[1].y ^ tagA) | (g0[1].w ^ tagB));
            LSTAMP(s & 63);
            if (!bad_) break;
            __builtin_amdgcn_s_sleep(RETRY);
        }
        STAMP(4);
        {
            const float qa = u2f(g0[0].x) + u2f(g0[1].x), qb = u2f(g0[0].z) + u2f(g0[1].z);
            auto ra = __builtin_amdgcn_permlane32_swap(f2u(qa), f2u(qa), false, false);
            auto rb = __builtin_amdgcn_permlane32_swap(f2u(qb), f2u(qb), false, false);
            *(LAS unsigned*)(L + L_Q + t * 128 + (lane & 31) * 4) = pk_bf16(u2f(ra[0]) + u2f(ra[1]), u2f(rb[0]) + u2f(rb[1]));
            ctl_add(L, C_QX, lane);
        }
        v4u ak[8];
        wait_ge(L, C_PROG0 + (t >> 1), 6);
        {
            const int base = L_RING + (t >> 1) * (NSLOT * 4096) + (t & 1) * 2048;
#pragma unroll
            for (int cb = 0; cb < 4; ++cb) { ak[2 * cb] = lds16(L, base + (2 + cb) * 4096 + row_a0); ak[2 * cb + 1] = lds16(L, base + (2 + cb) * 4096 + row_a1); }
        }
        ctl_add(L, C_RELQ, lane);
        STAMP(5);
        wait_ge(L, C_FILL, 9);
        wait_ge(L, C_QX, 4);
        STAMP(6);
        {
            acc = f32x4{0.f, 0.f, 0.f, 0.f};
#pragma unroll
            for (int s = 0; s < 8; ++s) acc = mfma(ak[s], lds16(L, L_Q + (32 * s + 8 * q4) * 2), acc);
            const float sc = 0.0625f * 1.4426950408889634f;
            const int z = 4 * (t & 1) + q4;
            if (l15 == 0) st16(ws, WS_H1 + h * 4096 + (2 * i16 + (t >> 1)) * 128 + ((z >> 1) + 4 * (z & 1)) * 16,
                               v4u{pk_bf16(acc.x * sc, acc.y * sc), tagA, pk_bf16(acc.z * sc, acc.w * sc), tagB});
            ctl_add(L, C_S2, lane);
        }
        STAMP(7);
        wait_ge_sleep(L, C_PROG0, 27); wait_ge_sleep(L, C_PROG1, 27);
#pragma unroll
        for (int s = 0; s < 8; ++s) ak[s] = lds_tr(L, SLOT(s & 1, 23 + (s >> 1)) + tra);
        STAMP(8);
        wait_ge_sleep(L, C_S4, 2);
        STAMP(9);
        __builtin_amdgcn_s_sleep(DLY3);
        v4u g2[8]; v2u st;
        const int o_h2 = WS_H2 + (h * 16 + l15) * 2048 + (256 * mq + 8 * q4) * 2;
#pragma unroll 1
        for (unsigned s = 0; s < SPIN_G; ++s) { PASSES(1, s);
            CBAR();
#pragma unroll
            for (int k = 0; k < 8; ++k) g2[k] = ld16(ws, o_h2 + k * 64);
            st = ld8(ws, WS_H2S + ((l15 * 16 + 4 * mq) * 16 + h) * 8);
            unsigned m = umax(st.x, st.y);
#pragma unroll
            for (int k = 0; k < 8; ++k) m = umax(m, umax4(g2[k]));
            if (!any_sent(m)) break;
            __builtin_amdgcn_s_sleep(RETRY);
        }
        STAMP(10);
        acc = f32x4{0.f, 0.f, 0.f, 0.f};
#pragma unroll
        for (int s = 0; s < 8; ++s) acc = mfma(ak[s], g2[s], acc);
        {
            const float rg = u2f(st.x), lg = u2f(st.y);
            const float R = row_max(rg);
            const float sc = __builtin_amdgcn_exp2f(rg - R);
            const float wgt = sc / row_sum(sc * lg);
            const float c0 = row_sum(acc.x * wgt), c1 = row_sum(acc.y * wgt), c2 = row_sum(acc.z * wgt), c3 = row_sum(acc.w * wgt);
            if (l15 == 0) *(LAS v2u*)(L + L_CTX + (16 * t + 4 * q4) * 2) = v2u{pk_bf16(c0, c1), pk_bf16(c2, c3)};
        }
        ctl_add(L, C_CNT5, lane);
        STAMP_OUT();
        return;
    }

    if (wv == 6) {
        const int bb = b - 60;
        const int fs1 = b / 7, ms1 = b % 7, fs2 = bb / 7, mt2 = bb % 7;
        const int o_hid = WS_HID + (fs1 * 7 + ms1) * 256 + (16 * l15 + 4 * q4) * 4;
        const int o_gp = WS_GP + fs2 * 1792 + (64 * mt2 + 16 * l15 + 4 * q4) * 4;
        if (l15 < 4) { if (b < 196) st16(ws, o_hid, S4); if (bb >= 0) st16(ws, o_gp, S4); }
        if (lane < C_NUM) ((LAS unsigned*)(L + L_CTL))[lane] = 0u;
        LWAIT();
        __builtin_amdgcn_s_barrier();
        const int u = b - 144;
        unsigned b1v = 0; v2u b2v = {0u, 0u}, xv = {0u, 0u};
        if (bb >= 0) b1v = ((const GAS u16*)p.b1)[64 * fs2 + lane];
        if (u >= 0) { b2v = ((const GAS v2u*)p.b2)[u]; xv = ((const GAS v2u*)p.x)[u]; }
        DRAIN();
        ctl_add(L, C_FILL, lane);
        STAMP(1);
        if (b < 196) {
            wait_ge_sleep(L, C_RELQ, 4); wait_ge_sleep(L, C_PROG0, 23); wait_ge_sleep(L, C_PROG1, 23);
            const v4u bx0 = lds16(L, L_XN + (64 * ms1 + 8 * q4) * 2), bx1 = lds16(L, L_XN + (64 * ms1 + 32 + 8 * q4) * 2);
            f32x4 a[4];
#pragma unroll
            for (int nt = 0; nt < 4; ++nt) {
                a[nt] = mfma(lds_tr(L, SLOT(0, 22) + TR_A(nt)), bx0, f32x4{0.f, 0.f, 0.f, 0.f});
                a[nt] = mfma(lds_tr(L, SLOT(1, 22) + TR_A(nt)), bx1, a[nt]);
            }
            const f32x4 o = l15 == 0 ? a[0] : l15 == 1 ? a[1] : l15 == 2 ? a[2] : a[3];
            if (l15 < 4) st16(ws, o_hid, san4(f4u(o)));
        }
        STAMP(3);
        wait_ge_sleep(L, C_CNT3, 8);
        if (bb >= 0) {
            unsigned hp[7];
            __builtin_amdgcn_s_sleep(8);
#pragma unroll 1
            for (unsigned s = 0; s < SPIN_G; ++s) { PASSES(2, s);
                CBAR();
                unsigned m = 0;
#pragma unroll
                for (int k = 0; k < 7; ++k) hp[k] = ld4(ws, WS_HID + (fs2 * 7 + k) * 256 + lane * 4);
#pragma unroll
                for (int k = 0; k < 7; ++k) m = umax(m, hp[k]);
                if (!any_sent(m)) break;
                __builtin_amdgcn_s_sleep(4);
            }
            float hs = 0.f;
#pragma unroll
            for (int k = 0; k < 7; ++k) hs += u2f(hp[k]);
            hs = rbf(rbf(hs) + bflo(b1v));
            hs = hs > 0.f ? hs : 0.f;
            *(LAS u16*)(L + L_HID + lane * 2) = (u16)pk_bf16(hs * hs, 0.f);
            CBAR();
            STAMP(4);
            wait_ge_sleep(L, C_PROG0, 28); wait_ge_sleep(L, C_PROG1, 28);
            const v4u bh0 = lds16(L, L_HID + 8 * q4 * 2), bh1 = lds16(L, L_HID + (32 + 8 * q4) * 2);
            f32x4 a[4];
#pragma unroll
            for (int nt = 0; nt < 4; ++nt) {
                a[nt] = mfma(lds_tr(L, SLOT(0, 27) + TR_A(nt)), bh0, f32x4{0.f, 0.f, 0.f, 0.f});
                a[nt] = mfma(lds_tr(L, SLOT(1, 27) + TR_A(nt)), bh1, a[nt]);
            }
            const f32x4 o = l15 == 0 ? a[0] : l15 == 1 ? a[1] : l15 == 2 ? a[2] : a[3];
            if (l15 < 4) st16(ws, o_gp, san4(f4u(o)));
        }
        STAMP(5);
        if (u >= 0) {
            v4u gv;
            const int lg = lane < 28 ? lane : 27;
            __builtin_amdgcn_s_sleep(8);
#pragma unroll 1
            for (unsigned s = 0; s < SPIN_G; ++s) { PASSES(3, s);
                CBAR();
                gv = ld16(ws, WS_GP + lg * 1792 + u * 16);
                if (!any_sent(umax4(gv))) break;
                __builtin_amdgcn_s_sleep(4);
            }
            f32x4 gf = u4f(gv);
            if (lane >= 28) gf = f32x4{0.f, 0.f, 0.f, 0.f};
            float g[4] = {wave_sum(gf.x), wave_sum(gf.y), wave_sum(gf.z), wave_sum(gf.w)};
            const float bb2[4] = {bflo(b2v.x), bfhi(b2v.x), bflo(b2v.y), bfhi(b2v.y)};
#pragma unroll
            for (int k = 0; k < 4; ++k) { const float a = rbf(rbf(g[k]) + bb2[k]); g[k] = rbf(a / (1.f + __expf(-a))); }
            STAMP(6);
            wait_ge(L, C_S6, 7);
            STAMP(7);
            __builtin_amdgcn_s_sleep(DLY4);
            v4u sv[4];
#pragma unroll 1
            for (unsigned s = 0; s < SPIN_G; ++s) { PASSES(4, s);
                CBAR();
#pragma unroll
                for (int k = 0; k < 4; ++k) sv[k] = ld16(ws, WS_H4 + (lane + 64 * k) * 1792 + u * 16);
                if (!any_sent(umax(umax(umax4(sv[0]), umax4(sv[1])), umax(umax4(sv[2]), umax4(sv[3]))))) break;
                __builtin_amdgcn_s_sleep(RETRY);
            }
            STAMP(8);
            const f32x4 sf = (u4f(sv[0]) + u4f(sv[1])) + (u4f(sv[2]) + u4f(sv[3]));
            const float s[4] = {wave_sum(sf.x), wave_sum(sf.y), wave_sum(sf.z), wave_sum(sf.w)};
            const float xx[4] = {bflo(xv.x), bfhi(xv.x), bflo(xv.y), bfhi(xv.y)};
            float o[4];
#pragma unroll
            for (int k = 0; k < 4; ++k) o[k] = xx[k] + rbf(rbf(s[k]) * g[k]);
            if (lane == 0) ((GAS v2u*)p.out)[u] = v2u{pk_bf16(o[0], o[1]), pk_bf16(o[2], o[3])};
            if (u == 0 && lane == 0) { const unsigned e0 = ep.x + 1u; st8(ws, WS_EPOCH, v2u{e0, ep.y + (e0 == 0u ? 1u : 0u)}); }
        }
        STAMP_OUT();
        return;
    }

    {
        const int w = wv - 7, kq = w >> 1, ch = w & 1;
        int cbm = w + 2 * mq; if (cbm >= 7) cbm -= 7;
        const int o_p = WS_P + (h * 16 + i16) * 1024 + lane * 16;
        const int o_ps = WS_PS + ((h * 16 + i16) * 16 + l15) * 8;
        const int o_h2 = WS_H2 + (l15 * 16 + h) * 2048 + (64 * i16 + 32 * ch + 8 * q4) * 2;
        const int o_h2s = WS_H2S + ((h * 16 + i16) * 16 + l15) * 8;
        const int o_h4 = WS_H4 + b * 1792 + (64 * cbm + 16 * l15 + 4 * q4) * 4;
        if (w == 0) { st16(ws, o_p, S4); if (lane < 16) { st8(ws, o_ps, S2); st8(ws, o_h2s, S2); } }
        if (w < 2) st16(ws, o_h2, S4);
        if (w < 7 && l15 < 4) st16(ws, o_h4, S4);
        __builtin_amdgcn_s_barrier();
        DRAIN();
        ctl_add(L, C_FILL, lane);
        v4u ar[8], ac[8];
        const int rbase = L_RING + ((6 + 2 * w) % NSLOT) * 4096, rbase1 = L_RING + ((7 + 2 * w) % NSLOT) * 4096;
        const int tr0 = TR_A(2 * ch), tr1 = TR_A(2 * ch + 1);
#define PULL_R() do { wait_ge(L, C_PROG0, 8 + 2 * w); \
            ar[0] = lds16(L, rbase + row_a0); ar[1] = lds16(L, rbase + row_a1); ar[2] = lds16(L, rbase1 + row_a0); ar[3] = lds16(L, rbase1 + row_a1); \
            ar[4] = lds16(L, rbase + 2048 + row_a0); ar[5] = lds16(L, rbase + 2048 + row_a1); ar[6] = lds16(L, rbase1 + 2048 + row_a0); ar[7] = lds16(L, rbase1 + 2048 + row_a1); } while (0)
#define PULL_C() do { wait_ge(L, C_PROG1, 10 + 4 * kq); \
            _Pragma("unroll") for (int u = 0; u < 4; ++u) { int cbase = L_RING + NSLOT * 4096 + ((6 + 4 * kq + u) % NSLOT) * 4096; if (u == 0 && kq == 0) cbase = L_RING + 6 * 4096; \
                ac[2 * u] = lds_tr(L, cbase + tr0); ac[2 * u + 1] = lds_tr(L, cbase + tr1); } \
            LWAIT(); ctl_add(L, C_RELA0 + kq, lane); } while (0)
        STAMP(1);
        if (w < 4) { PULL_R(); PULL_C(); }
        STAMP(2);
        wait_ge_sleep(L, C_S2, 4);
        __builtin_amdgcn_s_sleep(DLY1);
        v4u bq[4];
        {
            const unsigned tagA = ep.x + 1u, tagB = ep.y + (tagA == 0u ? 1u : 0u);
            const int c0 = (i16 + 2 * w) & 15, c1 = (i16 + 2 * w + 1) & 15;
            const int o0 = WS_H1 + l15 * 4096 + c0 * 256 + q4 * 16, o1 = WS_H1 + l15 * 4096 + c1 * 256 + q4 * 16;
            v4u g[8];
#pragma unroll 1
            for (unsigned s = 0; s < SPIN_G; ++s) { PASSES(5, s);
                CBAR();
                unsigned m = 0;
#pragma unroll
                for (int k = 0; k < 4; ++k) { g[k] = ld16(ws, o0 + 64 * k); g[4 + k] = ld16(ws, o1 + 64 * k); }
#pragma unroll
                for (int k = 0; k < 8; ++k) m |= (g[k].y ^ tagA) | (g[k].w ^ tagB);
                if (!any_nz(m)) break;
                __builtin_amdgcn_s_sleep(RETRY);
            }
#pragma unroll
            for (int k = 0; k < 4; ++k) bq[k] = v4u{g[2 * k].x, g[2 * k].z, g[2 * k + 1].x, g[2 * k + 1].z};
        }
        STAMP(3);
        if (w >= 4) PULL_R();
        STAMP(4);
        {
            f32x4 s0 = {0.f, 0.f, 0.f, 0.f}, s1 = s0;
#pragma unroll
            for (int k = 0; k < 4; ++k) { s0 = mfma(ar[k], bq[k], s0); s1 = mfma(ar[4 + k], bq[k], s1); }
            *(LAS f32x4*)(L + L_PART + w * 2048 + lane * 16) = s0;
            *(LAS f32x4*)(L + L_PART + w * 2048 + 1024 + lane * 16) = s1;
            ctl_add(L, C_CNT3, lane);
        }
        STAMP(5);
        if (w >= 4) PULL_C();
        const int red_a = (q4 >> 1) * 1024 + ((q4 & 1) * 32 + l15) * 16;
        if (w == 0) {
            wait_ge(L, C_CNT3, 8);
            STAMP(6);
            f32x4 a0 = {0.f, 0.f, 0.f, 0.f}, a1 = a0;
#pragma unroll
            for (int k = 0; k < 8; ++k) { a0 += *(LAS f32x4*)(L + L_PART + k * 2048 + red_a); a1 += *(LAS f32x4*)(L + L_PART + k * 2048 + red_a + 256); }
            const float mx = xrow_max(fmaxf(fmaxf(fmaxf(a0.x, a0.y), fmaxf(a0.z, a0.w)), fmaxf(fmaxf(a1.x, a1.y), fmaxf(a1.z, a1.w))));
            const float r = __builtin_ceilf(mx);
            const float p0 = __builtin_amdgcn_exp2f(a0.x - r), p1 = __builtin_amdgcn_exp2f(a0.y - r), p2 = __builtin_amdgcn_exp2f(a0.z - r), p3 = __builtin_amdgcn_exp2f(a0.w - r);
            const float p4 = __builtin_amdgcn_exp2f(a1.x - r), p5 = __builtin_amdgcn_exp2f(a1.y - r), p6 = __builtin_amdgcn_exp2f(a1.z - r), p7 = __builtin_amdgcn_exp2f(a1.w - r);
            st16(ws, o_p, san4(v4u{pk_bf16(p0, p1), pk_bf16(p2, p3), pk_bf16(p4, p5), pk_bf16(p6, p7)}));
            const float l = xrow_sum(((p0 + p1) + (p2 + p3)) + ((p4 + p5) + (p6 + p7)));
            if (lane < 16) st8(ws, o_ps, v2u{san(f2u(r)), san(f2u(l))});
            ctl_add(L, C_PPUB, lane);
        }
        STAMP(7);
        wait_ge(L, C_PPUB, 1);
        __builtin_amdgcn_s_sleep(DLY2);
        v4u bp[4]; v2u ps[4];
        {
            int op[4], os[4];
#pragma unroll
            for (int u = 0; u < 4; ++u) { const int iu = (i16 - 4 * kq - u) & 15; op[u] = WS_P + (h * 16 + iu) * 1024 + lane * 16; os[u] = WS_PS + ((h * 16 + iu) * 16 + l15) * 8; }
#pragma unroll 1
            for (unsigned s = 0; s < SPIN_G; ++s) { PASSES(6, s);
                CBAR();
                unsigned m = 0;
#pragma unroll
                for (int u = 0; u < 4; ++u) { bp[u] = ld16(ws, op[u]); ps[u] = ld8(ws, os[u]); }
#pragma unroll
                for (int u = 0; u < 4; ++u) m = umax(m, umax(umax4(bp[u]), umax(ps[u].x, ps[u].y)));
                if (!any_sent(m)) break;
                __builtin_amdgcn_s_sleep(RETRY);
            }
        }
        STAMP(8);
        {
            const float rw = fmaxf(fmaxf(u2f(ps[0].x), u2f(ps[1].x)), fmaxf(u2f(ps[2].x), u2f(ps[3].x)));
            f32x4 c0 = {0.f, 0.f, 0.f, 0.f}, c1 = c0;
            float lw = 0.f;
#pragma unroll
            for (int u = 0; u < 4; ++u) {
                const float dl = rw - u2f(ps[u].x);
                const unsigned dd = (unsigned)fminf(dl, 255.f) * 0x00800080u;
                const u16x2 d2 = __builtin_bit_cast(u16x2, dd);
                const unsigned x0 = bp[u].x, x1 = bp[u].y, x2 = bp[u].z, x3 = bp[u].w;
                const v4u bs = {__builtin_bit_cast(unsigned, __builtin_elementwise_sub_sat(__builtin_bit_cast(u16x2, x0), d2)),
                                __builtin_bit_cast(unsigned, __builtin_elementwise_sub_sat(__builtin_bit_cast(u16x2, x1), d2)),
                                __builtin_bit_cast(unsigned, __builtin_elementwise_sub_sat(__builtin_bit_cast(u16x2, x2), d2)),
                                __builtin_bit_cast(unsigned, __builtin_elementwise_sub_sat(__builtin_bit_cast(u16x2, x3), d2))};
                c0 = mfma(ac[2 * u], bs, c0); c1 = mfma(ac[2 * u + 1], bs, c1);
                lw += __builtin_amdgcn_exp2f(-dl) * u2f(ps[u].y);
            }
            *(LAS f32x4*)(L + L_PART + w * 2048 + lane * 16) = c0;
            *(LAS f32x4*)(L + L_PART + w * 2048 + 1024 + lane * 16) = c1;
            if (lane < 16) *(LAS f32x2*)(L + L_ST4 + ((ch * 4 + kq) * 16 + l15) * 8) = f32x2{rw, lw};
            ctl_add(L, C_CNT4A + ch, lane);
        }
        STAMP(9);
        if (w < 2) {
            wait_ge(L, C_CNT4A + ch, 4);
            STAMP(10);
            f32x4 v0[4], v1[4]; f32x2 sx[4];
#pragma unroll
            for (int k = 0; k < 4; ++k) {
                v0[k] = *(LAS f32x4*)(L + L_PART + (2 * k + ch) * 2048 + red_a); v1[k] = *(LAS f32x4*)(L + L_PART + (2 * k + ch) * 2048 + red_a + 256);
                sx[k] = *(LAS f32x2*)(L + L_ST4 + ((ch * 4 + k) * 16 + l15) * 8);
            }
            const float R = fmaxf(fmaxf(sx[0].x, sx[1].x), fmaxf(sx[2].x, sx[3].x));
            f32x4 a0 = {0.f, 0.f, 0.f, 0.f}, a1 = a0; float lg = 0.f;
#pragma unroll
            for (int k = 0; k < 4; ++k) { const float sc = __builtin_amdgcn_exp2f(sx[k].x - R); a0 += v0[k] * sc; a1 += v1[k] * sc; lg += sx[k].y * sc; }
            st16(ws, o_h2, san4(v4u{pk_bf16(a0.x, a0.y), pk_bf16(a0.z, a0.w), pk_bf16(a1.x, a1.y), pk_bf16(a1.z, a1.w)}));
            if (w == 0 && lane < 16) st8(ws, o_h2s, v2u{san(f2u(R)), san(f2u(lg))});
            ctl_add(L, C_S4, lane);
        }
        STAMP(11);
        if (w < 7) {
            wait_ge_sleep(L, C_PROG0, 29 + w); wait_ge_sleep(L, C_PROG1, 29 + w);
            const int ob0 = L_RING + ((28 + w) % NSLOT) * 4096, ob1 = ob0 + NSLOT * 4096;
#pragma unroll
            for (int nt = 0; nt < 4; ++nt) { ar[nt] = lds_tr(L, ob0 + TR_A(nt)); ar[4 + nt] = lds_tr(L, ob1 + TR_A(nt)); }
            STAMP(12);
            wait_ge(L, C_CNT5, 4);
            STAMP(13);
            const v4u bc0 = lds16(L, L_CTX + 8 * q4 * 2), bc1 = lds16(L, L_CTX + (32 + 8 * q4) * 2);
            f32x4 a[4];
#pragma unroll
            for (int nt = 0; nt < 4; ++nt) { a[nt] = mfma(ar[nt], bc0, f32x4{0.f, 0.f, 0.f, 0.f}); a[nt] = mfma(ar[4 + nt], bc1, a[nt]); }
            const f32x4 o = l15 == 0 ? a[0] : l15 == 1 ? a[1] : l15 == 2 ? a[2] : a[3];
            if (l15 < 4) st16(ws, o_h4, san4(f4u(o)));
            ctl_add(L, C_S6, lane);
        }
        STAMP_OUT();
    }
}

extern "C" void kernel_launch(void* const* d_in, const int* in_sizes, int n_in,
                              void* d_out, int out_size, void* d_ws, size_t ws_size,
                              hipStream_t stream) {
    Params p;
    p.x = d_in[0]; p.ckv = d_in[1]; p.wq = d_in[2]; p.wuk = d_in[3]; p.wuv = d_in[4]; p.wo = d_in[5];
    p.w1 = d_in[7]; p.b1 = d_in[8]; p.w2 = d_in[9]; p.b2 = d_in[10]; p.out = d_out; p.ws = d_ws;
    for (int i = 0; i < n_in; ++i) fprintf(stderr, "in[%d]=%p (%d) ", i, d_in[i], in_sizes[i]);
    fprintf(stderr, "out=%p ws=%p ws_size=%zu stream=%p\n", d_out, d_ws, ws_size, (void*)stream);
    { hipPointerAttribute_t a; if (hipPointerGetAttributes(&a, d_ws) == hipSuccess) fprintf(stderr, "ws attr: type %d dev %d managed %d flags %u\n", (int)a.type, a.device, (int)a.isManaged, a.allocationFlags);
      void* base; size_t sz; if (hipMemGetAddressRange((hipDeviceptr_t*)&base, &sz, (hipDeviceptr_t)d_in[1]) == hipSuccess) fprintf(stderr, "ckv range base %p size %zu\n", base, sz);
      if (hipMemGetAddressRange((hipDeviceptr_t*)&base, &sz, (hipDeviceptr_t)d_ws) == hipSuccess) fprintf(stderr, "ws range base %p size %zu\n", base, sz); }
    (void)hipFuncSetAttribute((const void*)fused_decode, hipFuncAttributeMaxDynamicSharedMemorySize, L_END);
    fused_decode<<<256, NWAVE * 64, L_END, stream>>>(p);
}
```

```cpp
#include <hip/hip_runtime.h>
#include <stdint.h>

typedef unsigned v4u __attribute__((ext_vector_type(4)));
typedef unsigned v2u __attribute__((ext_vector_type(2)));
typedef float f32x4 __attribute__((ext_vector_type(4)));
typedef float f32x2 __attribute__((ext_vector_type(2)));
typedef __bf16 bf16x8 __attribute__((ext_vector_type(8)));
typedef __bf16 bf16x2 __attribute__((ext_vector_type(2)));
typedef short s16x4 __attribute__((ext_vector_type(4)));
typedef unsigned short u16x2 __attribute__((ext_vector_type(2)));
typedef unsigned short u16;
#define LAS __attribute__((address_space(3)))
#define GAS __attribute__((address_space(1)))
typedef __amdgpu_buffer_rsrc_t rsrc_t;

#define NT_W 1
#define NT_C 1
#define NT_O 1
#define DLY0 8
#define DLY1 8
#define DLY2 8
#define DLY3 8
#define DLY4 8
#define RETRY 2
#define SPINSLEEP 0
#define WIN 12

constexpr int WS_EPOCH = 0;
constexpr int WS_H0 = 4096;
constexpr int WS_H1 = WS_H0 + 131072;
constexpr int WS_P = WS_H1 + 65536;
constexpr int WS_PS = WS_P + 262144;
constexpr int WS_H2 = WS_PS + 32768;
constexpr int WS_H2S = WS_H2 + 524288;
constexpr int WS_H4 = WS_H2S + 32768;
constexpr int WS_HID = WS_H4 + 256 * 1792;
constexpr int WS_GP = WS_HID + 28 * 7 * 256;
constexpr int WS_END = WS_GP + 28 * 1792;
constexpr int WS_DIAG = 4 << 20;

constexpr int NSLOT = 17;
constexpr int L_RING = 0;
constexpr int L_PART = 2 * NSLOT * 4096;
constexpr int L_XN = L_PART + 16384;
constexpr int L_Q = L_XN + 1024;
constexpr int L_CTX = L_Q + 512;
constexpr int L_HID = L_CTX + 128;
constexpr int L_ST4 = L_HID + 128;
constexpr int L_ST5 = L_ST4 + 1024;
constexpr int L_X = L_ST5 + 128;
constexpr int L_E = L_X + 1024;
constexpr int L_CTL = L_E + 16;
constexpr int L_END = L_CTL + 128;
enum { C_PROG0 = 0, C_PROG1, C_FILL, C_QX, C_S2, C_RELQ, C_RELA0, C_RELA1, C_RELA2, C_RELA3, C_CNT3, C_RED3, C_PPUB, C_CNT4A, C_CNT4B, C_S4, C_CNT5, C_S6, C_X, C_G2, C_NUM };

constexpr int NWAVE = 15;
constexpr unsigned SENT = 0xFFFFFFFFu;
constexpr int AUX_SC1 = 16;
constexpr unsigned SPIN_G = 1u << 15, SPIN_L = 1u << 19;

struct Params {
    const void* x; const void* ckv; const void* wq; const void* wuk; const void* wuv; const void* wo;
    const void* w1; const void* b1; const void* w2; const void* b2; void* out; void* ws;
};

__device__ __forceinline__ float u2f(unsigned u) { return __builtin_bit_cast(float, u); }
__device__ __forceinline__ unsigned f2u(float f) { return __builtin_bit_cast(unsigned, f); }
__device__ __forceinline__ unsigned pk_bf16(float a, float b) { f32x2 v = {a, b}; bf16x2 r = __builtin_convertvector(v, bf16x2); return __builtin_bit_cast(unsigned, r); }
__device__ __forceinline__ float rbf(float a) { return u2f(pk_bf16(a, a) << 16); }
__device__ __forceinline__ float bflo(unsigned u) { return u2f(u << 16); }
__device__ __forceinline__ float bfhi(unsigned u) { return u2f(u & 0xFFFF0000u); }
__device__ __forceinline__ unsigned san(unsigned u) { return u < 0xFFFFFFFEu ? u : 0xFFFFFFFEu; }
__device__ __forceinline__ v4u san4(v4u v) { return v4u{san(v.x), san(v.y), san(v.z), san(v.w)}; }
__device__ __forceinline__ v4u f4u(f32x4 v) { return __builtin_bit_cast(v4u, v); }
__device__ __forceinline__ f32x4 u4f(v4u v) { return __builtin_bit_cast(f32x4, v); }
__device__ __forceinline__ unsigned umax(unsigned a, unsigned b) { return a > b ? a : b; }
__device__ __forceinline__ unsigned umax4(v4u v) { return umax(umax(v.x, v.y), umax(v.z, v.w)); }
__device__ __forceinline__ f32x4 mfma(v4u a, v4u b, f32x4 c) {
    return __builtin_amdgcn_mfma_f32_16x16x32_bf16(__builtin_bit_cast(bf16x8, a), __builtin_bit_cast(bf16x8, b), c, 0, 0, 0);
}
template <int N> __device__ __forceinline__ float ror(float v) { return u2f((unsigned)__builtin_amdgcn_update_dpp(0, (int)f2u(v), 0x120 + N, 0xf, 0xf, false)); }
__device__ __forceinline__ float row_sum(float v) { v += ror<8>(v); v += ror<4>(v); v += ror<2>(v); v += ror<1>(v); return v; }
__device__ __forceinline__ float row_max(float v) { v = fmaxf(v, ror<8>(v)); v = fmaxf(v, ror<4>(v)); v = fmaxf(v, ror<2>(v)); v = fmaxf(v, ror<1>(v)); return v; }
__device__ __forceinline__ float xrow_sum(float v) {
    auto a = __builtin_amdgcn_permlane32_swap(f2u(v), f2u(v), false, false); v = u2f(a[0]) + u2f(a[1]);
    auto b = __builtin_amdgcn_permlane16_swap(f2u(v), f2u(v), false, false); return u2f(b[0]) + u2f(b[1]);
}
__device__ __forceinline__ float xrow_max(float v) {
    auto a = __builtin_amdgcn_permlane32_swap(f2u(v), f2u(v), false, false); v = fmaxf(u2f(a[0]), u2f(a[1]));
    auto b = __builtin_amdgcn_permlane16_swap(f2u(v), f2u(v), false, false); return fmaxf(u2f(b[0]), u2f(b[1]));
}
__device__ __forceinline__ float wave_sum(float v) { return xrow_sum(row_sum(v)); }

__device__ __forceinline__ rsrc_t mk_rsrc(const void* p, int bytes) { return __builtin_amdgcn_make_buffer_rsrc((void*)p, 0, bytes, 0x00020000); }
__device__ __forceinline__ v4u ld16(rsrc_t r, int off) { return __builtin_amdgcn_raw_buffer_load_b128(r, off, 0, AUX_SC1); }
__device__ __forceinline__ v2u ld8(rsrc_t r, int off) { return __builtin_amdgcn_raw_buffer_load_b64(r, off, 0, AUX_SC1); }
__device__ __forceinline__ unsigned ld4(rsrc_t r, int off) { return __builtin_amdgcn_raw_buffer_load_b32(r, off, 0, AUX_SC1); }
__device__ __forceinline__ void st16(rsrc_t r, int off, v4u v) { __builtin_amdgcn_raw_buffer_store_b128(v, r, off, 0, AUX_SC1); }
__device__ __forceinline__ void st8(rsrc_t r, int off, v2u v) { __builtin_amdgcn_raw_buffer_store_b64(v, r, off, 0, AUX_SC1); }
#define CBAR() asm volatile("" ::: "memory")
#define DRAIN() asm volatile("s_waitcnt vmcnt(0)" ::: "memory")
#define LWAIT() asm volatile("s_waitcnt lgkmcnt(0)" ::: "memory")

typedef LAS unsigned char* lds_t;
__device__ __forceinline__ v4u lds16(lds_t L, int off) { return *(LAS v4u*)(L + off); }
__device__ __forceinline__ v4u lds_tr(lds_t L, int off) {
    s16x4 a = __builtin_amdgcn_ds_read_tr16_b64_v4i16((LAS s16x4*)(L + off));
    s16x4 b = __builtin_amdgcn_ds_read_tr16_b64_v4i16((LAS s16x4*)(L + off + 512));
    v2u a2 = __builtin_bit_cast(v2u, a), b2 = __builtin_bit_cast(v2u, b);
    return v4u{a2.x, a2.y, b2.x, b2.y};
}
__device__ __forceinline__ void wait_ge(lds_t L, int word, unsigned want) {
    volatile LAS unsigned* p = (volatile LAS unsigned*)(L + L_CTL) + word;
#pragma unroll 1
    for (unsigned s = 0; s < SPIN_L; ++s) { if ((unsigned)__builtin_amdgcn_readfirstlane(*p) >= want) break; if (SPINSLEEP) __builtin_amdgcn_s_sleep(SPINSLEEP); }
    CBAR();
}
__device__ __forceinline__ void wait_ge_sleep(lds_t L, int word, unsigned want) {
    volatile LAS unsigned* p = (volatile LAS unsigned*)(L + L_CTL) + word;
#pragma unroll 1
    for (unsigned s = 0; s < SPIN_L; ++s) { if ((unsigned)__builtin_amdgcn_readfirstlane(*p) >= want) break; __builtin_amdgcn_s_sleep(1); }
    CBAR();
}
__device__ __forceinline__ void ctl_add(lds_t L, int word, int lane) {
    CBAR();
    if (lane == 0) __hip_atomic_fetch_add((LAS unsigned*)(L + L_CTL) + word, 1u, __ATOMIC_RELAXED, __HIP_MEMORY_SCOPE_WORKGROUP);
    CBAR();
}
__device__ __forceinline__ bool any_sent(unsigned m) { return __builtin_amdgcn_ballot_w64(m == SENT) != 0ull; }
__device__ __forceinline__ bool any_nz(unsigned m) { return __builtin_amdgcn_ballot_w64(m != 0u) != 0ull; }

#define STAMP(k)
#define STAMP_DECL
#define LSTAMP(n)
#define PASSES(k, s)
#define STAMP_OUT()

template <int NT>
__device__ __forceinline__ void dma_tile(rsrc_t r, unsigned soff, unsigned lds, unsigned v0, unsigned v1, unsigned v2, unsigned v3) {
    if (NT)
        asm volatile("s_mov_b32 m0, %6\n\ts_nop 3\n\t"
                     "buffer_load_dwordx4 %0, %4, %5 offen nt lds\n\t"
                     "buffer_load_dwordx4 %1, %4, %5 offen offset:1024 nt lds\n\t"
                     "buffer_load_dwordx4 %2, %4, %5 offen offset:2048 nt lds\n\t"
                     "buffer_load_dwordx4 %3, %4, %5 offen offset:3072 nt lds"
                     :: "v"(v0), "v"(v1), "v"(v2), "v"(v3), "s"(r), "s"(soff), "s"(lds) : "memory");
    else
        asm volatile("s_mov_b32 m0, %6\n\ts_nop 3\n\t"
                     "buffer_load_dwordx4 %0, %4, %5 offen lds\n\t"
                     "buffer_load_dwordx4 %1, %4, %5 offen offset:1024 lds\n\t"
                     "buffer_load_dwordx4 %2, %4, %5 offen offset:2048 lds\n\t"
                     "buffer_load_dwordx4 %3, %4, %5 offen offset:3072 lds"
                     :: "v"(v0), "v"(v1), "v"(v2), "v"(v3), "s"(r), "s"(soff), "s"(lds) : "memory");
}

__device__ __forceinline__ void layernorm(const v4u xv, lds_t L, int lane) {
    float e[8] = {bflo(xv.x), bfhi(xv.x), bflo(xv.y), bfhi(xv.y), bflo(xv.z), bfhi(xv.z), bflo(xv.w), bfhi(xv.w)};
    float s = ((e[0] + e[1]) + (e[2] + e[3])) + ((e[4] + e[5]) + (e[6] + e[7]));
    const float mean = wave_sum(s) * (1.0f / 448.0f);
    float v = 0.f;
#pragma unroll
    for (int k = 0; k < 8; ++k) { const float d = e[k] - mean; v += d * d; }
    v = lane < 56 ? v : 0.f;
    const float var = wave_sum(v) * (1.0f / 448.0f);
    const float mb = rbf(mean);
    const float sd = rbf(sqrtf(rbf(rbf(var) + 1e-5f)));
    float o[8];
#pragma unroll
    for (int k = 0; k < 8; ++k) o[k] = rbf(e[k] - mb) / sd;
    v4u w = {pk_bf16(o[0], o[1]), pk_bf16(o[2], o[3]), pk_bf16(o[4], o[5]), pk_bf16(o[6], o[7])};
    if (lane >= 56) w = v4u{0u, 0u, 0u, 0u};
    *(LAS v4u*)(L + L_XN + lane * 16) = w;
    CBAR();
}

__global__ void __launch_bounds__(NWAVE * 64, 4) fused_decode(Params p) {
    extern __shared__ __attribute__((aligned(16))) unsigned char smem[];
    lds_t L = (lds_t)smem;
    const int lane = threadIdx.x & 63, wv = __builtin_amdgcn_readfirstlane(threadIdx.x >> 6);
    const int b = blockIdx.x, j = b >> 3;
    const int h = 2 * (b & 7) + (j >> 4);
    const int i16 = j & 15, dq = (j >> 2) & 3, mq = j & 3;
    const int l15 = lane & 15, q4 = lane >> 4;
    STAMP_DECL

    if (wv < 2) {
        const int pp = wv;
        const unsigned r = lane >> 3, pc = lane & 7, s0 = (r >> 1) & 1;
        const unsigned ce = (pc ^ (s0 << 1)) << 4, co = (pc ^ ((s0 | 2) << 1)) << 4;
#define VOFFS(st) (r * (st) + ce), ((r + 8) * (st) + co - 1024), ((r + 16) * (st) + ce - 2048), ((r + 24) * (st) + co - 3072)
        unsigned lds = L_RING + pp * (NSLOT * 4096);
        const unsigned lds_lo = lds, lds_hi = lds + NSLOT * 4096;
        unsigned n = 0;
        volatile LAS unsigned* prog = (volatile LAS unsigned*)(L + L_CTL) + C_PROG0 + pp;
#define STEP() do { lds += 4096; if (lds == lds_hi) lds = lds_lo; ++n; asm volatile("s_waitcnt vmcnt(%0)" :: "i"(WIN) : "memory"); \
                    if (n > WIN / 4) *prog = n - WIN / 4; CBAR(); LSTAMP(n); } while (0)
        {
            if (pp == 0) {
                asm volatile("s_mov_b32 m0, %3\n\ts_nop 3\n\tbuffer_load_dwordx4 %0, %1, 0 offen lds\n\t"
                             "s_mov_b32 m0, %4\n\ts_mov_b64 exec, 1\n\tbuffer_load_dwordx4 %0, %2, 0 offen lds\n\ts_mov_b64 exec, -1"
                             :: "v"(lane * 16), "s"(mk_rsrc(p.x, 896)), "s"(mk_rsrc(p.ws, 16)), "s"(L_X), "s"(L_E) : "memory");
            }
            const rsrc_t rq = mk_rsrc(p.wq, 448 * 8192);
            const unsigned row0 = mq < 2 ? 128 * mq : 64 + 96 * mq;
            const unsigned so = (row0 + 32 * pp) * 8192 + (h * 256 + dq * 64) * 2;
            dma_tile<NT_W>(rq, so, lds, VOFFS(8192)); lds += 4096;
            dma_tile<NT_W>(mk_rsrc(p.wq, (pp == 1 && mq >= 2) ? 0 : 448 * 8192), so + 64 * 8192, lds, VOFFS(8192)); lds += 4096;
            const rsrc_t rk = mk_rsrc(p.wuk, 1024 * 8192);
            const unsigned sk = (64 * i16 + 32 * pp) * 8192 + h * 512;
            dma_tile<NT_W>(rk, sk, lds, VOFFS(8192)); lds += 4096;
            n = 3;
            __builtin_amdgcn_s_barrier();
            if (pp == 0) { asm volatile("s_waitcnt vmcnt(12)" ::: "memory"); ((volatile LAS unsigned*)(L + L_CTL))[C_X] = 1u; CBAR(); }
#pragma unroll 1
            for (int cb = 1; cb < 4; ++cb) { dma_tile<NT_W>(rk, sk + cb * 128, lds, VOFFS(8192)); STEP(); }
        }
        STAMP(1);
        {
#pragma unroll 1
            for (int t = 0; t < 16; ++t) {
                const unsigned rb = pp ? ((i16 - t) & 15) : i16, cb = pp ? i16 : ((i16 + t) & 15);
                const unsigned so = (512 * h + 32 * rb) * 2048 + cb * 128;
                const rsrc_t rc = mk_rsrc(p.ckv, (pp == 1 && t == 0) ? 0 : 8192 * 2048);
                if (t == 11) wait_ge_sleep(L, C_RELQ, 4);
                dma_tile<NT_C>(rc, so, lds, VOFFS(2048)); STEP();
            }
        }
        STAMP(2);
        {
            const rsrc_t r1 = mk_rsrc(p.w1, b < 196 ? 448 * 3584 : 0);
            dma_tile<NT_W>(r1, b < 196 ? (64 * (b % 7) + 32 * pp) * 3584 + (b / 7) * 128 : 0, lds, VOFFS(3584)); STEP();
            const rsrc_t rv = mk_rsrc(p.wuv, 1024 * 8192);
            const unsigned sv = (256 * mq + 32 * pp) * 8192 + (h * 256 + dq * 64) * 2;
            wait_ge_sleep(L, C_RELA0, 2);
#pragma unroll 1
            for (int k = 0; k < 4; ++k) { dma_tile<NT_W>(rv, sv + k * (64 * 8192), lds, VOFFS(8192)); STEP(); }
            STAMP(3);
            const int bb = b - 60;
            const rsrc_t r2 = mk_rsrc(p.w2, bb >= 0 ? 1792 * 896 : 0);
            wait_ge_sleep(L, C_RELA1, 2);
            dma_tile<NT_W>(r2, bb >= 0 ? (64 * (bb / 7) + 32 * pp) * 896 + (bb % 7) * 128 : 0, lds, VOFFS(896)); STEP();
            const rsrc_t ro = mk_rsrc(p.wo, 4096 * 896);
            const unsigned sw = (h * 256 + dq * 64 + 32 * pp) * 896;
#pragma unroll 1
            for (int k = 0; k < 7; ++k) {
                int cb = k + 2 * mq; if (cb >= 7) cb -= 7;
                if (k == 3) wait_ge_sleep(L, C_RELA2, 2);
                dma_tile<NT_O>(ro, sw + cb * 128, lds, VOFFS(896)); STEP();
            }
        }
        STAMP(4);
        if (WIN > 12) { asm volatile("s_waitcnt vmcnt(12)" ::: "memory"); *prog = 32; }
        asm volatile("s_waitcnt vmcnt(8)" ::: "memory"); *prog = 33;
        asm volatile("s_waitcnt vmcnt(4)" ::: "memory"); *prog = 34;
        asm volatile("s_waitcnt vmcnt(0)" ::: "memory"); *prog = 35;
        STAMP_OUT();
        return;
    }

    const rsrc_t ws = mk_rsrc(p.ws, WS_END);
    const v4u S4 = {SENT, SENT, SENT, SENT};
    const v2u S2 = {SENT, SENT};
    const int swr = ((l15 >> 1) & 1) | (((l15 >> 3) & 1) << 1);
    const int row_a0 = l15 * 128 + ((q4 ^ (swr << 1)) << 4), row_a1 = row_a0 ^ 64;
    const int swt = ((l15 >> 3) & 1) | ((q4 & 1) << 1);
    const int tr_b = (8 * q4 + (l15 >> 2)) * 128 + (((l15 >> 1) & 1) << 4) + 8 * (l15 & 1);
#define TR_A(nt) (tr_b + ((((nt) ^ swt)) << 5))
#define SLOT(pp, n) (L_RING + (pp) * (NSLOT * 4096) + ((n) % NSLOT) * 4096)

    if (wv < 6) {
        const int t = wv - 2;
        const int o_h0 = WS_H0 + ((h * 4 + dq) * 4 + mq) * 512 + (16 * t + 4 * q4 + 2 * l15) * 8;
        const int o_h1 = WS_H1 + (i16 * 16 + h) * 256 + (4 * t + q4) * 16;
        if (l15 < 2) st16(ws, o_h0, v4u{0u, 0u, 0u, 0u});
        if (l15 == 0) st16(ws, o_h1, v4u{0u, 0u, 0u, 0u});
        __builtin_amdgcn_s_barrier();
        wait_ge(L, C_X, 1);
        const v2u ep = *(LAS v2u*)(L + L_E);
        layernorm(lds16(L, L_X + lane * 16), L, lane);
        const unsigned tagA = ep.x + 1u, tagB = ep.y | 1u;
        const int tra = TR_A(t);
        const int row0 = mq < 2 ? 128 * mq : 64 + 96 * mq;
        v4u bx[4];
#pragma unroll
        for (int s = 0; s < 4; ++s) bx[s] = lds16(L, L_XN + (row0 + 32 * s + 8 * q4) * 2);
        STAMP(1);
        wait_ge(L, C_PROG0, 2); wait_ge(L, C_PROG1, 2);
        STAMP(2);
        f32x4 acc = {0.f, 0.f, 0.f, 0.f};
#pragma unroll
        for (int s = 0; s < 4; ++s) acc = mfma(lds_tr(L, SLOT(s & 1, s >> 1) + tra), bx[s], acc);
        if (l15 < 2) st16(ws, o_h0, v4u{f2u(l15 ? acc.z : acc.x), tagA, f2u(l15 ? acc.w : acc.y), tagB});
        STAMP(3);
        __builtin_amdgcn_s_sleep(DLY0);
        CBAR();
        v4u g0[2];
        const int o_g0 = WS_H0 + (h * 4 + t) * 2048 + lane * 16;
        g0[0] = ld16(ws, o_g0); g0[1] = ld16(ws, o_g0 + 1024);
        v4u ak[8];
        wait_ge(L, C_PROG0 + (t >> 1), 6);
        {
            const int base = L_RING + (t >> 1) * (NSLOT * 4096) + (t & 1) * 2048;
#pragma unroll
            for (int cb = 0; cb < 4; ++cb) { ak[2 * cb] = lds16(L, base + (2 + cb) * 4096 + row_a0); ak[2 * cb + 1] = lds16(L, base + (2 + cb) * 4096 + row_a1); }
        }
        ctl_add(L, C_RELQ, lane);
        STAMP(4);
#pragma unroll 1
        for (unsigned s = 0; s < SPIN_G; ++s) { PASSES(0, s);
            if (!any_nz((g0[0].y ^ tagA) | (g0[0].w ^ tagB) | (g0[1].y ^ tagA) | (g0[1].w ^ tagB))) break;
            __builtin_amdgcn_s_sleep(RETRY);
            CBAR();
            g0[0] = ld16(ws, o_g0); g0[1] = ld16(ws, o_g0 + 1024);
        }
        {
            const float qa = u2f(g0[0].x) + u2f(g0[1].x), qb = u2f(g0[0].z) + u2f(g0[1].z);
            auto ra = __builtin_amdgcn_permlane32_swap(f2u(qa), f2u(qa), false, false);
            auto rb = __builtin_amdgcn_permlane32_swap(f2u(qb), f2u(qb), false, false);
            *(LAS unsigned*)(L + L_Q + t * 128 + (lane & 31) * 4) = pk_bf16(u2f(ra[0]) + u2f(ra[1]), u2f(rb[0]) + u2f(rb[1]));
            ctl_add(L, C_QX, lane);
        }
        STAMP(5);
        wait_ge(L, C_FILL, 9);
        wait_ge(L, C_QX, 4);
        STAMP(6);
        {
            acc = f32x4{0.f, 0.f, 0.f, 0.f};
#pragma unroll
            for (int s = 0; s < 8; ++s) acc = mfma(ak[s], lds16(L, L_Q + (32 * s + 8 * q4) * 2), acc);
            const float sc = 0.0625f * 1.4426950408889634f;
            if (l15 == 0) st16(ws, o_h1, v4u{pk_bf16(acc.x * sc, acc.y * sc), tagA, pk_bf16(acc.z * sc, acc.w * sc), tagB});
            ctl_add(L, C_S2, lane);
        }
        STAMP(7);
        wait_ge_sleep(L, C_PROG0, 27); wait_ge_sleep(L, C_PROG1, 27);
#pragma unroll
        for (int s = 0; s < 8; ++s) ak[s] = lds_tr(L, SLOT(s & 1, 23 + (s >> 1)) + tra);
        STAMP(8);
        wait_ge_sleep(L, C_S4, 2);
        STAMP(9);
        __builtin_amdgcn_s_sleep(DLY3);
        {
            v4u g2[2]; v2u st;
            const int o_h2 = WS_H2 + (h * 4 + mq) * 8192 + t * 2048 + lane * 16;
            const int o_st = WS_H2S + (((4 * t + (lane & 3)) * 16 + 4 * mq) * 16 + h) * 8;
#pragma unroll 1
            for (unsigned s = 0; s < SPIN_G; ++s) { PASSES(1, s);
                CBAR();
                g2[0] = ld16(ws, o_h2); g2[1] = ld16(ws, o_h2 + 1024);
                st = v2u{0u, 0u};
                if (lane < 4) st = ld8(ws, o_st);
                if (!any_sent(umax(umax(st.x, st.y), umax(umax4(g2[0]), umax4(g2[1]))))) break;
                __builtin_amdgcn_s_sleep(RETRY);
            }
            const int wa = L_PART + ((lane >> 3) & 3) * 2048 + (4 * t + (lane >> 5)) * 128;
            *(LAS v4u*)(L + wa + (((lane & 7) ^ ((t >> 1) << 2)) << 4)) = g2[0];
            *(LAS v4u*)(L + wa + 256 + (((lane & 7) ^ (((t >> 1) << 2) | 2)) << 4)) = g2[1];
            if (lane < 4) *(LAS v2u*)(L + L_ST5 + (4 * t + lane) * 8) = st;
            ctl_add(L, C_G2, lane);
        }
        STAMP(10);
        wait_ge(L, C_G2, 4);
        STAMP(11);
        acc = f32x4{0.f, 0.f, 0.f, 0.f};
        const v2u st = *(LAS v2u*)(L + L_ST5 + l15 * 8);
#pragma unroll
        for (int s = 0; s < 8; ++s) acc = mfma(ak[s], lds16(L, L_PART + (s >> 1) * 2048 + ((s & 1) ? row_a1 : row_a0)), acc);
        {
            const float rg = u2f(st.x), lg = u2f(st.y);
            const float R = row_max(rg);
            const float sc = __builtin_amdgcn_exp2f(rg - R);
            const float wgt = sc / row_sum(sc * lg);
            const float c0 = row_sum(acc.x * wgt), c1 = row_sum(acc.y * wgt), c2 = row_sum(acc.z * wgt), c3 = row_sum(acc.w * wgt);
            if (l15 == 0) *(LAS v2u*)(L + L_CTX + (16 * t + 4 * q4) * 2) = v2u{pk_bf16(c0, c1), pk_bf16(c2, c3)};
        }
        ctl_add(L, C_CNT5, lane);
        STAMP_OUT();
        return;
    }

    if (wv == 6) {
        const int bb = b - 60;
        const int fs1 = b / 7, ms1 = b % 7, fs2 = bb / 7, mt2 = bb % 7;
        const int o_hid = WS_HID + (fs1 * 7 + ms1) * 256 + (16 * l15 + 4 * q4) * 4;
        const int o_gp = WS_GP + fs2 * 1792 + (64 * mt2 + 16 * l15 + 4 * q4) * 4;
        if (l15 < 4) { if (b < 196) st16(ws, o_hid, S4); if (bb >= 0) st16(ws, o_gp, S4); }
        if (lane < C_NUM) ((LAS unsigned*)(L + L_CTL))[lane] = 0u;
        LWAIT();
        __builtin_amdgcn_s_barrier();
        const int u = b - 144;
        DRAIN();
        ctl_add(L, C_FILL, lane);
        STAMP(1);
        wait_ge_sleep(L, C_RELQ, 4);
        unsigned b1v = 0; v2u b2v = {0u, 0u}, xv = {0u, 0u};
        if (bb >= 0) b1v = __builtin_nontemporal_load((const GAS u16*)p.b1 + 64 * fs2 + lane);
        if (u >= 0) { b2v = __builtin_nontemporal_load((const GAS v2u*)p.b2 + u); xv = *(LAS v2u*)(L + L_X + u * 8); }
        const v2u ep = *(LAS v2u*)(L + L_E);
        if (b < 196) {
            wait_ge_sleep(L, C_PROG0, 23); wait_ge_sleep(L, C_PROG1, 23);
            const v4u bx0 = lds16(L, L_XN + (64 * ms1 + 8 * q4) * 2), bx1 = lds16(L, L_XN + (64 * ms1 + 32 + 8 * q4) * 2);
            f32x4 a[4];
#pragma unroll
            for (int nt = 0; nt < 4; ++nt) {
                a[nt] = mfma(lds_tr(L, SLOT(0, 22) + TR_A(nt)), bx0, f32x4{0.f, 0.f, 0.f, 0.f});
                a[nt] = mfma(lds_tr(L, SLOT(1, 22) + TR_A(nt)), bx1, a[nt]);
            }
            const f32x4 o = l15 == 0 ? a[0] : l15 == 1 ? a[1] : l15 == 2 ? a[2] : a[3];
            if (l15 < 4) st16(ws, o_hid, san4(f4u(o)));
        }
        STAMP(3);
        wait_ge_sleep(L, C_CNT3, 8);
        if (bb >= 0) {
            unsigned hp[7];
            __builtin_amdgcn_s_sleep(8);
#pragma unroll 1
            for (unsigned s = 0; s < SPIN_G; ++s) { PASSES(2, s);
                CBAR();
                unsigned m = 0;
#pragma unroll
                for (int k = 0; k < 7; ++k) hp[k] = ld4(ws, WS_HID + (fs2 * 7 + k) * 256 + lane * 4);
#pragma unroll
                for (int k = 0; k < 7; ++k) m = umax(m, hp[k]);
                if (!any_sent(m)) break;
                __builtin_amdgcn_s_sleep(4);
            }
            float hs = 0.f;
#pragma unroll
            for (int k = 0; k < 7; ++k) hs += u2f(hp[k]);
            hs = rbf(rbf(hs) + bflo(b1v));
            hs = hs > 0.f ? hs : 0.f;
            *(LAS u16*)(L + L_HID + lane * 2) = (u16)pk_bf16(hs * hs, 0.f);
            CBAR();
            STAMP(4);
            wait_ge_sleep(L, C_PROG0, 28); wait_ge_sleep(L, C_PROG1, 28);
            const v4u bh0 = lds16(L, L_HID + 8 * q4 * 2), bh1 = lds16(L, L_HID + (32 + 8 * q4) * 2);
            f32x4 a[4];
#pragma unroll
            for (int nt = 0; nt < 4; ++nt) {
                a[nt] = mfma(lds_tr(L, SLOT(0, 27) + TR_A(nt)), bh0, f32x4{0.f, 0.f, 0.f, 0.f});
                a[nt] = mfma(lds_tr(L, SLOT(1, 27) + TR_A(nt)), bh1, a[nt]);
            }
            const f32x4 o = l15 == 0 ? a[0] : l15 == 1 ? a[1] : l15 == 2 ? a[2] : a[3];
            if (l15 < 4) st16(ws, o_gp, san4(f4u(o)));
        }
        STAMP(5);
        if (u >= 0) {
            v4u gv;
            const int lg = lane < 28 ? lane : 27;
            __builtin_amdgcn_s_sleep(8);
#pragma unroll 1
            for (unsigned s = 0; s < SPIN_G; ++s) { PASSES(3, s);
                CBAR();
                gv = ld16(ws, WS_GP + lg * 1792 + u * 16);
                if (!any_sent(umax4(gv))) break;
                __builtin_amdgcn_s_sleep(4);
            }
            f32x4 gf = u4f(gv);
            if (lane >= 28) gf = f32x4{0.f, 0.f, 0.f, 0.f};
            float g[4] = {wave_sum(gf.x), wave_sum(gf.y), wave_sum(gf.z), wave_sum(gf.w)};
            const float bb2[4] = {bflo(b2v.x), bfhi(b2v.x), bflo(b2v.y), bfhi(b2v.y)};
#pragma unroll
            for (int k = 0; k < 4; ++k) { const float a = rbf(rbf(g[k]) + bb2[k]); g[k] = rbf(a / (1.f + __expf(-a))); }
            STAMP(6);
            wait_ge(L, C_S6, 7);
            STAMP(7);
            __builtin_amdgcn_s_sleep(DLY4);
            v4u sv[4];
#pragma unroll 1
            for (unsigned s = 0; s < SPIN_G; ++s) { PASSES(4, s);
                CBAR();
#pragma unroll
                for (int k = 0; k < 4; ++k) sv[k] = ld16(ws, WS_H4 + (lane + 64 * k) * 1792 + u * 16);
                if (!any_sent(umax(umax(umax4(sv[0]), umax4(sv[1])), umax(umax4(sv[2]), umax4(sv[3]))))) break;
                __builtin_amdgcn_s_sleep(RETRY);
            }
            STAMP(8);
            const f32x4 sf = (u4f(sv[0]) + u4f(sv[1])) + (u4f(sv[2]) + u4f(sv[3]));
            const float s[4] = {wave_sum(sf.x), wave_sum(sf.y), wave_sum(sf.z), wave_sum(sf.w)};
            const float xx[4] = {bflo(xv.x), bfhi(xv.x), bflo(xv.y), bfhi(xv.y)};
            float o[4];
#pragma unroll
            for (int k = 0; k < 4; ++k) o[k] = xx[k] + rbf(rbf(s[k]) * g[k]);
            if (lane == 0) ((GAS v2u*)p.out)[u] = v2u{pk_bf16(o[0], o[1]), pk_bf16(o[2], o[3])};
            if (u == 0 && lane == 0) st8(ws, WS_EPOCH, v2u{ep.x + 1u, ep.y});
        }
        STAMP_OUT();
        return;
    }

    {
        const int w = wv - 7, kq = w >> 1, ch = w & 1;
        int cbm = w + 2 * mq; if (cbm >= 7) cbm -= 7;
        const int o_p = WS_P + (h * 16 + i16) * 1024 + lane * 16;
        const int o_ps = WS_PS + ((h * 16 + i16) * 16 + l15) * 8;
        const int o_h2 = WS_H2 + (l15 * 4 + (i16 >> 2)) * 8192 + h * 512 + (64 * (i16 & 3) + 32 * ch + 8 * q4) * 2;
        const int o_h2s = WS_H2S + ((h * 16 + i16) * 16 + l15) * 8;
        const int o_h4 = WS_H4 + b * 1792 + (64 * cbm + 16 * l15 + 4 * q4) * 4;
        if (w == 0) { st16(ws, o_p, S4); if (lane < 16) { st8(ws, o_ps, S2); st8(ws, o_h2s, S2); } }
        if (w < 2) st16(ws, o_h2, S4);
        if (w < 7 && l15 < 4) st16(ws, o_h4, S4);
        __builtin_amdgcn_s_barrier();
        DRAIN();
        ctl_add(L, C_FILL, lane);
        v4u ar[8], ac[8];
        const int rbase = L_RING + ((6 + 2 * w) % NSLOT) * 4096, rbase1 = L_RING + ((7 + 2 * w) % NSLOT) * 4096;
        const int tr0 = TR_A(2 * ch), tr1 = TR_A(2 * ch + 1);
#define PULL_R() do { wait_ge(L, C_PROG0, 8 + 2 * w); \
            ar[0] = lds16(L, rbase + row_a0); ar[1] = lds16(L, rbase + row_a1); ar[2] = lds16(L, rbase1 + row_a0); ar[3] = lds16(L, rbase1 + row_a1); \
            ar[4] = lds16(L, rbase + 2048 + row_a0); ar[5] = lds16(L, rbase + 2048 + row_a1); ar[6] = lds16(L, rbase1 + 2048 + row_a0); ar[7] = lds16(L, rbase1 + 2048 + row_a1); } while (0)
#define PULL_C() do { wait_ge(L, C_PROG1, 10 + 4 * kq); \
            _Pragma("unroll") for (int u = 0; u < 4; ++u) { int cbase = L_RING + NSLOT * 4096 + ((6 + 4 * kq + u) % NSLOT) * 4096; if (u == 0 && kq == 0) cbase = L_RING + 6 * 4096; \
                ac[2 * u] = lds_tr(L, cbase + tr0); ac[2 * u + 1] = lds_tr(L, cbase + tr1); } \
            LWAIT(); ctl_add(L, C_RELA0 + kq, lane); } while (0)
        STAMP(1);
        if (w < 4) { PULL_R(); PULL_C(); }
        STAMP(2);
        wait_ge_sleep(L, C_S2, 4);
        __builtin_amdgcn_s_sleep(DLY1);
        v4u bq[4];
        {
            const v2u ep = *(LAS v2u*)(L + L_E);
            const unsigned tagA = ep.x + 1u, tagB = ep.y | 1u;
            const int o0 = WS_H1 + ((i16 + 2 * w) & 15) * 4096 + lane * 16, o1 = WS_H1 + ((i16 + 2 * w + 1) & 15) * 4096 + lane * 16;
            v4u g[8];
#pragma unroll 1
            for (unsigned s = 0; s < SPIN_G; ++s) { PASSES(5, s);
                CBAR();
                unsigned m = 0;
#pragma unroll
                for (int k = 0; k < 4; ++k) { g[k] = ld16(ws, o0 + 1024 * k); g[4 + k] = ld16(ws, o1 + 1024 * k); }
#pragma unroll
                for (int k = 0; k < 8; ++k) m |= (g[k].y ^ tagA) | (g[k].w ^ tagB);
                if (!any_nz(m)) break;
                __builtin_amdgcn_s_sleep(RETRY);
            }
            const int pw = L_PART + w * 2048;
            const int wa0 = pw + q4 * 128 + (((l15 >> 1) ^ (q4 & 2)) << 4) + (l15 & 1) * 8, wa1 = wa0 ^ 64;
#pragma unroll
            for (int hb = 0; hb < 2; ++hb) {
#pragma unroll
                for (int k = 0; k < 4; ++k) *(LAS v2u*)(L + (k < 2 ? wa0 : wa1) + k * 512) = v2u{g[4 * hb + k].x, g[4 * hb + k].z};
                CBAR();
                bq[2 * hb] = lds16(L, pw + row_a0); bq[2 * hb + 1] = lds16(L, pw + row_a1);
                CBAR();
            }
        }
        STAMP(3);
        if (w >= 4) PULL_R();
        STAMP(4);
        {
            f32x4 s0 = {0.f, 0.f, 0.f, 0.f}, s1 = s0;
#pragma unroll
            for (int k = 0; k < 4; ++k) { s0 = mfma(ar[k], bq[k], s0); s1 = mfma(ar[4 + k], bq[k], s1); }
            *(LAS f32x4*)(L + L_PART + w * 2048 + lane * 16) = s0;
            *(LAS f32x4*)(L + L_PART + w * 2048 + 1024 + lane * 16) = s1;
            ctl_add(L, C_CNT3, lane);
        }
        STAMP(5);
        if (w >= 4) PULL_C();
        const int red_a = (q4 >> 1) * 1024 + ((q4 & 1) * 32 + l15) * 16;
        if (w == 0) {
            wait_ge(L, C_CNT3, 8);
            STAMP(6);
            f32x4 a0 = {0.f, 0.f, 0.f, 0.f}, a1 = a0;
#pragma unroll
            for (int k = 0; k < 8; ++k) { a0 += *(LAS f32x4*)(L + L_PART + k * 2048 + red_a); a1 += *(LAS f32x4*)(L + L_PART + k * 2048 + red_a + 256); }
            const float mx = xrow_max(fmaxf(fmaxf(fmaxf(a0.x, a0.y), fmaxf(a0.z, a0.w)), fmaxf(fmaxf(a1.x, a1.y), fmaxf(a1.z, a1.w))));
            const float r = __builtin_ceilf(mx);
            const float p0 = __builtin_amdgcn_exp2f(a0.x - r), p1 = __builtin_amdgcn_exp2f(a0.y - r), p2 = __builtin_amdgcn_exp2f(a0.z - r), p3 = __builtin_amdgcn_exp2f(a0.w - r);
            const float p4 = __builtin_amdgcn_exp2f(a1.x - r), p5 = __builtin_amdgcn_exp2f(a1.y - r), p6 = __builtin_amdgcn_exp2f(a1.z - r), p7 = __builtin_amdgcn_exp2f(a1.w - r);
            st16(ws, o_p, san4(v4u{pk_bf16(p0, p1), pk_bf16(p2, p3), pk_bf16(p4, p5), pk_bf16(p6, p7)}));
            const float l = xrow_sum(((p0 + p1) + (p2 + p3)) + ((p4 + p5) + (p6 + p7)));
            if (lane < 16) st8(ws, o_ps, v2u{san(f2u(r)), san(f2u(l))});
            ctl_add(L, C_PPUB, lane);
        }
        STAMP(7);
        wait_ge(L, C_PPUB, 1);
        __builtin_amdgcn_s_sleep(DLY2);
        v4u bp[4]; v2u ps[4];
        {
            int op[4], os[4];
#pragma unroll
            for (int u = 0; u < 4; ++u) { const int iu = (i16 - 4 * kq - u) & 15; op[u] = WS_P + (h * 16 + iu) * 1024 + lane * 16; os[u] = WS_PS + ((h * 16 + iu) * 16 + l15) * 8; }
#pragma unroll 1
            for (unsigned s = 0; s < SPIN_G; ++s) { PASSES(6, s);
                CBAR();
                unsigned m = 0;
#pragma unroll
                for (int u = 0; u < 4; ++u) { bp[u] = ld16(ws, op[u]); ps[u] = ld8(ws, os[u]); }
#pragma unroll
                for (int u = 0; u < 4; ++u) m = umax(m, umax(umax4(bp[u]), umax(ps[u].x, ps[u].y)));
                if (!any_sent(m)) break;
                __builtin_amdgcn_s_sleep(RETRY);
            }
        }
        STAMP(8);
        {
            const float rw = fmaxf(fmaxf(u2f(ps[0].x), u2f(ps[1].x)), fmaxf(u2f(ps[2].x), u2f(ps[3].x)));
            f32x4 c0 = {0.f, 0.f, 0.f, 0.f}, c1 = c0;
            float lw = 0.f;
#pragma unroll
            for (int u = 0; u < 4; ++u) {
                const float dl = rw - u2f(ps[u].x);
                const unsigned dd = (unsigned)fminf(dl, 255.f) * 0x00800080u;
                const u16x2 d2 = __builtin_bit_cast(u16x2, dd);
                const unsigned x0 = bp[u].x, x1 = bp[u].y, x2 = bp[u].z, x3 = bp[u].w;
                const v4u bs = {__builtin_bit_cast(unsigned, __builtin_elementwise_sub_sat(__builtin_bit_cast(u16x2, x0), d2)),
                                __builtin_bit_cast(unsigned, __builtin_elementwise_sub_sat(__builtin_bit_cast(u16x2, x1), d2)),
                                __builtin_bit_cast(unsigned, __builtin_elementwise_sub_sat(__builtin_bit_cast(u16x2, x2), d2)),
                                __builtin_bit_cast(unsigned, __builtin_elementwise_sub_sat(__builtin_bit_cast(u16x2, x3), d2))};
                c0 = mfma(ac[2 * u], bs, c0); c1 = mfma(ac[2 * u + 1], bs, c1);
                lw += __builtin_amdgcn_exp2f(-dl) * u2f(ps[u].y);
            }
            *(LAS f32x4*)(L + L_PART + w * 2048 + lane * 16) = c0;
            *(LAS f32x4*)(L + L_PART + w * 2048 + 1024 + lane * 16) = c1;
            if (lane < 16) *(LAS f32x2*)(L + L_ST4 + ((ch * 4 + kq) * 16 + l15) * 8) = f32x2{rw, lw};
            ctl_add(L, C_CNT4A + ch, lane);
        }
        STAMP(9);
        if (w < 2) {
            wait_ge(L, C_CNT4A + ch, 4);
            STAMP(10);
            f32x4 v0[4], v1[4]; f32x2 sx[4];
#pragma unroll
            for (int k = 0; k < 4; ++k) {
                v0[k] = *(LAS f32x4*)(L + L_PART + (2 * k + ch) * 2048 + red_a); v1[k] = *(LAS f32x4*)(L + L_PART + (2 * k + ch) * 2048 + red_a + 256);
                sx[k] = *(LAS f32x2*)(L + L_ST4 + ((ch * 4 + k) * 16 + l15) * 8);
            }
            const float R = fmaxf(fmaxf(sx[0].x, sx[1].x), fmaxf(sx[2].x, sx[3].x));
            f32x4 a0 = {0.f, 0.f, 0.f, 0.f}, a1 = a0; float lg = 0.f;
#pragma unroll
            for (int k = 0; k < 4; ++k) { const float sc = __builtin_amdgcn_exp2f(sx[k].x - R); a0 += v0[k] * sc; a1 += v1[k] * sc; lg += sx[k].y * sc; }
            st16(ws, o_h2, san4(v4u{pk_bf16(a0.x, a0.y), pk_bf16(a0.z, a0.w), pk_bf16(a1.x, a1.y), pk_bf16(a1.z, a1.w)}));
            if (w == 0 && lane < 16) st8(ws, o_h2s, v2u{san(f2u(R)), san(f2u(lg))});
            ctl_add(L, C_S4, lane);
        }
        STAMP(11);
        if (w < 7) {
            wait_ge_sleep(L, C_PROG0, 29 + w); wait_ge_sleep(L, C_PROG1, 29 + w);
            const int ob0 = L_RING + ((28 + w) % NSLOT) * 4096, ob1 = ob0 + NSLOT * 4096;
#pragma unroll
            for (int nt = 0; nt < 4; ++nt) { ar[nt] = lds_tr(L, ob0 + TR_A(nt)); ar[4 + nt] = lds_tr(L, ob1 + TR_A(nt)); }
            STAMP(12);
            wait_ge(L, C_CNT5, 4);
            STAMP(13);
            const v4u bc0 = lds16(L, L_CTX + 8 * q4 * 2), bc1 = lds16(L, L_CTX + (32 + 8 * q4) * 2);
            f32x4 a[4];
#pragma unroll
            for (int nt = 0; nt < 4; ++nt) { a[nt] = mfma(ar[nt], bc0, f32x4{0.f, 0.f, 0.f, 0.f}); a[nt] = mfma(ar[4 + nt], bc1, a[nt]); }
            const f32x4 o = l15 == 0 ? a[0] : l15 == 1 ? a[1] : l15 == 2 ? a[2] : a[3];
            if (l15 < 4) st16(ws, o_h4, san4(f4u(o)));
            ctl_add(L, C_S6, lane);
        }
        STAMP_OUT();
    }
}

extern "C" void kernel_launch(void* const* d_in, const int* in_sizes, int n_in,
                              void* d_out, int out_size, void* d_ws, size_t ws_size,
                              hipStream_t stream) {
    Params p;
    p.x = d_in[0]; p.ckv = d_in[1]; p.wq = d_in[2]; p.wuk = d_in[3]; p.wuv = d_in[4]; p.wo = d_in[5];
    p.w1 = d_in[7]; p.b1 = d_in[8]; p.w2 = d_in[9]; p.b2 = d_in[10]; p.out = d_out; p.ws = d_ws;
    (void)hipFuncSetAttribute((const void*)fused_decode, hipFuncAttributeMaxDynamicSharedMemorySize, L_END);
    fused_decode<<<256, NWAVE * 64, L_END, stream>>>(p);
}
```

```cpp
#include <hip/hip_runtime.h>
#include <stdint.h>

typedef unsigned v4u __attribute__((ext_vector_type(4)));
typedef unsigned v2u __attribute__((ext_vector_type(2)));
typedef float f32x4 __attribute__((ext_vector_type(4)));
typedef float f32x2 __attribute__((ext_vector_type(2)));
typedef __bf16 bf16x8 __attribute__((ext_vector_type(8)));
typedef __bf16 bf16x2 __attribute__((ext_vector_type(2)));
typedef short s16x4 __attribute__((ext_vector_type(4)));
typedef unsigned short u16x2 __attribute__((ext_vector_type(2)));
typedef unsigned short u16;
#define LAS __attribute__((address_space(3)))
#define GAS __attribute__((address_space(1)))
typedef __amdgpu_buffer_rsrc_t rsrc_t;

#define NT_W 1
#define NT_C 1
#define NT_O 1
#define DLY0 8
#define DLY1 8
#define DLY2 8
#define DLY3 8
#define DLY4 8
#define RETRY 2
#define SPINSLEEP 0
#define WIN 12

constexpr int WS_EPOCH = 0;
constexpr int WS_H0 = 4096;
constexpr int WS_H1 = WS_H0 + 131072;
constexpr int WS_P = WS_H1 + 65536;
constexpr int WS_PS = WS_P + 262144;
constexpr int WS_H2 = WS_PS + 32768;
constexpr int WS_H2S = WS_H2 + 524288;
constexpr int WS_H4 = WS_H2S + 32768;
constexpr int WS_HID = WS_H4 + 256 * 1792;
constexpr int WS_GP = WS_HID + 28 * 7 * 256;
constexpr int WS_END = WS_GP + 28 * 1792;
constexpr int WS_DIAG = 4 << 20;

constexpr int NSLOT = 17;
constexpr int L_RING = 0;
constexpr int L_PART = 2 * NSLOT * 4096;
constexpr int L_XN = L_PART + 16384;
constexpr int L_Q = L_XN + 1024;
constexpr int L_CTX = L_Q + 512;
constexpr int L_HID = L_CTX + 128;
constexpr int L_ST4 = L_HID + 128;
constexpr int L_ST5 = L_ST4 + 1024;
constexpr int L_X = L_ST5 + 128;
constexpr int L_E = L_X + 1024;
constexpr int L_CTL = L_E + 16;
constexpr int L_END = L_CTL + 128;
enum { C_PROG0 = 0, C_PROG1, C_FILL, C_QX, C_S2, C_RELQ, C_RELA0, C_RELA1, C_RELA2, C_RELA3, C_CNT3, C_RED3, C_PPUB, C_CNT4A, C_CNT4B, C_S4, C_CNT5, C_S6, C_X, C_G2, C_PROBE, C_NUM };

constexpr int NWAVE = 15;
constexpr unsigned SENT = 0xFFFFFFFFu;
constexpr int AUX_SC1 = 16;
constexpr unsigned SPIN_G = 1u << 15, SPIN_L = 1u << 19;

struct Params {
    const void* x; const void* ckv; const void* wq; const void* wuk; const void* wuv; const void* wo;
    const void* w1; const void* b1; const void* w2; const void* b2; void* out; void* ws;
};

__device__ __forceinline__ float u2f(unsigned u) { return __builtin_bit_cast(float, u); }
__device__ __forceinline__ unsigned f2u(float f) { return __builtin_bit_cast(unsigned, f); }
__device__ __forceinline__ unsigned pk_bf16(float a, float b) { f32x2 v = {a, b}; bf16x2 r = __builtin_convertvector(v, bf16x2); return __builtin_bit_cast(unsigned, r); }
__device__ __forceinline__ float rbf(float a) { return u2f(pk_bf16(a, a) << 16); }
__device__ __forceinline__ float bflo(unsigned u) { return u2f(u << 16); }
__device__ __forceinline__ float bfhi(unsigned u) { return u2f(u & 0xFFFF0000u); }
__device__ __forceinline__ unsigned san(unsigned u) { return u < 0xFFFFFFFEu ? u : 0xFFFFFFFEu; }
__device__ __forceinline__ v4u san4(v4u v) { return v4u{san(v.x), san(v.y), san(v.z), san(v.w)}; }
__device__ __forceinline__ v4u f4u(f32x4 v) { return __builtin_bit_cast(v4u, v); }
__device__ __forceinline__ f32x4 u4f(v4u v) { return __builtin_bit_cast(f32x4, v); }
__device__ __forceinline__ unsigned umax(unsigned a, unsigned b) { return a > b ? a : b; }
__device__ __forceinline__ unsigned umax4(v4u v) { return umax(umax(v.x, v.y), umax(v.z, v.w)); }
__device__ __forceinline__ f32x4 mfma(v4u a, v4u b, f32x4 c) {
    return __builtin_amdgcn_mfma_f32_16x16x32_bf16(__builtin_bit_cast(bf16x8, a), __builtin_bit_cast(bf16x8, b), c, 0, 0, 0);
}
template <int N> __device__ __forceinline__ float ror(float v) { return u2f((unsigned)__builtin_amdgcn_update_dpp(0, (int)f2u(v), 0x120 + N, 0xf, 0xf, false)); }
__device__ __forceinline__ float row_sum(float v) { v += ror<8>(v); v += ror<4>(v); v += ror<2>(v); v += ror<1>(v); return v; }
__device__ __forceinline__ float row_max(float v) { v = fmaxf(v, ror<8>(v)); v = fmaxf(v, ror<4>(v)); v = fmaxf(v, ror<2>(v)); v = fmaxf(v, ror<1>(v)); return v; }
__device__ __forceinline__ float xrow_sum(float v) {
    auto a = __builtin_amdgcn_permlane32_swap(f2u(v), f2u(v), false, false); v = u2f(a[0]) + u2f(a[1]);
    auto b = __builtin_amdgcn_permlane16_swap(f2u(v), f2u(v), false, false); return u2f(b[0]) + u2f(b[1]);
}
__device__ __forceinline__ float xrow_max(float v) {
    auto a = __builtin_amdgcn_permlane32_swap(f2u(v), f2u(v), false, false); v = fmaxf(u2f(a[0]), u2f(a[1]));
    auto b = __builtin_amdgcn_permlane16_swap(f2u(v), f2u(v), false, false); return fmaxf(u2f(b[0]), u2f(b[1]));
}
__device__ __forceinline__ float wave_sum(float v) { return xrow_sum(row_sum(v)); }

__device__ __forceinline__ rsrc_t mk_rsrc(const void* p, int bytes) { return __builtin_amdgcn_make_buffer_rsrc((void*)p, 0, bytes, 0x00020000); }
__device__ __forceinline__ v4u ld16(rsrc_t r, int off) { return __builtin_amdgcn_raw_buffer_load_b128(r, off, 0, AUX_SC1); }
__device__ __forceinline__ v2u ld8(rsrc_t r, int off) { return __builtin_amdgcn_raw_buffer_load_b64(r, off, 0, AUX_SC1); }
__device__ __forceinline__ unsigned ld4(rsrc_t r, int off) { return __builtin_amdgcn_raw_buffer_load_b32(r, off, 0, AUX_SC1); }
__device__ __forceinline__ void st16(rsrc_t r, int off, v4u v) { __builtin_amdgcn_raw_buffer_store_b128(v, r, off, 0, AUX_SC1); }
__device__ __forceinline__ void st8(rsrc_t r, int off, v2u v) { __builtin_amdgcn_raw_buffer_store_b64(v, r, off, 0, AUX_SC1); }
#define CBAR() asm volatile("" ::: "memory")
#define DRAIN() asm volatile("s_waitcnt vmcnt(0)" ::: "memory")
#define LWAIT() asm volatile("s_waitcnt lgkmcnt(0)" ::: "memory")

typedef LAS unsigned char* lds_t;
__device__ __forceinline__ v4u lds16(lds_t L, int off) { return *(LAS v4u*)(L + off); }
__device__ __forceinline__ v4u lds_tr(lds_t L, int off) {
    s16x4 a = __builtin_amdgcn_ds_read_tr16_b64_v4i16((LAS s16x4*)(L + off));
    s16x4 b = __builtin_amdgcn_ds_read_tr16_b64_v4i16((LAS s16x4*)(L + off + 512));
    v2u a2 = __builtin_bit_cast(v2u, a), b2 = __builtin_bit_cast(v2u, b);
    return v4u{a2.x, a2.y, b2.x, b2.y};
}
__device__ __forceinline__ void wait_ge(lds_t L, int word, unsigned want) {
    volatile LAS unsigned* p = (volatile LAS unsigned*)(L + L_CTL) + word;
#pragma unroll 1
    for (unsigned s = 0; s < SPIN_L; ++s) { if ((unsigned)__builtin_amdgcn_readfirstlane(*p) >= want) break; if (SPINSLEEP) __builtin_amdgcn_s_sleep(SPINSLEEP); }
    CBAR();
}
__device__ __forceinline__ void wait_ge_sleep(lds_t L, int word, unsigned want) {
    volatile LAS unsigned* p = (volatile LAS unsigned*)(L + L_CTL) + word;
#pragma unroll 1
    for (unsigned s = 0; s < SPIN_L; ++s) { if ((unsigned)__builtin_amdgcn_readfirstlane(*p) >= want) break; __builtin_amdgcn_s_sleep(1); }
    CBAR();
}
__device__ __forceinline__ void ctl_add(lds_t L, int word, int lane) {
    CBAR();
    if (lane == 0) __hip_atomic_fetch_add((LAS unsigned*)(L + L_CTL) + word, 1u, __ATOMIC_RELAXED, __HIP_MEMORY_SCOPE_WORKGROUP);
    CBAR();
}
__device__ __forceinline__ bool any_sent(unsigned m) { return __builtin_amdgcn_ballot_w64(m == SENT) != 0ull; }
__device__ __forceinline__ bool any_nz(unsigned m) { return __builtin_amdgcn_ballot_w64(m != 0u) != 0ull; }

#define STAMP(k)
#define STAMP_DECL
#define LSTAMP(n)
#define PASSES(k, s)
#define STAMP_OUT()

#define PROBE 1
#define PROBE_AT(id) do { if (PROBE == (id)) { const unsigned t_ = (unsigned)__builtin_amdgcn_s_memrealtime() - pt0; if (lane == 0) __hip_atomic_fetch_max((LAS unsigned*)(L + L_CTL) + C_PROBE, t_, __ATOMIC_RELAXED, __HIP_MEMORY_SCOPE_WORKGROUP); CBAR(); } } while (0)
template <int NT>
__device__ __forceinline__ void dma_tile(rsrc_t r, unsigned soff, unsigned lds, unsigned v0, unsigned v1, unsigned v2, unsigned v3) {
    if (NT)
        asm volatile("s_mov_b32 m0, %6\n\ts_nop 3\n\t"
                     "buffer_load_dwordx4 %0, %4, %5 offen nt lds\n\t"
                     "buffer_load_dwordx4 %1, %4, %5 offen offset:1024 nt lds\n\t"
                     "buffer_load_dwordx4 %2, %4, %5 offen offset:2048 nt lds\n\t"
                     "buffer_load_dwordx4 %3, %4, %5 offen offset:3072 nt lds"
                     :: "v"(v0), "v"(v1), "v"(v2), "v"(v3), "s"(r), "s"(soff), "s"(lds) : "memory");
    else
        asm volatile("s_mov_b32 m0, %6\n\ts_nop 3\n\t"
                     "buffer_load_dwordx4 %0, %4, %5 offen lds\n\t"
                     "buffer_load_dwordx4 %1, %4, %5 offen offset:1024 lds\n\t"
                     "buffer_load_dwordx4 %2, %4, %5 offen offset:2048 lds\n\t"
                     "buffer_load_dwordx4 %3, %4, %5 offen offset:3072 lds"
                     :: "v"(v0), "v"(v1), "v"(v2), "v"(v3), "s"(r), "s"(soff), "s"(lds) : "memory");
}

__device__ __forceinline__ void layernorm(const v4u xv, lds_t L, int lane) {
    float e[8] = {bflo(xv.x), bfhi(xv.x), bflo(xv.y), bfhi(xv.y), bflo(xv.z), bfhi(xv.z), bflo(xv.w), bfhi(xv.w)};
    float s = ((e[0] + e[1]) + (e[2] + e[3])) + ((e[4] + e[5]) + (e[6] + e[7]));
    const float mean = wave_sum(s) * (1.0f / 448.0f);
    float v = 0.f;
#pragma unroll
    for (int k = 0; k < 8; ++k) { const float d = e[k] - mean; v += d * d; }
    v = lane < 56 ? v : 0.f;
    const float var = wave_sum(v) * (1.0f / 448.0f);
    const float mb = rbf(mean);
    const float sd = rbf(sqrtf(rbf(rbf(var) + 1e-5f)));
    float o[8];
#pragma unroll
    for (int k = 0; k < 8; ++k) o[k] = rbf(e[k] - mb) / sd;
    v4u w = {pk_bf16(o[0], o[1]), pk_bf16(o[2], o[3]), pk_bf16(o[4], o[5]), pk_bf16(o[6], o[7])};
    if (lane >= 56) w = v4u{0u, 0u, 0u, 0u};
    *(LAS v4u*)(L + L_XN + lane * 16) = w;
    CBAR();
}

__global__ void __launch_bounds__(NWAVE * 64, 4) fused_decode(Params p) {
    extern __shared__ __attribute__((aligned(16))) unsigned char smem[];
    lds_t L = (lds_t)smem;
    const int lane = threadIdx.x & 63, wv = __builtin_amdgcn_readfirstlane(threadIdx.x >> 6);
    const int b = blockIdx.x, j = b >> 3;
    const int h = 2 * (b & 7) + (j >> 4);
    const int i16 = j & 15, dq = (j >> 2) & 3, mq = j & 3;
    const int l15 = lane & 15, q4 = lane >> 4;
    STAMP_DECL
    const unsigned pt0 = (unsigned)__builtin_amdgcn_s_memrealtime();

    if (wv < 2) {
        const int pp = wv;
        const unsigned r = lane >> 3, pc = lane & 7, s0 = (r >> 1) & 1;
        const unsigned ce = (pc ^ (s0 << 1)) << 4, co = (pc ^ ((s0 | 2) << 1)) << 4;
#define VOFFS(st) (r * (st) + ce), ((r + 8) * (st) + co - 1024), ((r + 16) * (st) + ce - 2048), ((r + 24) * (st) + co - 3072)
        unsigned lds = L_RING + pp * (NSLOT * 4096);
        const unsigned lds_lo = lds, lds_hi = lds + NSLOT * 4096;
        unsigned n = 0;
        volatile LAS unsigned* prog = (volatile LAS unsigned*)(L + L_CTL) + C_PROG0 + pp;
#define STEP() do { lds += 4096; if (lds == lds_hi) lds = lds_lo; ++n; asm volatile("s_waitcnt vmcnt(%0)" :: "i"(WIN) : "memory"); \
                    if (n > WIN / 4) *prog = n - WIN / 4; CBAR(); LSTAMP(n); } while (0)
        {
            if (pp == 0) {
                asm volatile("s_mov_b32 m0, %3\n\ts_nop 3\n\tbuffer_load_dwordx4 %0, %1, 0 offen lds\n\t"
                             "s_mov_b32 m0, %4\n\ts_mov_b64 exec, 1\n\tbuffer_load_dwordx4 %0, %2, 0 offen lds\n\ts_mov_b64 exec, -1"
                             :: "v"(lane * 16), "s"(mk_rsrc(p.x, 896)), "s"(mk_rsrc(p.ws, 16)), "s"(L_X), "s"(L_E) : "memory");
            }
            const rsrc_t rq = mk_rsrc(p.wq, 448 * 8192);
            const unsigned row0 = mq < 2 ? 128 * mq : 64 + 96 * mq;
            const unsigned so = (row0 + 32 * pp) * 8192 + (h * 256 + dq * 64) * 2;
            dma_tile<NT_W>(rq, so, lds, VOFFS(8192)); lds += 4096;
            dma_tile<NT_W>(mk_rsrc(p.wq, (pp == 1 && mq >= 2) ? 0 : 448 * 8192), so + 64 * 8192, lds, VOFFS(8192)); lds += 4096;
            const rsrc_t rk = mk_rsrc(p.wuk, 1024 * 8192);
            const unsigned sk = (64 * i16 + 32 * pp) * 8192 + h * 512;
            dma_tile<NT_W>(rk, sk, lds, VOFFS(8192)); lds += 4096;
            n = 3;
            __builtin_amdgcn_s_barrier();
            if (pp == 0) { asm volatile("s_waitcnt vmcnt(12)" ::: "memory"); ((volatile LAS unsigned*)(L + L_CTL))[C_X] = 1u; CBAR(); }
#pragma unroll 1
            for (int cb = 1; cb < 4; ++cb) { dma_tile<NT_W>(rk, sk + cb * 128, lds, VOFFS(8192)); STEP(); }
        }
        STAMP(1);
        {
#pragma unroll 1
            for (int t = 0; t < 16; ++t) {
                const unsigned rb = pp ? ((i16 - t) & 15) : i16, cb = pp ? i16 : ((i16 + t) & 15);
                const unsigned so = (512 * h + 32 * rb) * 2048 + cb * 128;
                const rsrc_t rc = mk_rsrc(p.ckv, (pp == 1 && t == 0) ? 0 : 8192 * 2048);
                if (t == 11) wait_ge_sleep(L, C_RELQ, 4);
                dma_tile<NT_C>(rc, so, lds, VOFFS(2048)); STEP();
            }
        }
        STAMP(2);
        {
            const rsrc_t r1 = mk_rsrc(p.w1, b < 196 ? 448 * 3584 : 0);
            dma_tile<NT_W>(r1, b < 196 ? (64 * (b % 7) + 32 * pp) * 3584 + (b / 7) * 128 : 0, lds, VOFFS(3584)); STEP();
            const rsrc_t rv = mk_rsrc(p.wuv, 1024 * 8192);
            const unsigned sv = (256 * mq + 32 * pp) * 8192 + (h * 256 + dq * 64) * 2;
            wait_ge_sleep(L, C_RELA0, 2);
#pragma unroll 1
            for (int k = 0; k < 4; ++k) { dma_tile<NT_W>(rv, sv + k * (64 * 8192), lds, VOFFS(8192)); STEP(); }
            STAMP(3);
            const int bb = b - 60;
            const rsrc_t r2 = mk_rsrc(p.w2, bb >= 0 ? 1792 * 896 : 0);
            wait_ge_sleep(L, C_RELA1, 2);
            dma_tile<NT_W>(r2, bb >= 0 ? (64 * (bb / 7) + 32 * pp) * 896 + (bb % 7) * 128 : 0, lds, VOFFS(896)); STEP();
            const rsrc_t ro = mk_rsrc(p.wo, 4096 * 896);
            const unsigned sw = (h * 256 + dq * 64 + 32 * pp) * 896;
#pragma unroll 1
            for (int k = 0; k < 7; ++k) {
                int cb = k + 2 * mq; if (cb >= 7) cb -= 7;
                if (k == 3) wait_ge_sleep(L, C_RELA2, 2);
                dma_tile<NT_O>(ro, sw + cb * 128, lds, VOFFS(896)); STEP();
            }
        }
        STAMP(4);
        if (WIN > 12) { asm volatile("s_waitcnt vmcnt(12)" ::: "memory"); *prog = 32; }
        asm volatile("s_waitcnt vmcnt(8)" ::: "memory"); *prog = 33;
        asm volatile("s_waitcnt vmcnt(4)" ::: "memory"); *prog = 34;
        asm volatile("s_waitcnt vmcnt(0)" ::: "memory"); *prog = 35;
        PROBE_AT(5);
        STAMP_OUT();
        return;
    }

    const rsrc_t ws = mk_rsrc(p.ws, WS_END);
    const v4u S4 = {SENT, SENT, SENT, SENT};
    const v2u S2 = {SENT, SENT};
    const int swr = ((l15 >> 1) & 1) | (((l15 >> 3) & 1) << 1);
    const int row_a0 = l15 * 128 + ((q4 ^ (swr << 1)) << 4), row_a1 = row_a0 ^ 64;
    const int swt = ((l15 >> 3) & 1) | ((q4 & 1) << 1);
    const int tr_b = (8 * q4 + (l15 >> 2)) * 128 + (((l15 >> 1) & 1) << 4) + 8 * (l15 & 1);
#define TR_A(nt) (tr_b + ((((nt) ^ swt)) << 5))
#define SLOT(pp, n) (L_RING + (pp) * (NSLOT * 4096) + ((n) % NSLOT) * 4096)

    if (wv < 6) {
        const int t = wv - 2;
        const int o_h0 = WS_H0 + ((h * 4 + dq) * 4 + mq) * 512 + (16 * t + 4 * q4 + 2 * l15) * 8;
        const int o_h1 = WS_H1 + (i16 * 16 + h) * 256 + (4 * t + q4) * 16;
        if (l15 < 2) st16(ws, o_h0, v4u{0u, 0u, 0u, 0u});
        if (l15 == 0) st16(ws, o_h1, v4u{0u, 0u, 0u, 0u});
        __builtin_amdgcn_s_barrier();
        wait_ge(L, C_X, 1);
        PROBE_AT(1);
        const v2u ep = *(LAS v2u*)(L + L_E);
        layernorm(lds16(L, L_X + lane * 16), L, lane);
        const unsigned tagA = ep.x + 1u, tagB = ep.y | 1u;
        const int tra = TR_A(t);
        const int row0 = mq < 2 ? 128 * mq : 64 + 96 * mq;
        v4u bx[4];
#pragma unroll
        for (int s = 0; s < 4; ++s) bx[s] = lds16(L, L_XN + (row0 + 32 * s + 8 * q4) * 2);
        STAMP(1);
        wait_ge(L, C_PROG0, 2); wait_ge(L, C_PROG1, 2);
        STAMP(2);
        f32x4 acc = {0.f, 0.f, 0.f, 0.f};
#pragma unroll
        for (int s = 0; s < 4; ++s) acc = mfma(lds_tr(L, SLOT(s & 1, s >> 1) + tra), bx[s], acc);
        if (l15 < 2) st16(ws, o_h0, v4u{f2u(l15 ? acc.z : acc.x), tagA, f2u(l15 ? acc.w : acc.y), tagB});
        STAMP(3);
        PROBE_AT(2);
        __builtin_amdgcn_s_sleep(DLY0);
        CBAR();
        v4u g0[2];
        const int o_g0 = WS_H0 + (h * 4 + t) * 2048 + lane * 16;
        g0[0] = ld16(ws, o_g0); g0[1] = ld16(ws, o_g0 + 1024);
        v4u ak[8];
        wait_ge(L, C_PROG0 + (t >> 1), 6);
        {
            const int base = L_RING + (t >> 1) * (NSLOT * 4096) + (t & 1) * 2048;
#pragma unroll
            for (int cb = 0; cb < 4; ++cb) { ak[2 * cb] = lds16(L, base + (2 + cb) * 4096 + row_a0); ak[2 * cb + 1] = lds16(L, base + (2 + cb) * 4096 + row_a1); }
        }
        ctl_add(L, C_RELQ, lane);
        STAMP(4);
#pragma unroll 1
        for (unsigned s = 0; s < SPIN_G; ++s) { PASSES(0, s);
            if (!any_nz((g0[0].y ^ tagA) | (g0[0].w ^ tagB) | (g0[1].y ^ tagA) | (g0[1].w ^ tagB))) break;
            __builtin_amdgcn_s_sleep(RETRY);
            CBAR();
            g0[0] = ld16(ws, o_g0); g0[1] = ld16(ws, o_g0 + 1024);
        }
        {
            const float qa = u2f(g0[0].x) + u2f(g0[1].x), qb = u2f(g0[0].z) + u2f(g0[1].z);
            auto ra = __builtin_amdgcn_permlane32_swap(f2u(qa), f2u(qa), false, false);
            auto rb = __builtin_amdgcn_permlane32_swap(f2u(qb), f2u(qb), false, false);
            *(LAS unsigned*)(L + L_Q + t * 128 + (lane & 31) * 4) = pk_bf16(u2f(ra[0]) + u2f(ra[1]), u2f(rb[0]) + u2f(rb[1]));
            ctl_add(L, C_QX, lane);
        }
        STAMP(5);
        wait_ge(L, C_FILL, 9);
        wait_ge(L, C_QX, 4);
        STAMP(6);
        {
            acc = f32x4{0.f, 0.f, 0.f, 0.f};
#pragma unroll
            for (int s = 0; s < 8; ++s) acc = mfma(ak[s], lds16(L, L_Q + (32 * s + 8 * q4) * 2), acc);
            const float sc = 0.0625f * 1.4426950408889634f;
            if (l15 == 0) st16(ws, o_h1, v4u{pk_bf16(acc.x * sc, acc.y * sc), tagA, pk_bf16(acc.z * sc, acc.w * sc), tagB});
            ctl_add(L, C_S2, lane);
        }
        STAMP(7);
        PROBE_AT(3);
        wait_ge_sleep(L, C_PROG0, 27); wait_ge_sleep(L, C_PROG1, 27);
#pragma unroll
        for (int s = 0; s < 8; ++s) ak[s] = lds_tr(L, SLOT(s & 1, 23 + (s >> 1)) + tra);
        STAMP(8);
        wait_ge_sleep(L, C_S4, 2);
        STAMP(9);
        __builtin_amdgcn_s_sleep(DLY3);
        {
            v4u g2[2]; v2u st;
            const int o_h2 = WS_H2 + (h * 4 + mq) * 8192 + t * 2048 + lane * 16;
            const int o_st = WS_H2S + (((4 * t + (lane & 3)) * 16 + 4 * mq) * 16 + h) * 8;
#pragma unroll 1
            for (unsigned s = 0; s < SPIN_G; ++s) { PASSES(1, s);
                CBAR();
                g2[0] = ld16(ws, o_h2); g2[1] = ld16(ws, o_h2 + 1024);
                st = v2u{0u, 0u};
                if (lane < 4) st = ld8(ws, o_st);
                if (!any_sent(umax(umax(st.x, st.y), umax(umax4(g2[0]), umax4(g2[1]))))) break;
                __builtin_amdgcn_s_sleep(RETRY);
            }
            const int wa = L_PART + ((lane >> 3) & 3) * 2048 + (4 * t + (lane >> 5)) * 128;
            *(LAS v4u*)(L + wa + (((lane & 7) ^ ((t >> 1) << 2)) << 4)) = g2[0];
            *(LAS v4u*)(L + wa + 256 + (((lane & 7) ^ (((t >> 1) << 2) | 2)) << 4)) = g2[1];
            if (lane < 4) *(LAS v2u*)(L + L_ST5 + (4 * t + lane) * 8) = st;
            ctl_add(L, C_G2, lane);
        }
        STAMP(10);
        wait_ge(L, C_G2, 4);
        STAMP(11);
        acc = f32x4{0.f, 0.f, 0.f, 0.f};
        const v2u st = *(LAS v2u*)(L + L_ST5 + l15 * 8);
#pragma unroll
        for (int s = 0; s < 8; ++s) acc = mfma(ak[s], lds16(L, L_PART + (s >> 1) * 2048 + ((s & 1) ? row_a1 : row_a0)), acc);
        {
            const float rg = u2f(st.x), lg = u2f(st.y);
            const float R = row_max(rg);
            const float sc = __builtin_amdgcn_exp2f(rg - R);
            const float wgt = sc / row_sum(sc * lg);
            const float c0 = row_sum(acc.x * wgt), c1 = row_sum(acc.y * wgt), c2 = row_sum(acc.z * wgt), c3 = row_sum(acc.w * wgt);
            if (l15 == 0) *(LAS v2u*)(L + L_CTX + (16 * t + 4 * q4) * 2) = v2u{pk_bf16(c0, c1), pk_bf16(c2, c3)};
        }
        ctl_add(L, C_CNT5, lane);
        STAMP_OUT();
        return;
    }

    if (wv == 6) {
        const int bb = b - 60;
        const int fs1 = b / 7, ms1 = b % 7, fs2 = bb / 7, mt2 = bb % 7;
        const int o_hid = WS_HID + (fs1 * 7 + ms1) * 256 + (16 * l15 + 4 * q4) * 4;
        const int o_gp = WS_GP + fs2 * 1792 + (64 * mt2 + 16 * l15 + 4 * q4) * 4;
        if (l15 < 4) { if (b < 196) st16(ws, o_hid, S4); if (bb >= 0) st16(ws, o_gp, S4); }
        if (lane < C_NUM) ((LAS unsigned*)(L + L_CTL))[lane] = 0u;
        LWAIT();
        __builtin_amdgcn_s_barrier();
        const int u = b - 144;
        DRAIN();
        ctl_add(L, C_FILL, lane);
        STAMP(1);
        wait_ge_sleep(L, C_RELQ, 4);
        unsigned b1v = 0; v2u b2v = {0u, 0u}, xv = {0u, 0u};
        if (bb >= 0) b1v = __builtin_nontemporal_load((const GAS u16*)p.b1 + 64 * fs2 + lane);
        if (u >= 0) { b2v = __builtin_nontemporal_load((const GAS v2u*)p.b2 + u); xv = *(LAS v2u*)(L + L_X + u * 8); }
        const v2u ep = *(LAS v2u*)(L + L_E);
        if (b < 196) {
            wait_ge_sleep(L, C_PROG0, 23); wait_ge_sleep(L, C_PROG1, 23);
            const v4u bx0 = lds16(L, L_XN + (64 * ms1 + 8 * q4) * 2), bx1 = lds16(L, L_XN + (64 * ms1 + 32 + 8 * q4) * 2);
            f32x4 a[4];
#pragma unroll
            for (int nt = 0; nt < 4; ++nt) {
                a[nt] = mfma(lds_tr(L, SLOT(0, 22) + TR_A(nt)), bx0, f32x4{0.f, 0.f, 0.f, 0.f});
                a[nt] = mfma(lds_tr(L, SLOT(1, 22) + TR_A(nt)), bx1, a[nt]);
            }
            const f32x4 o = l15 == 0 ? a[0] : l15 == 1 ? a[1] : l15 == 2 ? a[2] : a[3];
            if (l15 < 4) st16(ws, o_hid, san4(f4u(o)));
        }
        STAMP(3);
        wait_ge_sleep(L, C_CNT3, 8);
        if (bb >= 0) {
            unsigned hp[7];
            __builtin_amdgcn_s_sleep(8);
#pragma unroll 1
            for (unsigned s = 0; s < SPIN_G; ++s) { PASSES(2, s);
                CBAR();
                unsigned m = 0;
#pragma unroll
                for (int k = 0; k < 7; ++k) hp[k] = ld4(ws, WS_HID + (fs2 * 7 + k) * 256 + lane * 4);
#pragma unroll
                for (int k = 0; k < 7; ++k) m = umax(m, hp[k]);
                if (!any_sent(m)) break;
                __builtin_amdgcn_s_sleep(4);
            }
            float hs = 0.f;
#pragma unroll
            for (int k = 0; k < 7; ++k) hs += u2f(hp[k]);
            hs = rbf(rbf(hs) + bflo(b1v));
            hs = hs > 0.f ? hs : 0.f;
            *(LAS u16*)(L + L_HID + lane * 2) = (u16)pk_bf16(hs * hs, 0.f);
            CBAR();
            STAMP(4);
            wait_ge_sleep(L, C_PROG0, 28); wait_ge_sleep(L, C_PROG1, 28);
            const v4u bh0 = lds16(L, L_HID + 8 * q4 * 2), bh1 = lds16(L, L_HID + (32 + 8 * q4) * 2);
            f32x4 a[4];
#pragma unroll
            for (int nt = 0; nt < 4; ++nt) {
                a[nt] = mfma(lds_tr(L, SLOT(0, 27) + TR_A(nt)), bh0, f32x4{0.f, 0.f, 0.f, 0.f});
                a[nt] = mfma(lds_tr(L, SLOT(1, 27) + TR_A(nt)), bh1, a[nt]);
            }
            const f32x4 o = l15 == 0 ? a[0] : l15 == 1 ? a[1] : l15 == 2 ? a[2] : a[3];
            if (l15 < 4) st16(ws, o_gp, san4(f4u(o)));
        }
        STAMP(5);
        if (u >= 0) {
            v4u gv;
            const int lg = lane < 28 ? lane : 27;
            __builtin_amdgcn_s_sleep(8);
#pragma unroll 1
            for (unsigned s = 0; s < SPIN_G; ++s) { PASSES(3, s);
                CBAR();
                gv = ld16(ws, WS_GP + lg * 1792 + u * 16);
                if (!any_sent(umax4(gv))) break;
                __builtin_amdgcn_s_sleep(4);
            }
            f32x4 gf = u4f(gv);
            if (lane >= 28) gf = f32x4{0.f, 0.f, 0.f, 0.f};
            float g[4] = {wave_sum(gf.x), wave_sum(gf.y), wave_sum(gf.z), wave_sum(gf.w)};
            const float bb2[4] = {bflo(b2v.x), bfhi(b2v.x), bflo(b2v.y), bfhi(b2v.y)};
#pragma unroll
            for (int k = 0; k < 4; ++k) { const float a = rbf(rbf(g[k]) + bb2[k]); g[k] = rbf(a / (1.f + __expf(-a))); }
            STAMP(6);
            wait_ge(L, C_S6, 7);
            STAMP(7);
            __builtin_amdgcn_s_sleep(DLY4);
            v4u sv[4];
#pragma unroll 1
            for (unsigned s = 0; s < SPIN_G; ++s) { PASSES(4, s);
                CBAR();
#pragma unroll
                for (int k = 0; k < 4; ++k) sv[k] = ld16(ws, WS_H4 + (lane + 64 * k) * 1792 + u * 16);
                if (!any_sent(umax(umax(umax4(sv[0]), umax4(sv[1])), umax(umax4(sv[2]), umax4(sv[3]))))) break;
                __builtin_amdgcn_s_sleep(RETRY);
            }
            STAMP(8);
            const f32x4 sf = (u4f(sv[0]) + u4f(sv[1])) + (u4f(sv[2]) + u4f(sv[3]));
            const float s[4] = {wave_sum(sf.x), wave_sum(sf.y), wave_sum(sf.z), wave_sum(sf.w)};
            const float xx[4] = {bflo(xv.x), bfhi(xv.x), bflo(xv.y), bfhi(xv.y)};
            float o[4];
#pragma unroll
            for (int k = 0; k < 4; ++k) o[k] = xx[k] + rbf(rbf(s[k]) * g[k]);
            if (lane == 0) ((GAS v2u*)p.out)[u] = v2u{pk_bf16(o[0], o[1]), pk_bf16(o[2], o[3])};
            if (u == 0 && lane == 0) st8(ws, WS_EPOCH, v2u{ep.x + 1u, ep.y});
            PROBE_AT(0);
            if (PROBE >= 0) {
                const unsigned d_ = __builtin_amdgcn_readfirstlane(((volatile LAS unsigned*)(L + L_CTL))[C_PROBE]);
                const unsigned t1_ = (unsigned)__builtin_amdgcn_s_memrealtime();
                while ((unsigned)__builtin_amdgcn_s_memrealtime() - t1_ < 10u * (d_ < 20000u ? d_ : 20000u)) __builtin_amdgcn_s_sleep(4);
            }
        }
        STAMP_OUT();
        return;
    }

    {
        const int w = wv - 7, kq = w >> 1, ch = w & 1;
        int cbm = w + 2 * mq; if (cbm >= 7) cbm -= 7;
        const int o_p = WS_P + (h * 16 + i16) * 1024 + lane * 16;
        const int o_ps = WS_PS + ((h * 16 + i16) * 16 + l15) * 8;
        const int o_h2 = WS_H2 + (l15 * 4 + (i16 >> 2)) * 8192 + h * 512 + (64 * (i16 & 3) + 32 * ch + 8 * q4) * 2;
        const int o_h2s = WS_H2S + ((h * 16 + i16) * 16 + l15) * 8;
        const int o_h4 = WS_H4 + b * 1792 + (64 * cbm + 16 * l15 + 4 * q4) * 4;
        if (w == 0) { st16(ws, o_p, S4); if (lane < 16) { st8(ws, o_ps, S2); st8(ws, o_h2s, S2); } }
        if (w < 2) st16(ws, o_h2, S4);
        if (w < 7 && l15 < 4) st16(ws, o_h4, S4);
        __builtin_amdgcn_s_barrier();
        DRAIN();
        ctl_add(L, C_FILL, lane);
        v4u ar[8], ac[8];
        const int rbase = L_RING + ((6 + 2 * w) % NSLOT) * 4096, rbase1 = L_RING + ((7 + 2 * w) % NSLOT) * 4096;
        const int tr0 = TR_A(2 * ch), tr1 = TR_A(2 * ch + 1);
#define PULL_R() do { wait_ge(L, C_PROG0, 8 + 2 * w); \
            ar[0] = lds16(L, rbase + row_a0); ar[1] = lds16(L, rbase + row_a1); ar[2] = lds16(L, rbase1 + row_a0); ar[3] = lds16(L, rbase1 + row_a1); \
            ar[4] = lds16(L, rbase + 2048 + row_a0); ar[5] = lds16(L, rbase + 2048 + row_a1); ar[6] = lds16(L, rbase1 + 2048 + row_a0); ar[7] = lds16(L, rbase1 + 2048 + row_a1); } while (0)
#define PULL_C() do { wait_ge(L, C_PROG1, 10 + 4 * kq); \
            _Pragma("unroll") for (int u = 0; u < 4; ++u) { int cbase = L_RING + NSLOT * 4096 + ((6 + 4 * kq + u) % NSLOT) * 4096; if (u == 0 && kq == 0) cbase = L_RING + 6 * 4096; \
                ac[2 * u] = lds_tr(L, cbase + tr0); ac[2 * u + 1] = lds_tr(L, cbase + tr1); } \
            LWAIT(); ctl_add(L, C_RELA0 + kq, lane); } while (0)
        STAMP(1);
        if (w < 4) { PULL_R(); PULL_C(); }
        STAMP(2);
        wait_ge_sleep(L, C_S2, 4);
        __builtin_amdgcn_s_sleep(DLY1);
        v4u bq[4];
        {
            const v2u ep = *(LAS v2u*)(L + L_E);
            const unsigned tagA = ep.x + 1u, tagB = ep.y | 1u;
            const int o0 = WS_H1 + ((i16 + 2 * w) & 15) * 4096 + lane * 16, o1 = WS_H1 + ((i16 + 2 * w + 1) & 15) * 4096 + lane * 16;
            v4u g[8];
#pragma unroll 1
            for (unsigned s = 0; s < SPIN_G; ++s) { PASSES(5, s);
                CBAR();
                unsigned m = 0;
#pragma unroll
                for (int k = 0; k < 4; ++k) { g[k] = ld16(ws, o0 + 1024 * k); g[4 + k] = ld16(ws, o1 + 1024 * k); }
#pragma unroll
                for (int k = 0; k < 8; ++k) m |= (g[k].y ^ tagA) | (g[k].w ^ tagB);
                if (!any_nz(m)) break;
                __builtin_amdgcn_s_sleep(RETRY);
            }
            const int pw = L_PART + w * 2048;
            const int wa0 = pw + q4 * 128 + (((l15 >> 1) ^ (q4 & 2)) << 4) + (l15 & 1) * 8, wa1 = wa0 ^ 64;
#pragma unroll
            for (int hb = 0; hb < 2; ++hb) {
#pragma unroll
                for (int k = 0; k < 4; ++k) *(LAS v2u*)(L + (k < 2 ? wa0 : wa1) + k * 512) = v2u{g[4 * hb + k].x, g[4 * hb + k].z};
                CBAR();
                bq[2 * hb] = lds16(L, pw + row_a0); bq[2 * hb + 1] = lds16(L, pw + row_a1);
                CBAR();
            }
        }
        STAMP(3);
        if (w >= 4) PULL_R();
        STAMP(4);
        {
            f32x4 s0 = {0.f, 0.f, 0.f, 0.f}, s1 = s0;
#pragma unroll
            for (int k = 0; k < 4; ++k) { s0 = mfma(ar[k], bq[k], s0); s1 = mfma(ar[4 + k], bq[k], s1); }
            *(LAS f32x4*)(L + L_PART + w * 2048 + lane * 16) = s0;
            *(LAS f32x4*)(L + L_PART + w * 2048 + 1024 + lane * 16) = s1;
            ctl_add(L, C_CNT3, lane);
        }
        STAMP(5);
        if (w >= 4) PULL_C();
        const int red_a = (q4 >> 1) * 1024 + ((q4 & 1) * 32 + l15) * 16;
        if (w == 0) {
            wait_ge(L, C_CNT3, 8);
            STAMP(6);
            f32x4 a0 = {0.f, 0.f, 0.f, 0.f}, a1 = a0;
#pragma unroll
            for (int k = 0; k < 8; ++k) { a0 += *(LAS f32x4*)(L + L_PART + k * 2048 + red_a); a1 += *(LAS f32x4*)(L + L_PART + k * 2048 + red_a + 256); }
            const float mx = xrow_max(fmaxf(fmaxf(fmaxf(a0.x, a0.y), fmaxf(a0.z, a0.w)), fmaxf(fmaxf(a1.x, a1.y), fmaxf(a1.z, a1.w))));
            const float r = __builtin_ceilf(mx);
            const float p0 = __builtin_amdgcn_exp2f(a0.x - r), p1 = __builtin_amdgcn_exp2f(a0.y - r), p2 = __builtin_amdgcn_exp2f(a0.z - r), p3 = __builtin_amdgcn_exp2f(a0.w - r);
            const float p4 = __builtin_amdgcn_exp2f(a1.x - r), p5 = __builtin_amdgcn_exp2f(a1.y - r), p6 = __builtin_amdgcn_exp2f(a1.z - r), p7 = __builtin_amdgcn_exp2f(a1.w - r);
            st16(ws, o_p, san4(v4u{pk_bf16(p0, p1), pk_bf16(p2, p3), pk_bf16(p4, p5), pk_bf16(p6, p7)}));
            const float l = xrow_sum(((p0 + p1) + (p2 + p3)) + ((p4 + p5) + (p6 + p7)));
            if (lane < 16) st8(ws, o_ps, v2u{san(f2u(r)), san(f2u(l))});
            ctl_add(L, C_PPUB, lane);
            PROBE_AT(4);
        }
        STAMP(7);
        wait_ge(L, C_PPUB, 1);
        __builtin_amdgcn_s_sleep(DLY2);
        v4u bp[4]; v2u ps[4];
        {
            int op[4], os[4];
#pragma unroll
            for (int u = 0; u < 4; ++u) { const int iu = (i16 - 4 * kq - u) & 15; op[u] = WS_P + (h * 16 + iu) * 1024 + lane * 16; os[u] = WS_PS + ((h * 16 + iu) * 16 + l15) * 8; }
#pragma unroll 1
            for (unsigned s = 0; s < SPIN_G; ++s) { PASSES(6, s);
                CBAR();
                unsigned m = 0;
#pragma unroll
                for (int u = 0; u < 4; ++u) { bp[u] = ld16(ws, op[u]); ps[u] = ld8(ws, os[u]); }
#pragma unroll
                for (int u = 0; u < 4; ++u) m = umax(m, umax(umax4(bp[u]), umax(ps[u].x, ps[u].y)));
                if (!any_sent(m)) break;
                __builtin_amdgcn_s_sleep(RETRY);
            }
        }
        STAMP(8);
        {
            const float rw = fmaxf(fmaxf(u2f(ps[0].x), u2f(ps[1].x)), fmaxf(u2f(ps[2].x), u2f(ps[3].x)));
            f32x4 c0 = {0.f, 0.f, 0.f, 0.f}, c1 = c0;
            float lw = 0.f;
#pragma unroll
            for (int u = 0; u < 4; ++u) {
                const float dl = rw - u2f(ps[u].x);
                const unsigned dd = (unsigned)fminf(dl, 255.f) * 0x00800080u;
                const u16x2 d2 = __builtin_bit_cast(u16x2, dd);
                const unsigned x0 = bp[u].x, x1 = bp[u].y, x2 = bp[u].z, x3 = bp[u].w;
                const v4u bs = {__builtin_bit_cast(unsigned, __builtin_elementwise_sub_sat(__builtin_bit_cast(u16x2, x0), d2)),
                                __builtin_bit_cast(unsigned, __builtin_elementwise_sub_sat(__builtin_bit_cast(u16x2, x1), d2)),
                                __builtin_bit_cast(unsigned, __builtin_elementwise_sub_sat(__builtin_bit_cast(u16x2, x2), d2)),
                                __builtin_bit_cast(unsigned, __builtin_elementwise_sub_sat(__builtin_bit_cast(u16x2, x3), d2))};
                c0 = mfma(ac[2 * u], bs, c0); c1 = mfma(ac[2 * u + 1], bs, c1);
                lw += __builtin_amdgcn_exp2f(-dl) * u2f(ps[u].y);
            }
            *(LAS f32x4*)(L + L_PART + w * 2048 + lane * 16) = c0;
            *(LAS f32x4*)(L + L_PART + w * 2048 + 1024 + lane * 16) = c1;
            if (lane < 16) *(LAS f32x2*)(L + L_ST4 + ((ch * 4 + kq) * 16 + l15) * 8) = f32x2{rw, lw};
            ctl_add(L, C_CNT4A + ch, lane);
        }
        STAMP(9);
        if (w < 2) {
            wait_ge(L, C_CNT4A + ch, 4);
            STAMP(10);
            f32x4 v0[4], v1[4]; f32x2 sx[4];
#pragma unroll
            for (int k = 0; k < 4; ++k) {
                v0[k] = *(LAS f32x4*)(L + L_PART + (2 * k + ch) * 2048 + red_a); v1[k] = *(LAS f32x4*)(L + L_PART + (2 * k + ch) * 2048 + red_a + 256);
                sx[k] = *(LAS f32x2*)(L + L_ST4 + ((ch * 4 + k) * 16 + l15) * 8);
            }
            const float R = fmaxf(fmaxf(sx[0].x, sx[1].x), fmaxf(sx[2].x, sx[3].x));
            f32x4 a0 = {0.f, 0.f, 0.f, 0.f}, a1 = a0; float lg = 0.f;
#pragma unroll
            for (int k = 0; k < 4; ++k) { const float sc = __builtin_amdgcn_exp2f(sx[k].x - R); a0 += v0[k] * sc; a1 += v1[k] * sc; lg += sx[k].y * sc; }
            st16(ws, o_h2, san4(v4u{pk_bf16(a0.x, a0.y), pk_bf16(a0.z, a0.w), pk_bf16(a1.x, a1.y), pk_bf16(a1.z, a1.w)}));
            if (w == 0 && lane < 16) st8(ws, o_h2s, v2u{san(f2u(R)), san(f2u(lg))});
            ctl_add(L, C_S4, lane);
        }
        STAMP(11);
        if (w < 7) {
            wait_ge_sleep(L, C_PROG0, 29 + w); wait_ge_sleep(L, C_PROG1, 29 + w);
            const int ob0 = L_RING + ((28 + w) % NSLOT) * 4096, ob1 = ob0 + NSLOT * 4096;
#pragma unroll
            for (int nt = 0; nt < 4; ++nt) { ar[nt] = lds_tr(L, ob0 + TR_A(nt)); ar[4 + nt] = lds_tr(L, ob1 + TR_A(nt)); }
            STAMP(12);
            wait_ge(L, C_CNT5, 4);
            STAMP(13);
            const v4u bc0 = lds16(L, L_CTX + 8 * q4 * 2), bc1 = lds16(L, L_CTX + (32 + 8 * q4) * 2);
            f32x4 a[4];
#pragma unroll
            for (int nt = 0; nt < 4; ++nt) { a[nt] = mfma(ar[nt], bc0, f32x4{0.f, 0.f, 0.f, 0.f}); a[nt] = mfma(ar[4 + nt], bc1, a[nt]); }
            const f32x4 o = l15 == 0 ? a[0] : l15 == 1 ? a[1] : l15 == 2 ? a[2] : a[3];
            if (l15 < 4) st16(ws, o_h4, san4(f4u(o)));
            ctl_add(L, C_S6, lane);
        }
        STAMP_OUT();
    }
}

extern "C" void kernel_launch(void* const* d_in, const int* in_sizes, int n_in,
                              void* d_out, int out_size, void* d_ws, size_t ws_size,
                              hipStream_t stream) {
    Params p;
    p.x = d_in[0]; p.ckv = d_in[1]; p.wq = d_in[2]; p.wuk = d_in[3]; p.wuv = d_in[4]; p.wo = d_in[5];
    p.w1 = d_in[7]; p.b1 = d_in[8]; p.w2 = d_in[9]; p.b2 = d_in[10]; p.out = d_out; p.ws = d_ws;
    (void)hipFuncSetAttribute((const void*)fused_decode, hipFuncAttributeMaxDynamicSharedMemorySize, L_END);
    fused_decode<<<256, NWAVE * 64, L_END, stream>>>(p);
}
```

```cpp
#include <hip/hip_runtime.h>
#include <stdint.h>

typedef unsigned v4u __attribute__((ext_vector_type(4)));
typedef unsigned v2u __attribute__((ext_vector_type(2)));
typedef float f32x4 __attribute__((ext_vector_type(4)));
typedef float f32x2 __attribute__((ext_vector_type(2)));
typedef __bf16 bf16x8 __attribute__((ext_vector_type(8)));
typedef __bf16 bf16x2 __attribute__((ext_vector_type(2)));
typedef short s16x4 __attribute__((ext_vector_type(4)));
typedef unsigned short u16x2 __attribute__((ext_vector_type(2)));
typedef unsigned short u16;
#define LAS __attribute__((address_space(3)))
#define GAS __attribute__((address_space(1)))
typedef __amdgpu_buffer_rsrc_t rsrc_t;

#define NT_W 1
#define NT_C 1
#define NT_O 1
#define DLY0 8
#define DLY1 8
#define DLY2 8
#define DLY3 8
#define DLY4 8
#define RETRY 2
#define SPINSLEEP 0
#define WIN 12

constexpr int WS_EPOCH = 0;
constexpr int WS_H0 = 4096;
constexpr int WS_H1 = WS_H0 + 131072;
constexpr int WS_P = WS_H1 + 65536;
constexpr int WS_PS = WS_P + 262144;
constexpr int WS_H2 = WS_PS + 32768;
constexpr int WS_H2S = WS_H2 + 524288;
constexpr int WS_H4 = WS_H2S + 32768;
constexpr int WS_HID = WS_H4 + 256 * 1792;
constexpr int WS_GP = WS_HID + 28 * 7 * 256;
constexpr int WS_END = WS_GP + 28 * 1792;
constexpr int WS_DIAG = 4 << 20;

constexpr int NSLOT = 17;
constexpr int L_RING = 0;
constexpr int L_PART = 2 * NSLOT * 4096;
constexpr int L_XN = L_PART + 16384;
constexpr int L_Q = L_XN + 1024;
constexpr int L_CTX = L_Q + 512;
constexpr int L_HID = L_CTX + 128;
constexpr int L_ST4 = L_HID + 128;
constexpr int L_ST5 = L_ST4 + 1024;
constexpr int L_X = L_ST5 + 128;
constexpr int L_E = L_X + 1024;
constexpr int L_CTL = L_E + 16;
constexpr int L_END = L_CTL + 128;
enum { C_PROG0 = 0, C_PROG1, C_FILL, C_QX, C_S2, C_RELQ, C_RELA0, C_RELA1, C_RELA2, C_RELA3, C_CNT3, C_RED3, C_PPUB, C_CNT4A, C_CNT4B, C_S4, C_CNT5, C_S6, C_X, C_G2, C_NUM };

constexpr int NWAVE = 15;
constexpr unsigned SENT = 0xFFFFFFFFu;
constexpr int AUX_SC1 = 16;
constexpr unsigned SPIN_G = 1u << 15, SPIN_L = 1u << 19;

struct Params {
    const void* x; const void* ckv; const void* wq; const void* wuk; const void* wuv; const void* wo;
    const void* w1; const void* b1; const void* w2; const void* b2; void* out; void* ws;
};

__device__ __forceinline__ float u2f(unsigned u) { return __builtin_bit_cast(float, u); }
__device__ __forceinline__ unsigned f2u(float f) { return __builtin_bit_cast(unsigned, f); }
__device__ __forceinline__ unsigned pk_bf16(float a, float b) { f32x2 v = {a, b}; bf16x2 r = __builtin_convertvector(v, bf16x2); return __builtin_bit_cast(unsigned, r); }
__device__ __forceinline__ float rbf(float a) { return u2f(pk_bf16(a, a) << 16); }
__device__ __forceinline__ float bflo(unsigned u) { return u2f(u << 16); }
__device__ __forceinline__ float bfhi(unsigned u) { return u2f(u & 0xFFFF0000u); }
__device__ __forceinline__ unsigned san(unsigned u) { return u < 0xFFFFFFFEu ? u : 0xFFFFFFFEu; }
__device__ __forceinline__ v4u san4(v4u v) { return v4u{san(v.x), san(v.y), san(v.z), san(v.w)}; }
__device__ __forceinline__ v4u f4u(f32x4 v) { return __builtin_bit_cast(v4u, v); }
__device__ __forceinline__ f32x4 u4f(v4u v) { return __builtin_bit_cast(f32x4, v); }
__device__ __forceinline__ unsigned umax(unsigned a, unsigned b) { return a > b ? a : b; }
__device__ __forceinline__ unsigned umax4(v4u v) { return umax(umax(v.x, v.y), umax(v.z, v.w)); }
__device__ __forceinline__ f32x4 mfma(v4u a, v4u b, f32x4 c) {
    return __builtin_amdgcn_mfma_f32_16x16x32_bf16(__builtin_bit_cast(bf16x8, a), __builtin_bit_cast(bf16x8, b), c, 0, 0, 0);
}
template <int N> __device__ __forceinline__ float ror(float v) { return u2f((unsigned)__builtin_amdgcn_update_dpp(0, (int)f2u(v), 0x120 + N, 0xf, 0xf, false)); }
__device__ __forceinline__ float row_sum(float v) { v += ror<8>(v); v += ror<4>(v); v += ror<2>(v); v += ror<1>(v); return v; }
__device__ __forceinline__ float row_max(float v) { v = fmaxf(v, ror<8>(v)); v = fmaxf(v, ror<4>(v)); v = fmaxf(v, ror<2>(v)); v = fmaxf(v, ror<1>(v)); return v; }
__device__ __forceinline__ float xrow_sum(float v) {
    auto a = __builtin_amdgcn_permlane32_swap(f2u(v), f2u(v), false, false); v = u2f(a[0]) + u2f(a[1]);
    auto b = __builtin_amdgcn_permlane16_swap(f2u(v), f2u(v), false, false); return u2f(b[0]) + u2f(b[1]);
}
__device__ __forceinline__ float xrow_max(float v) {
    auto a = __builtin_amdgcn_permlane32_swap(f2u(v), f2u(v), false, false); v = fmaxf(u2f(a[0]), u2f(a[1]));
    auto b = __builtin_amdgcn_permlane16_swap(f2u(v), f2u(v), false, false); return fmaxf(u2f(b[0]), u2f(b[1]));
}
__device__ __forceinline__ float wave_sum(float v) { return xrow_sum(row_sum(v)); }

__device__ __forceinline__ rsrc_t mk_rsrc(const void* p, int bytes) { return __builtin_amdgcn_make_buffer_rsrc((void*)p, 0, bytes, 0x00020000); }
__device__ __forceinline__ v4u ld16(rsrc_t r, int off) { return __builtin_amdgcn_raw_buffer_load_b128(r, off, 0, AUX_SC1); }
__device__ __forceinline__ v2u ld8(rsrc_t r, int off) { return __builtin_amdgcn_raw_buffer_load_b64(r, off, 0, AUX_SC1); }
__device__ __forceinline__ unsigned ld4(rsrc_t r, int off) { return __builtin_amdgcn_raw_buffer_load_b32(r, off, 0, AUX_SC1); }
__device__ __forceinline__ void st16(rsrc_t r, int off, v4u v) { __builtin_amdgcn_raw_buffer_store_b128(v, r, off, 0, AUX_SC1); }
__device__ __forceinline__ void st8(rsrc_t r, int off, v2u v) { __builtin_amdgcn_raw_buffer_store_b64(v, r, off, 0, AUX_SC1); }
#define CBAR() asm volatile("" ::: "memory")
#define DRAIN() asm volatile("s_waitcnt vmcnt(0)" ::: "memory")
#define LWAIT() asm volatile("s_waitcnt lgkmcnt(0)" ::: "memory")

typedef LAS unsigned char* lds_t;
__device__ __forceinline__ v4u lds16(lds_t L, int off) { return *(LAS v4u*)(L + off); }
__device__ __forceinline__ v4u lds_tr(lds_t L, int off) {
    s16x4 a = __builtin_amdgcn_ds_read_tr16_b64_v4i16((LAS s16x4*)(L + off));
    s16x4 b = __builtin_amdgcn_ds_read_tr16_b64_v4i16((LAS s16x4*)(L + off + 512));
    v2u a2 = __builtin_bit_cast(v2u, a), b2 = __builtin_bit_cast(v2u, b);
    return v4u{a2.x, a2.y, b2.x, b2.y};
}
__device__ __forceinline__ void wait_ge(lds_t L, int word, unsigned want) {
    volatile LAS unsigned* p = (volatile LAS unsigned*)(L + L_CTL) + word;
#pragma unroll 1
    for (unsigned s = 0; s < SPIN_L; ++s) { if ((unsigned)__builtin_amdgcn_readfirstlane(*p) >= want) break; if (SPINSLEEP) __builtin_amdgcn_s_sleep(SPINSLEEP); }
    CBAR();
}
__device__ __forceinline__ void wait_ge_sleep(lds_t L, int word, unsigned want) {
    volatile LAS unsigned* p = (volatile LAS unsigned*)(L + L_CTL) + word;
#pragma unroll 1
    for (unsigned s = 0; s < SPIN_L; ++s) { if ((unsigned)__builtin_amdgcn_readfirstlane(*p) >= want) break; __builtin_amdgcn_s_sleep(1); }
    CBAR();
}
__device__ __forceinline__ void ctl_add(lds_t L, int word, int lane) {
    CBAR();
    if (lane == 0) __hip_atomic_fetch_add((LAS unsigned*)(L + L_CTL) + word, 1u, __ATOMIC_RELAXED, __HIP_MEMORY_SCOPE_WORKGROUP);
    CBAR();
}
__device__ __forceinline__ bool any_sent(unsigned m) { return __builtin_amdgcn_ballot_w64(m == SENT) != 0ull; }
__device__ __forceinline__ bool any_nz(unsigned m) { return __builtin_amdgcn_ballot_w64(m != 0u) != 0ull; }

#define STAMP(k)
#define STAMP_DECL
#define LSTAMP(n)
#define PASSES(k, s)
#define STAMP_OUT()

template <int NT>
__device__ __forceinline__ void dma_tile(rsrc_t r, unsigned soff, unsigned lds, unsigned v0, unsigned v1, unsigned v2, unsigned v3) {
    if (NT)
        asm volatile("s_mov_b32 m0, %6\n\ts_nop 3\n\t"
                     "buffer_load_dwordx4 %0, %4, %5 offen nt lds\n\t"
                     "buffer_load_dwordx4 %1, %4, %5 offen offset:1024 nt lds\n\t"
                     "buffer_load_dwordx4 %2, %4, %5 offen offset:2048 nt lds\n\t"
                     "buffer_load_dwordx4 %3, %4, %5 offen offset:3072 nt lds"
                     :: "v"(v0), "v"(v1), "v"(v2), "v"(v3), "s"(r), "s"(soff), "s"(lds) : "memory");
    else
        asm volatile("s_mov_b32 m0, %6\n\ts_nop 3\n\t"
                     "buffer_load_dwordx4 %0, %4, %5 offen lds\n\t"
                     "buffer_load_dwordx4 %1, %4, %5 offen offset:1024 lds\n\t"
                     "buffer_load_dwordx4 %2, %4, %5 offen offset:2048 lds\n\t"
                     "buffer_load_dwordx4 %3, %4, %5 offen offset:3072 lds"
                     :: "v"(v0), "v"(v1), "v"(v2), "v"(v3), "s"(r), "s"(soff), "s"(lds) : "memory");
}

__device__ __forceinline__ void layernorm(const v4u xv, lds_t L, int lane) {
    float e[8] = {bflo(xv.x), bfhi(xv.x), bflo(xv.y), bfhi(xv.y), bflo(xv.z), bfhi(xv.z), bflo(xv.w), bfhi(xv.w)};
    float s = ((e[0] + e[1]) + (e[2] + e[3])) + ((e[4] + e[5]) + (e[6] + e[7]));
    const float mean = wave_sum(s) * (1.0f / 448.0f);
    float v = 0.f;
#pragma unroll
    for (int k = 0; k < 8; ++k) { const float d = e[k] - mean; v += d * d; }
    v = lane < 56 ? v : 0.f;
    const float var = wave_sum(v) * (1.0f / 448.0f);
    const float mb = rbf(mean);
    const float sd = rbf(sqrtf(rbf(rbf(var) + 1e-5f)));
    float o[8];
#pragma unroll
    for (int k = 0; k < 8; ++k) o[k] = rbf(e[k] - mb) / sd;
    v4u w = {pk_bf16(o[0], o[1]), pk_bf16(o[2], o[3]), pk_bf16(o[4], o[5]), pk_bf16(o[6], o[7])};
    if (lane >= 56) w = v4u{0u, 0u, 0u, 0u};
    *(LAS v4u*)(L + L_XN + lane * 16) = w;
    CBAR();
}

__global__ void __launch_bounds__(NWAVE * 64, 4) fused_decode(Params p) {
    extern __shared__ __attribute__((aligned(16))) unsigned char smem[];
    lds_t L = (lds_t)smem;
    const int lane = threadIdx.x & 63, wv = __builtin_amdgcn_readfirstlane(threadIdx.x >> 6);
    const int b = blockIdx.x, j = b >> 3;
    const int h = 2 * (b & 7) + (j >> 4);
    const int i16 = j & 15, dq = (j >> 2) & 3, mq = j & 3;
    const int l15 = lane & 15, q4 = lane >> 4;
    STAMP_DECL

    if (wv < 2) {
        const int pp = wv;
        const unsigned r = lane >> 3, pc = lane & 7, s0 = (r >> 1) & 1;
        const unsigned ce = (pc ^ (s0 << 1)) << 4, co = (pc ^ ((s0 | 2) << 1)) << 4;
#define VOFFS(st) (r * (st) + ce), ((r + 8) * (st) + co - 1024), ((r + 16) * (st) + ce - 2048), ((r + 24) * (st) + co - 3072)
        unsigned lds = L_RING + pp * (NSLOT * 4096);
        const unsigned lds_lo = lds, lds_hi = lds + NSLOT * 4096;
        unsigned n = 0;
        volatile LAS unsigned* prog = (volatile LAS unsigned*)(L + L_CTL) + C_PROG0 + pp;
#define STEP() do { lds += 4096; if (lds == lds_hi) lds = lds_lo; ++n; asm volatile("s_waitcnt vmcnt(%0)" :: "i"(WIN) : "memory"); \
                    if (n > WIN / 4) *prog = n - WIN / 4; CBAR(); LSTAMP(n); } while (0)
        {
            if (pp == 0) {
                asm volatile("s_mov_b32 m0, %3\n\ts_nop 3\n\tbuffer_load_dwordx4 %0, %1, 0 offen lds\n\t"
                             "s_mov_b32 m0, %4\n\ts_mov_b64 exec, 1\n\tbuffer_load_dwordx4 %0, %2, 0 offen lds\n\ts_mov_b64 exec, -1"
                             :: "v"(lane * 16), "s"(mk_rsrc(p.x, 896)), "s"(mk_rsrc(p.ws, 16)), "s"(L_X), "s"(L_E) : "memory");
            }
            const rsrc_t rq = mk_rsrc(p.wq, 448 * 8192);
            const unsigned row0 = mq < 2 ? 128 * mq : 64 + 96 * mq;
            const unsigned so = (row0 + 32 * pp) * 8192 + (h * 256 + dq * 64) * 2;
            dma_tile<NT_W>(rq, so, lds, VOFFS(8192)); lds += 4096;
            dma_tile<NT_W>(mk_rsrc(p.wq, (pp == 1 && mq >= 2) ? 0 : 448 * 8192), so + 64 * 8192, lds, VOFFS(8192)); lds += 4096;
            const rsrc_t rk = mk_rsrc(p.wuk, 1024 * 8192);
            const unsigned sk = (64 * i16 + 32 * pp) * 8192 + h * 512;
            dma_tile<NT_W>(rk, sk, lds, VOFFS(8192)); lds += 4096;
            n = 3;
            __builtin_amdgcn_s_barrier();
            if (pp == 0) { asm volatile("s_waitcnt vmcnt(12)" ::: "memory"); ((volatile LAS unsigned*)(L + L_CTL))[C_X] = 1u; CBAR(); }
#pragma unroll 1
            for (int cb = 1; cb < 4; ++cb) { dma_tile<NT_W>(rk, sk + cb * 128, lds, VOFFS(8192)); STEP(); }
        }
        STAMP(1);
        {
#pragma unroll 1
            for (int t = 0; t < 16; ++t) {
                const unsigned rb = pp ? ((i16 - t) & 15) : i16, cb = pp ? i16 : ((i16 + t) & 15);
                const unsigned so = (512 * h + 32 * rb) * 2048 + cb * 128;
                const rsrc_t rc = mk_rsrc(p.ckv, (pp == 1 && t == 0) ? 0 : 8192 * 2048);
                if (t == 11) wait_ge_sleep(L, C_RELQ, 4);
                dma_tile<NT_C>(rc, so, lds, VOFFS(2048)); STEP();
            }
        }
        STAMP(2);
        {
            const rsrc_t r1 = mk_rsrc(p.w1, b < 196 ? 448 * 3584 : 0);
            dma_tile<NT_W>(r1, b < 196 ? (64 * (b % 7) + 32 * pp) * 3584 + (b / 7) * 128 : 0, lds, VOFFS(3584)); STEP();
            const rsrc_t rv = mk_rsrc(p.wuv, 1024 * 8192);
            const unsigned sv = (256 * mq + 32 * pp) * 8192 + (h * 256 + dq * 64) * 2;
            wait_ge_sleep(L, C_RELA0, 2);
#pragma unroll 1
            for (int k = 0; k < 4; ++k) { dma_tile<NT_W>(rv, sv + k * (64 * 8192), lds, VOFFS(8192)); STEP(); }
            STAMP(3);
            const int bb = b - 60;
            const rsrc_t r2 = mk_rsrc(p.w2, bb >= 0 ? 1792 * 896 : 0);
            wait_ge_sleep(L, C_RELA1, 2);
            dma_tile<NT_W>(r2, bb >= 0 ? (64 * (bb / 7) + 32 * pp) * 896 + (bb % 7) * 128 : 0, lds, VOFFS(896)); STEP();
            const rsrc_t ro = mk_rsrc(p.wo, 4096 * 896);
            const unsigned sw = (h * 256 + dq * 64 + 32 * pp) * 896;
#pragma unroll 1
            for (int k = 0; k < 7; ++k) {
                int cb = k + 2 * mq; if (cb >= 7) cb -= 7;
                if (k == 3) wait_ge_sleep(L, C_RELA2, 2);
                dma_tile<NT_O>(ro, sw + cb * 128, lds, VOFFS(896)); STEP();
            }
        }
        STAMP(4);
        if (WIN > 12) { asm volatile("s_waitcnt vmcnt(12)" ::: "memory"); *prog = 32; }
        asm volatile("s_waitcnt vmcnt(8)" ::: "memory"); *prog = 33;
        asm volatile("s_waitcnt vmcnt(4)" ::: "memory"); *prog = 34;
        asm volatile("s_waitcnt vmcnt(0)" ::: "memory"); *prog = 35;
        STAMP_OUT();
        return;
    }

    const rsrc_t ws = mk_rsrc(p.ws, WS_END);
    const v4u S4 = {SENT, SENT, SENT, SENT};
    const v2u S2 = {SENT, SENT};
    const int swr = ((l15 >> 1) & 1) | (((l15 >> 3) & 1) << 1);
    const int row_a0 = l15 * 128 + ((q4 ^ (swr << 1)) << 4), row_a1 = row_a0 ^ 64;
    const int swt = ((l15 >> 3) & 1) | ((q4 & 1) << 1);
    const int tr_b = (8 * q4 + (l15 >> 2)) * 128 + (((l15 >> 1) & 1) << 4) + 8 * (l15 & 1);
#define TR_A(nt) (tr_b + ((((nt) ^ swt)) << 5))
#define SLOT(pp, n) (L_RING + (pp) * (NSLOT * 4096) + ((n) % NSLOT) * 4096)

    if (wv < 6) {
        const int t = wv - 2;
        const int o_h0 = WS_H0 + ((h * 4 + dq) * 4 + mq) * 512 + (16 * t + 4 * q4 + 2 * l15) * 8;
        const int o_h1 = WS_H1 + (i16 * 16 + h) * 256 + (4 * t + q4) * 16;
        if (l15 < 2) st16(ws, o_h0, v4u{0u, 0u, 0u, 0u});
        if (l15 == 0) st16(ws, o_h1, v4u{0u, 0u, 0u, 0u});
        __builtin_amdgcn_s_barrier();
        wait_ge(L, C_X, 1);
        const v2u ep = *(LAS v2u*)(L + L_E);
        layernorm(lds16(L, L_X + lane * 16), L, lane);
        const unsigned tagA = ep.x + 1u, tagB = ep.y | 1u;
        const int tra = TR_A(t);
        const int row0 = mq < 2 ? 128 * mq : 64 + 96 * mq;
        v4u bx[4];
#pragma unroll
        for (int s = 0; s < 4; ++s) bx[s] = lds16(L, L_XN + (row0 + 32 * s + 8 * q4) * 2);
        STAMP(1);
        wait_ge(L, C_PROG0, 2); wait_ge(L, C_PROG1, 2);
        STAMP(2);
        f32x4 acc = {0.f, 0.f, 0.f, 0.f};
#pragma unroll
        for (int s = 0; s < 4; ++s) acc = mfma(lds_tr(L, SLOT(s & 1, s >> 1) + tra), bx[s], acc);
        if (l15 < 2) st16(ws, o_h0, v4u{f2u(l15 ? acc.z : acc.x), tagA, f2u(l15 ? acc.w : acc.y), tagB});
        STAMP(3);
        __builtin_amdgcn_s_sleep(DLY0);
        CBAR();
        v4u g0[2];
        const int o_g0 = WS_H0 + (h * 4 + t) * 2048 + lane * 16;
        g0[0] = ld16(ws, o_g0); g0[1] = ld16(ws, o_g0 + 1024);
        v4u ak[8];
        wait_ge(L, C_PROG0 + (t >> 1), 6);
        {
            const int base = L_RING + (t >> 1) * (NSLOT * 4096) + (t & 1) * 2048;
#pragma unroll
            for (int cb = 0; cb < 4; ++cb) { ak[2 * cb] = lds16(L, base + (2 + cb) * 4096 + row_a0); ak[2 * cb + 1] = lds16(L, base + (2 + cb) * 4096 + row_a1); }
        }
        ctl_add(L, C_RELQ, lane);
        STAMP(4);
#pragma unroll 1
        for (unsigned s = 0; s < SPIN_G; ++s) { PASSES(0, s);
            if (!any_nz((g0[0].y ^ tagA) | (g0[0].w ^ tagB) | (g0[1].y ^ tagA) | (g0[1].w ^ tagB))) break;
            __builtin_amdgcn_s_sleep(RETRY);
            CBAR();
            g0[0] = ld16(ws, o_g0); g0[1] = ld16(ws, o_g0 + 1024);
        }
        {
            const float qa = u2f(g0[0].x) + u2f(g0[1].x), qb = u2f(g0[0].z) + u2f(g0[1].z);
            auto ra = __builtin_amdgcn_permlane32_swap(f2u(qa), f2u(qa), false, false);
            auto rb = __builtin_amdgcn_permlane32_swap(f2u(qb), f2u(qb), false, false);
            *(LAS unsigned*)(L + L_Q + t * 128 + (lane & 31) * 4) = pk_bf16(u2f(ra[0]) + u2f(ra[1]), u2f(rb[0]) + u2f(rb[1]));
            ctl_add(L, C_QX, lane);
        }
        STAMP(5);
        wait_ge(L, C_FILL, 9);
        wait_ge(L, C_QX, 4);
        STAMP(6);
        {
            acc = f32x4{0.f, 0.f, 0.f, 0.f};
#pragma unroll
            for (int s = 0; s < 8; ++s) acc = mfma(ak[s], lds16(L, L_Q + (32 * s + 8 * q4) * 2), acc);
            const float sc = 0.0625f * 1.4426950408889634f;
            if (l15 == 0) st16(ws, o_h1, v4u{pk_bf16(acc.x * sc, acc.y * sc), tagA, pk_bf16(acc.z * sc, acc.w * sc), tagB});
            ctl_add(L, C_S2, lane);
        }
        STAMP(7);
        wait_ge_sleep(L, C_PROG0, 27); wait_ge_sleep(L, C_PROG1, 27);
#pragma unroll
        for (int s = 0; s < 8; ++s) ak[s] = lds_tr(L, SLOT(s & 1, 23 + (s >> 1)) + tra);
        STAMP(8);
        wait_ge_sleep(L, C_S4, 2);
        STAMP(9);
        __builtin_amdgcn_s_sleep(DLY3);
        {
            v4u g2[2]; v2u st;
            const int o_h2 = WS_H2 + (h * 4 + mq) * 8192 + t * 2048 + lane * 16;
            const int o_st = WS_H2S + (((4 * t + (lane & 3)) * 16 + 4 * mq) * 16 + h) * 8;
#pragma unroll 1
            for (unsigned s = 0; s < SPIN_G; ++s) { PASSES(1, s);
                CBAR();
                g2[0] = ld16(ws, o_h2); g2[1] = ld16(ws, o_h2 + 1024);
                st = v2u{0u, 0u};
                if (lane < 4) st = ld8(ws, o_st);
                if (!any_sent(umax(umax(st.x, st.y), umax(umax4(g2[0]), umax4(g2[1]))))) break;
                __builtin_amdgcn_s_sleep(RETRY);
            }
            const int wa = L_PART + ((lane >> 3) & 3) * 2048 + (4 * t + (lane >> 5)) * 128;
            *(LAS v4u*)(L + wa + (((lane & 7) ^ ((t >> 1) << 2)) << 4)) = g2[0];
            *(LAS v4u*)(L + wa + 256 + (((lane & 7) ^ (((t >> 1) << 2) | 2)) << 4)) = g2[1];
            if (lane < 4) *(LAS v2u*)(L + L_ST5 + (4 * t + lane) * 8) = st;
            ctl_add(L, C_G2, lane);
        }
        STAMP(10);
        wait_ge(L, C_G2, 4);
        STAMP(11);
        acc = f32x4{0.f, 0.f, 0.f, 0.f};
        const v2u st = *(LAS v2u*)(L + L_ST5 + l15 * 8);
#pragma unroll
        for (int s = 0; s < 8; ++s) acc = mfma(ak[s], lds16(L, L_PART + (s >> 1) * 2048 + ((s & 1) ? row_a1 : row_a0)), acc);
        {
            const float rg = u2f(st.x), lg = u2f(st.y);
            const float R = row_max(rg);
            const float sc = __builtin_amdgcn_exp2f(rg - R);
            const float wgt = sc / row_sum(sc * lg);
            const float c0 = row_sum(acc.x * wgt), c1 = row_sum(acc.y * wgt), c2 = row_sum(acc.z * wgt), c3 = row_sum(acc.w * wgt);
            if (l15 == 0) *(LAS v2u*)(L + L_CTX + (16 * t + 4 * q4) * 2) = v2u{pk_bf16(c0, c1), pk_bf16(c2, c3)};
        }
        ctl_add(L, C_CNT5, lane);
        STAMP_OUT();
        return;
    }

    if (wv == 6) {
        const int bb = b - 60;
        const int fs1 = b / 7, ms1 = b % 7, fs2 = bb / 7, mt2 = bb % 7;
        const int o_hid = WS_HID + (fs1 * 7 + ms1) * 256 + (16 * l15 + 4 * q4) * 4;
        const int o_gp = WS_GP + fs2 * 1792 + (64 * mt2 + 16 * l15 + 4 * q4) * 4;
        if (l15 < 4) { if (b < 196) st16(ws, o_hid, S4); if (bb >= 0) st16(ws, o_gp, S4); }
        if (lane < C_NUM) ((LAS unsigned*)(L + L_CTL))[lane] = 0u;
        LWAIT();
        __builtin_amdgcn_s_barrier();
        const int u = b - 144;
        DRAIN();
        ctl_add(L, C_FILL, lane);
        STAMP(1);
        wait_ge_sleep(L, C_RELQ, 4);
        unsigned b1v = 0; v2u b2v = {0u, 0u}, xv = {0u, 0u};
        if (bb >= 0) b1v = __builtin_nontemporal_load((const GAS u16*)p.b1 + 64 * fs2 + lane);
        if (u >= 0) { b2v = __builtin_nontemporal_load((const GAS v2u*)p.b2 + u); xv = *(LAS v2u*)(L + L_X + u * 8); }
        const v2u ep = *(LAS v2u*)(L + L_E);
        if (b < 196) {
            wait_ge_sleep(L, C_PROG0, 23); wait_ge_sleep(L, C_PROG1, 23);
            const v4u bx0 = lds16(L, L_XN + (64 * ms1 + 8 * q4) * 2), bx1 = lds16(L, L_XN + (64 * ms1 + 32 + 8 * q4) * 2);
            f32x4 a[4];
#pragma unroll
            for (int nt = 0; nt < 4; ++nt) {
                a[nt] = mfma(lds_tr(L, SLOT(0, 22) + TR_A(nt)), bx0, f32x4{0.f, 0.f, 0.f, 0.f});
                a[nt] = mfma(lds_tr(L, SLOT(1, 22) + TR_A(nt)), bx1, a[nt]);
            }
            const f32x4 o = l15 == 0 ? a[0] : l15 == 1 ? a[1] : l15 == 2 ? a[2] : a[3];
            if (l15 < 4) st16(ws, o_hid, san4(f4u(o)));
        }
        STAMP(3);
        wait_ge_sleep(L, C_CNT3, 8);
        if (bb >= 0) {
            unsigned hp[7];
            __builtin_amdgcn_s_sleep(8);
#pragma unroll 1
            for (unsigned s = 0; s < SPIN_G; ++s) { PASSES(2, s);
                CBAR();
                unsigned m = 0;
#pragma unroll
                for (int k = 0; k < 7; ++k) hp[k] = ld4(ws, WS_HID + (fs2 * 7 + k) * 256 + lane * 4);
#pragma unroll
                for (int k = 0; k < 7; ++k) m = umax(m, hp[k]);
                if (!any_sent(m)) break;
                __builtin_amdgcn_s_sleep(4);
            }
            float hs = 0.f;
#pragma unroll
            for (int k = 0; k < 7; ++k) hs += u2f(hp[k]);
            hs = rbf(rbf(hs) + bflo(b1v));
            hs = hs > 0.f ? hs : 0.f;
            *(LAS u16*)(L + L_HID + lane * 2) = (u16)pk_bf16(hs * hs, 0.f);
            CBAR();
            STAMP(4);
            wait_ge_sleep(L, C_PROG0, 28); wait_ge_sleep(L, C_PROG1, 28);
            const v4u bh0 = lds16(L, L_HID + 8 * q4 * 2), bh1 = lds16(L, L_HID + (32 + 8 * q4) * 2);
            f32x4 a[4];
#pragma unroll
            for (int nt = 0; nt < 4; ++nt) {
                a[nt] = mfma(lds_tr(L, SLOT(0, 27) + TR_A(nt)), bh0, f32x4{0.f, 0.f, 0.f, 0.f});
                a[nt] = mfma(lds_tr(L, SLOT(1, 27) + TR_A(nt)), bh1, a[nt]);
            }
            const f32x4 o = l15 == 0 ? a[0] : l15 == 1 ? a[1] : l15 == 2 ? a[2] : a[3];
            if (l15 < 4) st16(ws, o_gp, san4(f4u(o)));
        }
        STAMP(5);
        if (u >= 0) {
            v4u gv;
            const int lg = lane < 28 ? lane : 27;
            __builtin_amdgcn_s_sleep(8);
#pragma unroll 1
            for (unsigned s = 0; s < SPIN_G; ++s) { PASSES(3, s);
                CBAR();
                gv = ld16(ws, WS_GP + lg * 1792 + u * 16);
                if (!any_sent(umax4(gv))) break;
                __builtin_amdgcn_s_sleep(4);
            }
            f32x4 gf = u4f(gv);
            if (lane >= 28) gf = f32x4{0.f, 0.f, 0.f, 0.f};
            float g[4] = {wave_sum(gf.x), wave_sum(gf.y), wave_sum(gf.z), wave_sum(gf.w)};
            const float bb2[4] = {bflo(b2v.x), bfhi(b2v.x), bflo(b2v.y), bfhi(b2v.y)};
#pragma unroll
            for (int k = 0; k < 4; ++k) { const float a = rbf(rbf(g[k]) + bb2[k]); g[k] = rbf(a / (1.f + __expf(-a))); }
            STAMP(6);
            wait_ge(L, C_S6, 7);
            STAMP(7);
            __builtin_amdgcn_s_sleep(DLY4);
            v4u sv[4];
#pragma unroll 1
            for (unsigned s = 0; s < SPIN_G; ++s) { PASSES(4, s);
                CBAR();
#pragma unroll
                for (int k = 0; k < 4; ++k) sv[k] = ld16(ws, WS_H4 + (lane + 64 * k) * 1792 + u * 16);
                if (!any_sent(umax(umax(umax4(sv[0]), umax4(sv[1])), umax(umax4(sv[2]), umax4(sv[3]))))) break;
                __builtin_amdgcn_s_sleep(RETRY);
            }
            STAMP(8);
            const f32x4 sf = (u4f(sv[0]) + u4f(sv[1])) + (u4f(sv[2]) + u4f(sv[3]));
            const float s[4] = {wave_sum(sf.x), wave_sum(sf.y), wave_sum(sf.z), wave_sum(sf.w)};
            const float xx[4] = {bflo(xv.x), bfhi(xv.x), bflo(xv.y), bfhi(xv.y)};
            float o[4];
#pragma unroll
            for (int k = 0; k < 4; ++k) o[k] = xx[k] + rbf(rbf(s[k]) * g[k]);
            if (lane == 0) ((GAS v2u*)p.out)[u] = v2u{pk_bf16(o[0], o[1]), pk_bf16(o[2], o[3])};
            if (u == 0 && lane == 0) st8(ws, WS_EPOCH, v2u{ep.x + 1u, ep.y});
        }
        STAMP_OUT();
        return;
    }

    {
        const int w = wv - 7, kq = w >> 1, ch = w & 1;
        int cbm = w + 2 * mq; if (cbm >= 7) cbm -= 7;
        const int o_p = WS_P + (h * 16 + i16) * 1024 + lane * 16;
        const int o_ps = WS_PS + ((h * 16 + i16) * 16 + l15) * 8;
        const int o_h2 = WS_H2 + (l15 * 4 + (i16 >> 2)) * 8192 + h * 512 + (64 * (i16 & 3) + 32 * ch + 8 * q4) * 2;
        const int o_h2s = WS_H2S + ((h * 16 + i16) * 16 + l15) * 8;
        const int o_h4 = WS_H4 + b * 1792 + (64 * cbm + 16 * l15 + 4 * q4) * 4;
        if (w == 0) { st16(ws, o_p, S4); if (lane < 16) { st8(ws, o_ps, S2); st8(ws, o_h2s, S2); } }
        if (w < 2) st16(ws, o_h2, S4);
        if (w < 7 && l15 < 4) st16(ws, o_h4, S4);
        __builtin_amdgcn_s_barrier();
        DRAIN();
        ctl_add(L, C_FILL, lane);
        v4u ar[8], ac[8];
        const int rbase = L_RING + ((6 + 2 * w) % NSLOT) * 4096, rbase1 = L_RING + ((7 + 2 * w) % NSLOT) * 4096;
        const int tr0 = TR_A(2 * ch), tr1 = TR_A(2 * ch + 1);
#define PULL_R() do { wait_ge(L, C_PROG0, 8 + 2 * w); \
            ar[0] = lds16(L, rbase + row_a0); ar[1] = lds16(L, rbase + row_a1); ar[2] = lds16(L, rbase1 + row_a0); ar[3] = lds16(L, rbase1 + row_a1); \
            ar[4] = lds16(L, rbase + 2048 + row_a0); ar[5] = lds16(L, rbase + 2048 + row_a1); ar[6] = lds16(L, rbase1 + 2048 + row_a0); ar[7] = lds16(L, rbase1 + 2048 + row_a1); } while (0)
#define PULL_C() do { wait_ge(L, C_PROG1, 10 + 4 * kq); \
            _Pragma("unroll") for (int u = 0; u < 4; ++u) { int cbase = L_RING + NSLOT * 4096 + ((6 + 4 * kq + u) % NSLOT) * 4096; if (u == 0 && kq == 0) cbase = L_RING + 6 * 4096; \
                ac[2 * u] = lds_tr(L, cbase + tr0); ac[2 * u + 1] = lds_tr(L, cbase + tr1); } \
            LWAIT(); ctl_add(L, C_RELA0 + kq, lane); } while (0)
        STAMP(1);
        if (w < 4) { PULL_R(); PULL_C(); }
        STAMP(2);
        wait_ge_sleep(L, C_S2, 4);
        __builtin_amdgcn_s_sleep(DLY1);
        v4u bq[4];
        {
            const v2u ep = *(LAS v2u*)(L + L_E);
            const unsigned tagA = ep.x + 1u, tagB = ep.y | 1u;
            const int o0 = WS_H1 + ((i16 + 2 * w) & 15) * 4096 + lane * 16, o1 = WS_H1 + ((i16 + 2 * w + 1) & 15) * 4096 + lane * 16;
            v4u g[8];
#pragma unroll 1
            for (unsigned s = 0; s < SPIN_G; ++s) { PASSES(5, s);
                CBAR();
                unsigned m = 0;
#pragma unroll
                for (int k = 0; k < 4; ++k) { g[k] = ld16(ws, o0 + 1024 * k); g[4 + k] = ld16(ws, o1 + 1024 * k); }
#pragma unroll
                for (int k = 0; k < 8; ++k) m |= (g[k].y ^ tagA) | (g[k].w ^ tagB);
                if (!any_nz(m)) break;
                __builtin_amdgcn_s_sleep(RETRY);
            }
            const int pw = L_PART + w * 2048;
            const int wa0 = pw + q4 * 128 + (((l15 >> 1) ^ (q4 & 2)) << 4) + (l15 & 1) * 8, wa1 = wa0 ^ 64;
#pragma unroll
            for (int hb = 0; hb < 2; ++hb) {
#pragma unroll
                for (int k = 0; k < 4; ++k) *(LAS v2u*)(L + (k < 2 ? wa0 : wa1) + k * 512) = v2u{g[4 * hb + k].x, g[4 * hb + k].z};
                CBAR();
                bq[2 * hb] = lds16(L, pw + row_a0); bq[2 * hb + 1] = lds16(L, pw + row_a1);
                CBAR();
            }
        }
        STAMP(3);
        if (w >= 4) PULL_R();
        STAMP(4);
        {
            f32x4 s0 = {0.f, 0.f, 0.f, 0.f}, s1 = s0;
#pragma unroll
            for (int k = 0; k < 4; ++k) { s0 = mfma(ar[k], bq[k], s0); s1 = mfma(ar[4 + k], bq[k], s1); }
            *(LAS f32x4*)(L + L_PART + w * 2048 + lane * 16) = s0;
            *(LAS f32x4*)(L + L_PART + w * 2048 + 1024 + lane * 16) = s1;
            ctl_add(L, C_CNT3, lane);
        }
        STAMP(5);
        if (w >= 4) PULL_C();
        const int red_a = (q4 >> 1) * 1024 + ((q4 & 1) * 32 + l15) * 16;
        if (w == 0) {
            wait_ge(L, C_CNT3, 8);
            STAMP(6);
            f32x4 a0 = {0.f, 0.f, 0.f, 0.f}, a1 = a0;
#pragma unroll
            for (int k = 0; k < 8; ++k) { a0 += *(LAS f32x4*)(L + L_PART + k * 2048 + red_a); a1 += *(LAS f32x4*)(L + L_PART + k * 2048 + red_a + 256); }
            const float mx = xrow_max(fmaxf(fmaxf(fmaxf(a0.x, a0.y), fmaxf(a0.z, a0.w)), fmaxf(fmaxf(a1.x, a1.y), fmaxf(a1.z, a1.w))));
            const float r = __builtin_ceilf(mx);
            const float p0 = __builtin_amdgcn_exp2f(a0.x - r), p1 = __builtin_amdgcn_exp2f(a0.y - r), p2 = __builtin_amdgcn_exp2f(a0.z - r), p3 = __builtin_amdgcn_exp2f(a0.w - r);
            const float p4 = __builtin_amdgcn_exp2f(a1.x - r), p5 = __builtin_amdgcn_exp2f(a1.y - r), p6 = __builtin_amdgcn_exp2f(a1.z - r), p7 = __builtin_amdgcn_exp2f(a1.w - r);
            st16(ws, o_p, san4(v4u{pk_bf16(p0, p1), pk_bf16(p2, p3), pk_bf16(p4, p5), pk_bf16(p6, p7)}));
            const float l = xrow_sum(((p0 + p1) + (p2 + p3)) + ((p4 + p5) + (p6 + p7)));
            if (lane < 16) st8(ws, o_ps, v2u{san(f2u(r)), san(f2u(l))});
            ctl_add(L, C_PPUB, lane);
        }
        STAMP(7);
        wait_ge(L, C_PPUB, 1);
        __builtin_amdgcn_s_sleep(DLY2);
        v4u bp[4]; v2u ps[4];
        {
            int op[4], os[4];
#pragma unroll
            for (int u = 0; u < 4; ++u) { const int iu = (i16 - 4 * kq - u) & 15; op[u] = WS_P + (h * 16 + iu) * 1024 + lane * 16; os[u] = WS_PS + ((h * 16 + iu) * 16 + l15) * 8; }
#pragma unroll 1
            for (unsigned s = 0; s < SPIN_G; ++s) { PASSES(6, s);
                CBAR();
                unsigned m = 0;
#pragma unroll
                for (int u = 0; u < 4; ++u) { bp[u] = ld16(ws, op[u]); ps[u] = ld8(ws, os[u]); }
#pragma unroll
                for (int u = 0; u < 4; ++u) m = umax(m, umax(umax4(bp[u]), umax(ps[u].x, ps[u].y)));
                if (!any_sent(m)) break;
                __builtin_amdgcn_s_sleep(RETRY);
            }
        }
        STAMP(8);
        {
            const float rw = fmaxf(fmaxf(u2f(ps[0].x), u2f(ps[1].x)), fmaxf(u2f(ps[2].x), u2f(ps[3].x)));
            f32x4 c0 = {0.f, 0.f, 0.f, 0.f}, c1 = c0;
            float lw = 0.f;
#pragma unroll
            for (int u = 0; u < 4; ++u) {
                const float dl = rw - u2f(ps[u].x);
                const unsigned dd = (unsigned)fminf(dl, 255.f) * 0x00800080u;
                const u16x2 d2 = __builtin_bit_cast(u16x2, dd);
                const unsigned x0 = bp[u].x, x1 = bp[u].y, x2 = bp[u].z, x3 = bp[u].w;
                const v4u bs = {__builtin_bit_cast(unsigned, __builtin_elementwise_sub_sat(__builtin_bit_cast(u16x2, x0), d2)),
                                __builtin_bit_cast(unsigned, __builtin_elementwise_sub_sat(__builtin_bit_cast(u16x2, x1), d2)),
                                __builtin_bit_cast(unsigned, __builtin_elementwise_sub_sat(__builtin_bit_cast(u16x2, x2), d2)),
                                __builtin_bit_cast(unsigned, __builtin_elementwise_sub_sat(__builtin_bit_cast(u16x2, x3), d2))};
                c0 = mfma(ac[2 * u], bs, c0); c1 = mfma(ac[2 * u + 1], bs, c1);
                lw += __builtin_amdgcn_exp2f(-dl) * u2f(ps[u].y);
            }
            *(LAS f32x4*)(L + L_PART + w * 2048 + lane * 16) = c0;
            *(LAS f32x4*)(L + L_PART + w * 2048 + 1024 + lane * 16) = c1;
            if (lane < 16) *(LAS f32x2*)(L + L_ST4 + ((ch * 4 + kq) * 16 + l15) * 8) = f32x2{rw, lw};
            ctl_add(L, C_CNT4A + ch, lane);
        }
        STAMP(9);
        if (w < 2) {
            wait_ge(L, C_CNT4A + ch, 4);
            STAMP(10);
            f32x4 v0[4], v1[4]; f32x2 sx[4];
#pragma unroll
            for (int k = 0; k < 4; ++k) {
                v0[k] = *(LAS f32x4*)(L + L_PART + (2 * k + ch) * 2048 + red_a); v1[k] = *(LAS f32x4*)(L + L_PART + (2 * k + ch) * 2048 + red_a + 256);
                sx[k] = *(LAS f32x2*)(L + L_ST4 + ((ch * 4 + k) * 16 + l15) * 8);
            }
            const float R = fmaxf(fmaxf(sx[0].x, sx[1].x), fmaxf(sx[2].x, sx[3].x));
            f32x4 a0 = {0.f, 0.f, 0.f, 0.f}, a1 = a0; float lg = 0.f;
#pragma unroll
            for (int k = 0; k < 4; ++k) { const float sc = __builtin_amdgcn_exp2f(sx[k].x - R); a0 += v0[k] * sc; a1 += v1[k] * sc; lg += sx[k].y * sc; }
            st16(ws, o_h2, san4(v4u{pk_bf16(a0.x, a0.y), pk_bf16(a0.z, a0.w), pk_bf16(a1.x, a1.y), pk_bf16(a1.z, a1.w)}));
            if (w == 0 && lane < 16) st8(ws, o_h2s, v2u{san(f2u(R)), san(f2u(lg))});
            ctl_add(L, C_S4, lane);
        }
        STAMP(11);
        if (w < 7) {
            wait_ge_sleep(L, C_PROG0, 29 + w); wait_ge_sleep(L, C_PROG1, 29 + w);
            const int ob0 = L_RING + ((28 + w) % NSLOT) * 4096, ob1 = ob0 + NSLOT * 4096;
#pragma unroll
            for (int nt = 0; nt < 4; ++nt) { ar[nt] = lds_tr(L, ob0 + TR_A(nt)); ar[4 + nt] = lds_tr(L, ob1 + TR_A(nt)); }
            STAMP(12);
            wait_ge(L, C_CNT5, 4);
            STAMP(13);
            const v4u bc0 = lds16(L, L_CTX + 8 * q4 * 2), bc1 = lds16(L, L_CTX + (32 + 8 * q4) * 2);
            f32x4 a[4];
#pragma unroll
            for (int nt = 0; nt < 4; ++nt) { a[nt] = mfma(ar[nt], bc0, f32x4{0.f, 0.f, 0.f, 0.f}); a[nt] = mfma(ar[4 + nt], bc1, a[nt]); }
            const f32x4 o = l15 == 0 ? a[0] : l15 == 1 ? a[1] : l15 == 2 ? a[2] : a[3];
            if (l15 < 4) st16(ws, o_h4, san4(f4u(o)));
            ctl_add(L, C_S6, lane);
        }
        STAMP_OUT();
    }
}

__global__ void __launch_bounds__(1024) null_k(unsigned* p) { extern __shared__ unsigned char sm_[]; if (p == nullptr) sm_[threadIdx.x] = 1; }

extern "C" void kernel_launch(void* const* d_in, const int* in_sizes, int n_in,
                              void* d_out, int out_size, void* d_ws, size_t ws_size,
                              hipStream_t stream) {
    Params p;
    p.x = d_in[0]; p.ckv = d_in[1]; p.wq = d_in[2]; p.wuk = d_in[3]; p.wuv = d_in[4]; p.wo = d_in[5];
    p.w1 = d_in[7]; p.b1 = d_in[8]; p.w2 = d_in[9]; p.b2 = d_in[10]; p.out = d_out; p.ws = d_ws;
    (void)hipFuncSetAttribute((const void*)fused_decode, hipFuncAttributeMaxDynamicSharedMemorySize, L_END);
    fused_decode<<<256, NWAVE * 64, L_END, stream>>>(p);
    null_k<<<1, 64, 0, stream>>>((unsigned*)d_ws);
}
```

```cpp
#include <hip/hip_runtime.h>
#include <stdint.h>

typedef unsigned v4u __attribute__((ext_vector_type(4)));
typedef unsigned v2u __attribute__((ext_vector_type(2)));
typedef float f32x4 __attribute__((ext_vector_type(4)));
typedef float f32x2 __attribute__((ext_vector_type(2)));
typedef __bf16 bf16x8 __attribute__((ext_vector_type(8)));
typedef __bf16 bf16x2 __attribute__((ext_vector_type(2)));
typedef short s16x4 __attribute__((ext_vector_type(4)));
typedef unsigned short u16x2 __attribute__((ext_vector_type(2)));
typedef unsigned short u16;
#define LAS __attribute__((address_space(3)))
#define GAS __attribute__((address_space(1)))
typedef __amdgpu_buffer_rsrc_t rsrc_t;

#define LATE 1
#define EARLY 1
#define NT_W 1
#define WSOFF 0
#define DLY0 16
#define DLYF 40
#define FSTART 7u
#define DLY2 0
#define DLY3 40
#define DLY3V 0
#define DLY4 50
#define RETRY 1
#define RETRY1 4
#define WF 4
#define WS 0
#define NSLOW 3
#define G1START 8
#define PRIO 2
#define HIDSTART 24u
#define RGATE 8
#define EGATE 1
#define WV 2
#ifndef NV
#define NV 20
#endif
#define HSHIFT 2

constexpr int WS_EPOCH = 0;
constexpr int WS_FLAG = 4096;
constexpr int WS_H0 = WS_FLAG + 16384;
constexpr int WS_H1 = WS_H0 + 65536;
constexpr int WS_P = WS_H1 + 32768;
constexpr int WS_PS = WS_P + 262144;
constexpr int WS_H2 = WS_PS + 32768;
constexpr int WS_H2S = WS_H2 + 524288;
constexpr int WS_H4 = WS_H2S + 4096;
constexpr int WS_HID = WS_H4 + 28 * 8192;
constexpr int WS_GP = WS_HID + 98 * 512;
constexpr int WS_END = WS_GP + 14 * 1792;
constexpr int WS_DIAG = 4 << 20;
static_assert(WS_END - WS_H1 <= 128 * 9 * 1024, "nine 1 KB pieces per filling workgroup");

constexpr int L_PART = 34 * 4096;
constexpr int L_XN = L_PART + 16384;
constexpr int L_Q = L_XN + 1024;
constexpr int L_CTX = L_Q + 512;
constexpr int L_HID = L_CTX + 128;
constexpr int L_ST4 = L_HID + 256;
constexpr int L_ST5 = L_ST4 + 512;
constexpr int L_X = L_ST5 + 128;
constexpr int L_E = L_X + 1024;
constexpr int L_CTL = L_E + 16;
constexpr int L_END = L_CTL + 512;
constexpr int L_S2P = L_PART;
enum { C_PROG = 0  , C_X = 2, C_FLOK = 3, C_PPUB = 4  , C_FILL = 8  , C_QX = 20  , C_QH = 24  , C_RELQ = 28  ,
       C_RELR = 32  , C_RELC = 36  , C_P3 = 40  , C_P4 = 48  , C_CG = 56  , C_G2 = 64  , C_CTX = 72  , C_S4 = 76  , C_NUM = 80 };

constexpr int NWAVE = 15;
constexpr unsigned SENT = 0xFFFFFFFFu;
constexpr int AUX_SC1 = 16;
constexpr unsigned SPIN_G = 1u << 15;

struct Params {
    const void* x; const void* ckv; const void* wq; const void* wuk; const void* wuv; const void* wo;
    const void* w1; const void* b1; const void* w2; const void* b2; void* out; void* ws;
};

__device__ __forceinline__ float u2f(unsigned u) { return __builtin_bit_cast(float, u); }
__device__ __forceinline__ unsigned f2u(float f) { return __builtin_bit_cast(unsigned, f); }
__device__ __forceinline__ unsigned pk_bf16(float a, float b) { f32x2 v = {a, b}; bf16x2 r = __builtin_convertvector(v, bf16x2); return __builtin_bit_cast(unsigned, r); }
__device__ __forceinline__ float rbf(float a) { return u2f(pk_bf16(a, a) << 16); }
__device__ __forceinline__ float bflo(unsigned u) { return u2f(u << 16); }
__device__ __forceinline__ float bfhi(unsigned u) { return u2f(u & 0xFFFF0000u); }
__device__ __forceinline__ unsigned san(unsigned u) { return u < 0xFFFFFFFEu ? u : 0xFFFFFFFEu; }
__device__ __forceinline__ v4u san4(v4u v) { return v4u{san(v.x), san(v.y), san(v.z), san(v.w)}; }
__device__ __forceinline__ v4u f4u(f32x4 v) { return __builtin_bit_cast(v4u, v); }
__device__ __forceinline__ f32x4 u4f(v4u v) { return __builtin_bit_cast(f32x4, v); }
__device__ __forceinline__ unsigned umax(unsigned a, unsigned b) { return a > b ? a : b; }
__device__ __forceinline__ unsigned umax4(v4u v) { return umax(umax(v.x, v.y), umax(v.z, v.w)); }
__device__ __forceinline__ float fmx(float a, float b) { return __builtin_amdgcn_fmed3f(a, b, __builtin_inff()); }
__device__ __forceinline__ f32x4 mfma(v4u a, v4u b, f32x4 c) {
    return __builtin_amdgcn_mfma_f32_16x16x32_bf16(__builtin_bit_cast(bf16x8, a), __builtin_bit_cast(bf16x8, b), c, 0, 0, 0);
}
template <int N> __device__ __forceinline__ float ror(float v) { return u2f((unsigned)__builtin_amdgcn_update_dpp(0, (int)f2u(v), 0x120 + N, 0xf, 0xf, false)); }
__device__ __forceinline__ float row_sum(float v) { v += ror<8>(v); v += ror<4>(v); v += ror<2>(v); v += ror<1>(v); return v; }
__device__ __forceinline__ float row_max(float v) { v = fmx(v, ror<8>(v)); v = fmx(v, ror<4>(v)); v = fmx(v, ror<2>(v)); v = fmx(v, ror<1>(v)); return v; }
__device__ __forceinline__ float xrow_sum(float v) {
    auto a = __builtin_amdgcn_permlane32_swap(f2u(v), f2u(v), false, false); v = u2f(a[0]) + u2f(a[1]);
    auto b = __builtin_amdgcn_permlane16_swap(f2u(v), f2u(v), false, false); return u2f(b[0]) + u2f(b[1]);
}
__device__ __forceinline__ float xrow_max(float v) {
    auto a = __builtin_amdgcn_permlane32_swap(f2u(v), f2u(v), false, false); v = fmx(u2f(a[0]), u2f(a[1]));
    auto b = __builtin_amdgcn_permlane16_swap(f2u(v), f2u(v), false, false); return fmx(u2f(b[0]), u2f(b[1]));
}
__device__ __forceinline__ float wave_sum(float v) { return xrow_sum(row_sum(v)); }
__device__ __forceinline__ float wave_sum4(f32x4 v) {
    auto a = __builtin_amdgcn_permlane32_swap(f2u(v.x), f2u(v.y), false, false); const float xy = u2f(a[0]) + u2f(a[1]);
    auto b = __builtin_amdgcn_permlane32_swap(f2u(v.z), f2u(v.w), false, false); const float zw = u2f(b[0]) + u2f(b[1]);
    auto c = __builtin_amdgcn_permlane16_swap(f2u(xy), f2u(zw), false, false);
    return row_sum(u2f(c[0]) + u2f(c[1]));
}

__device__ __forceinline__ float sel4(int q, const f32x4 (&a)[4]) { const float x0 = a[0].x, x1 = a[1].x, x2 = a[2].x, x3 = a[3].x; return q == 0 ? x0 : q == 1 ? x1 : q == 2 ? x2 : x3; }
__device__ __forceinline__ float quad_sum4(f32x4 v) {
    auto a = __builtin_amdgcn_permlane32_swap(f2u(v.x), f2u(v.y), false, false); const float xy = u2f(a[0]) + u2f(a[1]);
    auto b = __builtin_amdgcn_permlane32_swap(f2u(v.z), f2u(v.w), false, false); const float zw = u2f(b[0]) + u2f(b[1]);
    auto c = __builtin_amdgcn_permlane16_swap(f2u(xy), f2u(zw), false, false);
    float r = u2f(c[0]) + u2f(c[1]); r += ror<8>(r); r += ror<4>(r); return r;
}
__device__ __forceinline__ rsrc_t mk_rsrc(const void* p, int bytes) { return __builtin_amdgcn_make_buffer_rsrc((void*)p, 0, bytes, 0x00020000); }
__device__ __forceinline__ v4u ld16(rsrc_t r, int off) { return __builtin_amdgcn_raw_buffer_load_b128(r, off, 0, AUX_SC1); }
__device__ __forceinline__ v2u ld8(rsrc_t r, int off) { return __builtin_amdgcn_raw_buffer_load_b64(r, off, 0, AUX_SC1); }
__device__ __forceinline__ unsigned ld4(rsrc_t r, int off) { return __builtin_amdgcn_raw_buffer_load_b32(r, off, 0, AUX_SC1); }
__device__ __forceinline__ void st16(rsrc_t r, int off, v4u v) { __builtin_amdgcn_raw_buffer_store_b128(v, r, off, 0, AUX_SC1); }
__device__ __forceinline__ void st4(rsrc_t r, int off, unsigned v) { __builtin_amdgcn_raw_buffer_store_b32(v, r, off, 0, AUX_SC1); }
__device__ __forceinline__ void st8(rsrc_t r, int off, v2u v) { __builtin_amdgcn_raw_buffer_store_b64(v, r, off, 0, AUX_SC1); }
constexpr int OOB = 0x40000000;
#define CBAR() asm volatile("" ::: "memory")
#define SB() __builtin_amdgcn_sched_barrier(0)
#define HI() __builtin_amdgcn_s_setprio(3)
#define PIN(x) asm volatile("" : "+v"(x))
#define DRAIN() asm volatile("s_waitcnt vmcnt(0)" ::: "memory")
#define LWAIT() asm volatile("s_waitcnt lgkmcnt(0)" ::: "memory")

typedef LAS unsigned char* lds_t;
__device__ __forceinline__ v4u lds16(int off) { return *(LAS v4u*)off; }
__device__ __forceinline__ f32x4 lds16f(int off) { return *(LAS f32x4*)off; }
template <int D2> __device__ __forceinline__ v4u lds_tr(int off) {
    s16x4 a = __builtin_amdgcn_ds_read_tr16_b64_v4i16((LAS s16x4*)off);
    s16x4 b = __builtin_amdgcn_ds_read_tr16_b64_v4i16((LAS s16x4*)(off + D2));
    v2u a2 = __builtin_bit_cast(v2u, a), b2 = __builtin_bit_cast(v2u, b);
    return v4u{a2.x, a2.y, b2.x, b2.y};
}
#define MINSLEEP 0
template <int SLEEP0> __device__ __forceinline__ void wait_lds(int addr, unsigned want) {
    constexpr int SLEEP = SLEEP0 > MINSLEEP ? SLEEP0 : MINSLEEP;
    unsigned v, c;
    if (SLEEP)
        asm volatile("s_mov_b32 %1, 0x40000\n1:\n\tds_read_b32 %0, %2\n\ts_sub_u32 %1, %1, 1\n\ts_waitcnt lgkmcnt(0)\n\tv_cmp_gt_u32 vcc, %3, %0\n\t"
                     "s_cbranch_scc1 2f\n\ts_cbranch_vccz 2f\n\ts_sleep %4\n\ts_branch 1b\n2:"
                     : "=&v"(v), "=&s"(c) : "v"(addr), "v"(want), "i"(SLEEP) : "memory", "vcc", "scc");
    else
        asm volatile("s_mov_b32 %1, 0x80000\n1:\n\tds_read_b32 %0, %2\n\ts_sub_u32 %1, %1, 1\n\ts_waitcnt lgkmcnt(0)\n\tv_cmp_gt_u32 vcc, %3, %0\n\t"
                     "s_cbranch_scc1 2f\n\ts_cbranch_vccnz 1b\n2:"
                     : "=&v"(v), "=&s"(c) : "v"(addr), "v"(want) : "memory", "vcc", "scc");
}
#define CW(word) (L_CTL + 4 * (word))
#define WAITP(S, word, want) wait_lds<S>(CW(word), (want))
#define WAITF(S, word, n) wait_lds<S>(CW(word) + 4 * (lane & ((n) - 1)), 1u)
__device__ __forceinline__ void sig(int addr, unsigned v) { asm volatile("ds_write_b32 %0, %1" :: "v"(addr), "v"(v) : "memory"); }
__device__ __forceinline__ bool any_sent(unsigned m) { return __builtin_amdgcn_ballot_w64(m == SENT) != 0ull; }
__device__ __forceinline__ bool any_nz(unsigned m) { return __builtin_amdgcn_ballot_w64(m != 0u) != 0ull; }

#define STAMP(k)
#define STAMP_DECL
#define LSTAMP(n)
#define PASSES(k, s)
#define STAMP_OUT()

__device__ __forceinline__ void wait_vm(int n) {
    switch (n) {
#define VMC(x) case x: asm volatile("s_waitcnt vmcnt(" #x ")" ::: "memory"); break;
        VMC(0) VMC(4) VMC(8) VMC(12) VMC(16) VMC(20) VMC(24) VMC(28) VMC(32) VMC(36) VMC(40)
    }
}
__device__ __forceinline__ void dma_tile(rsrc_t r, unsigned soff, unsigned lds, unsigned v0, unsigned v1, unsigned v2, unsigned v3) {
#define POL " nt"
    asm volatile("s_mov_b32 m0, %6\n\ts_nop 0\n\t"
                 "buffer_load_dwordx4 %0, %4, %5 offen" POL " lds\n\t"
                 "buffer_load_dwordx4 %1, %4, %5 offen offset:1024" POL " lds\n\t"
                 "buffer_load_dwordx4 %2, %4, %5 offen offset:2048" POL " lds\n\t"
                 "buffer_load_dwordx4 %3, %4, %5 offen offset:3072" POL " lds"
                 :: "v"(v0), "v"(v1), "v"(v2), "v"(v3), "s"(r), "s"(soff), "s"(lds) : "memory");
}

template <int W>
__device__ __forceinline__ void dma_seg(rsrc_t r, int& n, int end, unsigned ts, unsigned tl, int& vp, int prog, unsigned v0, unsigned v1, unsigned v2, unsigned v3) {
    unsigned so, sl; int t;
    asm volatile("s_nop 1\n\tv_readlane_b32 %[so], %[ts], %[n]\n\tv_readlane_b32 %[sl], %[tl], %[n]\n\ts_nop 3\n"
                 "1:\n\ts_mov_b32 m0, %[sl]\n\ts_add_u32 %[n], %[n], 1\n\t"
                 "buffer_load_dwordx4 %[v0], %[r], %[so] offen" POL " lds\n\t"
                 "buffer_load_dwordx4 %[v1], %[r], %[so] offen offset:1024" POL " lds\n\t"
                 "buffer_load_dwordx4 %[v2], %[r], %[so] offen offset:2048" POL " lds\n\t"
                 "buffer_load_dwordx4 %[v3], %[r], %[so] offen offset:3072" POL " lds\n\t"
                 "v_readlane_b32 %[sl], %[tl], %[n]\n\tv_readlane_b32 %[so], %[ts], %[n]\n\t"
                 "s_sub_u32 %[t], %[n], %[w]\n\tv_max_i32 %[vp], %[t], %[vp]\n\t"
                 "s_waitcnt vmcnt(%[w4])\n\tds_write_b32 %[pa], %[vp]\n\t"
                 "s_cmp_lt_i32 %[n], %[e]\n\ts_cbranch_scc1 1b"
                 : [n] "+s"(n), [vp] "+v"(vp), [so] "=&s"(so), [sl] "=&s"(sl), [t] "=&s"(t)
                 : [r] "s"(r), [e] "s"(end), [ts] "v"(ts), [tl] "v"(tl), [pa] "v"(prog), [v0] "v"(v0), [v1] "v"(v1), [v2] "v"(v2), [v3] "v"(v3), [w] "i"(W), [w4] "i"(4 * W)
                 : "memory", "scc");
}

__global__ void __launch_bounds__(NWAVE * 64, 4) fused_decode(Params p) {
    extern __shared__ __attribute__((aligned(16))) unsigned char smem[];
    const int lane = threadIdx.x & 63, wv = __builtin_amdgcn_readfirstlane(threadIdx.x >> 6);
    const int b = blockIdx.x, jj = (b >> 3) & 15;
    const bool ecu = b < 128;
    const int hh = 2 * (b & 7) + (jj >> 3);
    const int i8 = jj & 7, dq = i8 >> 1, hf = i8 & 1;
    const int e = (b & 7) * 16 + jj;
    const int l15 = lane & 15, q4 = lane >> 4;
#define CSLOT(mm, pq) (((mm) < 12 ? 2 * (mm) + (pq) : (mm) == 12 ? 32 + (pq) : 2 * (mm) - 26 + (pq)) * 4096)
#define RHO(mm) (2 * ((i8 + 1 + (mm)) & 7) + 1 - ((mm) >> 3))
    STAMP_DECL

    if (wv < 2) {
        const int pp = wv;
        const unsigned r = lane >> 3, pc = lane & 7, s0 = (r >> 1) & 1;
        const unsigned ce = (pc ^ (s0 << 1)) << 4, co = (pc ^ ((s0 | 2) << 1)) << 4;
#define VOFFS(st) (r * (st) + ce), ((r + 8) * (st) + co - 1024), ((r + 16) * (st) + ce - 2048), ((r + 24) * (st) + co - 3072)
        int n = 0, vp = 0;
        const int prog = CW(C_PROG + pp);
        __builtin_amdgcn_s_setprio(1);
#define DOWN(W) do { wait_vm(4 * (W)); vp = vp > n - (W) ? vp : n - (W); sig(prog, vp); } while (0)
#define BLOCK(addr) do { const int a_ = (addr); _Pragma("unroll") for (int x_ = WF - 1; x_ >= 0; --x_) { if (__builtin_amdgcn_ballot_w64(*(volatile LAS unsigned*)a_ == 0u) == 0ull) break; DOWN(x_); } wait_lds<0>(a_, 1u); } while (0)
#define DRAINW(W, T) do { _Pragma("unroll") for (int x_ = (W) - 1; x_ >= (T); --x_) DOWN(x_); } while (0)
        if (pp == 0)
            asm volatile("s_mov_b32 m0, %3\n\ts_nop 0\n\tbuffer_load_dwordx4 %0, %1, 0 offen lds\n\t"
                         "s_mov_b32 m0, %4\n\ts_mov_b64 exec, 1\n\tbuffer_load_dwordx4 %0, %2, 0 offen lds\n\ts_mov_b64 exec, -1"
                         :: "v"(lane * 16), "s"(mk_rsrc(p.x, ecu ? 896 : 0)), "s"(mk_rsrc(p.ws, 16)), "s"(L_X), "s"(L_E) : "memory");
        if (ecu) {
            const rsrc_t rq = mk_rsrc(p.wq, 448 * 8192);
            const unsigned so = (224 * hf + 32 * pp) * 8192 + (((hh + HSHIFT) & 15) * 256 + dq * 64) * 2;
            dma_tile(rq, so, pp * 4096, VOFFS(8192));
            dma_tile(rq, so + 64 * 8192, (2 + pp) * 4096, VOFFS(8192));
            dma_tile(rq, so + 128 * 8192, (4 + pp) * 4096, VOFFS(8192));
            dma_tile(mk_rsrc(p.wq, pp ? 0 : 448 * 8192), so + 192 * 8192, (6 + pp) * 4096, VOFFS(8192));
            const int e2 = e - 30;
            const int k = lane - 4, mm = lane - 16;
            unsigned ts = (128 * i8 + 32 * pp + (k & 1) * 64) * 8192 + hh * 512 + (((k >> 1) + i8) & 3) * 128, tl = (2 * lane + pp) * 4096;
            if (lane >= 12) ts = e < 98 ? (64 * (e % 7) + 32 * pp) * 3584 + (e / 7) * 256 + (lane & 1) * 128 : 0;
            if (lane >= 14) ts = e2 >= 0 ? (128 * (e2 / 7) + 32 * pp + (lane & 1) * 64) * 896 + (e2 % 7) * 128 : 0;
            if (lane >= 16) { ts = (512 * hh + 32 * RHO(mm)) * 2048 + (2 * i8 + 1 - pp) * 128; tl = CSLOT(mm, pp); }
            __builtin_amdgcn_s_barrier();
            if (pp == 0) { asm volatile("s_waitcnt vmcnt(16)" ::: "memory"); sig(CW(C_X), 1u); }
            n = 4;
            dma_seg<WF>(mk_rsrc(p.wuk, EARLY * 1024 * 8192), n, 12, ts, tl, vp, prog, VOFFS(8192));
            STAMP(1);
            BLOCK(EGATE ? CW(C_PPUB) : CW(C_QH) + 4 * (lane & 7));
            STAMP(2);
            dma_seg<WF>(mk_rsrc(p.w1, e < 98 ? LATE * 448 * 3584 : 0), n, 14, ts, tl, vp, prog, VOFFS(3584));
            dma_seg<WF>(mk_rsrc(p.w2, e2 >= 0 ? LATE * 1792 * 896 : 0), n, 16, ts, tl, vp, prog, VOFFS(896));
            const rsrc_t rc = mk_rsrc(p.ckv, EARLY * 8192 * 2048);
            dma_seg<WF>(rc, n, 29, ts, tl, vp, prog, VOFFS(2048));
            STAMP(3);
            WAITF(0, C_RELC, 2);
            dma_seg<WF>(rc, n, 32, ts, tl, vp, prog, VOFFS(2048));
        } else {
            int cb = lane - 24 + 4 * hf; if (cb >= 7) cb -= 7;
            unsigned ts = (512 * hh + 64 * i8 + (lane >> 3) * 32) * 2048 + ((2 * i8 + 2 * lane + pp) & 15) * 128, tl = 2 * lane + pp;
            if (lane >= 16) { ts = (512 * hf + 32 * pp + (lane - 16) * 64) * 8192 + (hh * 256 + dq * 64) * 2; tl = 2 * lane + pp - 32; }
            if (lane >= 24) { ts = (hh * 256 + dq * 64 + 32 * pp) * 896 + cb * 128; tl = lane == 24 ? 32 + pp : 2 * lane + pp - 34; }
            tl *= 4096;
            const rsrc_t rc = mk_rsrc(p.ckv, EARLY * 8192 * 2048);
            __builtin_amdgcn_s_barrier();
            if (pp == 0) { DRAIN(); sig(CW(C_X), 1u); }
            wait_lds<0>(CW(C_FILL) + 4 * (lane < 9 ? lane : 0), 1u);
            if (NSLOW) dma_seg<WS>(rc, n, NSLOW, ts, tl, vp, prog, VOFFS(2048));
            if (RGATE) { dma_seg<WF>(rc, n, RGATE, ts, tl, vp, prog, VOFFS(2048)); STAMP(5); BLOCK(CW(C_QH) + 4 * (lane & 3)); STAMP(6); }
            dma_seg<WF>(rc, n, 16, ts, tl, vp, prog, VOFFS(2048));
            STAMP(1);
            BLOCK(CW(C_RELR) + 4 * (lane & 3));
            dma_seg<WV>(mk_rsrc(p.wuv, LATE * 1024 * 8192), n, NV, ts, tl, vp, prog, VOFFS(8192));
            dma_seg<WF>(mk_rsrc(p.wuv, LATE * 1024 * 8192), n, 24, ts, tl, vp, prog, VOFFS(8192));
            STAMP(2);
            const rsrc_t ro = mk_rsrc(p.wo, LATE * 4096 * 896);
            dma_seg<WF>(ro, n, 25, ts, tl, vp, prog, VOFFS(896));
            BLOCK(CW(C_P3 + 4) + 4 * (lane & 3));
            dma_seg<WF>(ro, n, 31, ts, tl, vp, prog, VOFFS(896));
        }
        STAMP(4);
        DRAINW(WF, 0);
        STAMP_OUT();
        return;
    }

    const rsrc_t ws = mk_rsrc(p.ws, WS_END);
    const v4u S4 = {SENT, SENT, SENT, SENT};
    const f32x4 Z4 = {0.f, 0.f, 0.f, 0.f};
    const int swr = ((l15 >> 1) & 1) | (((l15 >> 3) & 1) << 1);
    const int row_a0 = l15 * 128 + ((q4 ^ (swr << 1)) << 4), row_a1 = row_a0 ^ 64;
    const int swt = ((l15 >> 3) & 1) | ((q4 & 1) << 1);
    const int tr_b = (8 * q4 + (l15 >> 2)) * 128 + (((l15 >> 1) & 1) << 4) + 8 * (l15 & 1);
#define TR_A(nt) (tr_b + ((((nt) ^ swt)) << 5))
#define WAITT(S, k) wait_lds<S>(CW(C_PROG) + 4 * (lane & 1), (k))

    if (wv == 6) *(LAS v2u*)(L_CTL + lane * 8) = v2u{0u, 0u};
    if (!ecu && wv < 11) st16(ws, WS_H1 + ((wv - 2) * 128 + e) * 1024 + lane * 16, S4);
    LWAIT();
    __builtin_amdgcn_s_barrier();
    if (!ecu && wv < 11) { DRAIN(); sig(CW(C_FILL + wv - 2), 1u); }

    if (wv == 6) {
        wait_lds<0>((!ecu && lane < 9) ? CW(C_FILL + lane) : CW(C_X), 1u);
        const v2u ep = *(LAS v2u*)(L_E);
        const unsigned tagA = ep.x + 1u, tagB = ep.y | 1u;
        if (!ecu) st16(ws, lane < 8 ? WS_FLAG + e * 128 + lane * 16 : OOB, v4u{tagA, tagB, tagA, tagB});
        const int u = e < 28 ? e : -1;
        const int col = 16 * (u >= 0 ? u : 0) + 4 * (lane & 3) + 2 * (q4 & 1) + (q4 >> 1);
        const unsigned xv = *(LAS u16*)(L_X + col * 2);
        STAMP(1);
        if (ecu) WAITT(2, FSTART); else __builtin_amdgcn_s_sleep(DLYF);
        HI();
#pragma unroll 1
        for (unsigned s = 0; s < SPIN_G; ++s) { PASSES(7, s);
            CBAR();
            unsigned m = 0;
#pragma unroll
            for (int k = 0; k < 4; ++k) m |= ld4(ws, WS_FLAG + k * 4096 + (lane >> 1) * 128 + (lane & 1) * 4) ^ ((lane & 1) ? tagB : tagA);
            if (!any_nz(m)) break;
            __builtin_amdgcn_s_sleep(4);
        }
        sig(CW(C_FLOK), 1u);
        STAMP(2);
        if (ecu) {
            const int e2 = e - 30, fs2 = e2 / 7, mt2 = e2 % 7;
            unsigned b1a = 0, b1b = 0, b2v = 0;
            WAITF(2, C_RELQ, 4);
            if (e2 >= 0) { b1a = ((const GAS u16*)p.b1)[128 * fs2 + lane]; b1b = ((const GAS u16*)p.b1)[128 * fs2 + 64 + lane]; }
            if (u >= 0) b2v = ((const GAS u16*)p.b2)[col];
            if (e < 98) {
                const int ms1 = e % 7;
                const v4u bx0 = lds16(L_XN + (64 * ms1 + 8 * q4) * 2), bx1 = lds16(L_XN + (64 * ms1 + 32 + 8 * q4) * 2);
                WAITT(2, 14u);
#pragma unroll
                for (int cb = 0; cb < 2; ++cb) {
                    f32x4 a[4];
#pragma unroll
                    for (int nt = 0; nt < 4; ++nt) {
                        a[nt] = mfma(bx0, lds_tr<512>((24 + 2 * cb) * 4096 + TR_A(nt)), Z4);
                        a[nt] = mfma(bx1, lds_tr<512>((25 + 2 * cb) * 4096 + TR_A(nt)), a[nt]);
                    }
                    st4(ws, WS_HID + e * 512 + cb * 256 + lane * 4, san(f2u(sel4(q4, a))));
                }
            }
            STAMP(3);
            if (e2 >= 0) {
                unsigned hp[14];
                WAITT(2, HIDSTART);
#pragma unroll 1
                for (unsigned s = 0; s < SPIN_G; ++s) { PASSES(2, s);
                    CBAR();
                    unsigned m = 0;
#pragma unroll
                    for (int k = 0; k < 14; ++k) hp[k] = ld4(ws, WS_HID + fs2 * 3584 + k * 256 + lane * 4);
#pragma unroll
                    for (int k = 0; k < 14; ++k) m = umax(m, hp[k]);
                    if (!any_sent(m)) break;
                    __builtin_amdgcn_s_sleep(4);
                }
                float ha = 0.f, hb = 0.f;
#pragma unroll
                for (int k = 0; k < 7; ++k) { ha += u2f(hp[2 * k]); hb += u2f(hp[2 * k + 1]); }
                ha = rbf(rbf(ha) + bflo(b1a)); hb = rbf(rbf(hb) + bflo(b1b));
                ha = ha > 0.f ? ha : 0.f; hb = hb > 0.f ? hb : 0.f;
                const unsigned hq = pk_bf16(ha * ha, hb * hb);
                *(LAS u16*)(L_HID + lane * 2) = (u16)hq; *(LAS u16*)(L_HID + 128 + lane * 2) = (u16)(hq >> 16);
                CBAR();
                STAMP(4);
                f32x4 a[4] = {Z4, Z4, Z4, Z4};
#pragma unroll
                for (int k = 0; k < 4; ++k) {
                    const v4u bh = lds16(L_HID + k * 64 + q4 * 16);
#pragma unroll
                    for (int nt = 0; nt < 4; ++nt) a[nt] = mfma(bh, lds_tr<512>((28 + k) * 4096 + TR_A(nt)), a[nt]);
                }
                st4(ws, WS_GP + fs2 * 1792 + (64 * mt2 + lane) * 4, san(f2u(sel4(q4, a))));
            }
            STAMP(5);
            if (u >= 0) {
                v4u gv;
                WAITF(2, C_CG, 8);
#pragma unroll 1
                for (unsigned s = 0; s < SPIN_G; ++s) { PASSES(3, s);
                    CBAR();
                    gv = ld16(ws, lane < 56 ? WS_GP + (lane >> 2) * 1792 + u * 64 + (lane & 3) * 16 : OOB);
                    if (!any_sent(umax4(gv))) break;
                    __builtin_amdgcn_s_sleep(4);
                }
                float xf = bflo(xv);
                const float ga = rbf(rbf(quad_sum4(u4f(gv))) + bflo(b2v));
                float g = rbf(ga / (1.f + __expf(-ga)));
                int o_s = WS_H4 + u * 8192 + lane * 16;
                int o_o = l15 < 4 ? col * 2 : OOB;
                const rsrc_t ro = mk_rsrc(p.out, 896);
                PIN(g); PIN(xf); PIN(o_s); PIN(o_o);
                v4u sv[8];
#pragma unroll
                for (int k = 0; k < 8; ++k) sv[k] = S4;
                STAMP(6);
                WAITF(2, C_S4, 4);
                __builtin_amdgcn_s_sleep(DLY4);
#pragma unroll 1
                for (unsigned s = 0; s < SPIN_G; ++s) { PASSES(4, s);
                    CBAR();
                    unsigned m = 0;
#pragma unroll
                    for (int k = 0; k < 8; ++k) if (any_sent(umax4(sv[k]))) sv[k] = __builtin_amdgcn_raw_buffer_load_b128(ws, o_s + (k & 3) * 1024, (k >> 2) * 4096, AUX_SC1);
#pragma unroll
                    for (int k = 0; k < 8; ++k) m = umax(m, umax4(sv[k]));
                    if (!any_sent(m)) break;
                    __builtin_amdgcn_s_sleep(RETRY);
                }
                STAMP(8);
                const float s = quad_sum4(((u4f(sv[0]) + u4f(sv[1])) + (u4f(sv[2]) + u4f(sv[3]))) + ((u4f(sv[4]) + u4f(sv[5])) + (u4f(sv[6]) + u4f(sv[7]))));
                const float o = xf + rbf(rbf(s) * g);
                __builtin_amdgcn_raw_buffer_store_b16((short)pk_bf16(o, o), ro, o_o, 0, 0);
                if (u == 0) st8(ws, lane == 0 ? WS_EPOCH : OOB, v2u{tagA, ep.y});
            }
        }
        STAMP_OUT();
        return;
    }

    if (ecu) {
        if (wv < 6) {
            const int t = wv - 2;
            int o_h0 = lane < 8 ? WS_H0 + ((((hh + HSHIFT) & 15) * 4 + dq) * 2 + hf) * 512 + t * 128 + lane * 16 : OOB;
            int o_h1 = lane < 16 ? WS_H1 + ((2 * i8 + (lane >> 3)) * 16 + hh) * 128 + (lane & 7) * 16 : OOB;
            st16(ws, o_h0, v4u{0u, 0u, 0u, 0u});
            int tra = TR_A(t);
            int a_bx = L_XN + (224 * hf + 8 * q4) * 2;
            int o_g0 = WS_H0 + (hh * 4 + t) * 1024 + lane * 16;
            PIN(tra); PIN(a_bx); PIN(o_h0); PIN(o_g0);
            WAITP(0, C_X, 1u);
            const v2u ep = *(LAS v2u*)(L_E);
            {
                const v4u xv = lds16(L_X + lane * 16);
                float ev[8] = {bflo(xv.x), bfhi(xv.x), bflo(xv.y), bfhi(xv.y), bflo(xv.z), bfhi(xv.z), bflo(xv.w), bfhi(xv.w)};
                const float s = ((ev[0] + ev[1]) + (ev[2] + ev[3])) + ((ev[4] + ev[5]) + (ev[6] + ev[7]));
                const float mean = wave_sum(s) * (1.0f / 448.0f);
                float v = 0.f;
#pragma unroll
                for (int k = 0; k < 8; ++k) { const float d = ev[k] - mean; v += d * d; }
                v = lane < 56 ? v : 0.f;
                const float var = wave_sum(v) * (1.0f / 448.0f);
                const float mb = rbf(mean);
                const float rs = 1.0f / rbf(__builtin_amdgcn_sqrtf(rbf(rbf(var) + 1e-5f)));
                float o[8];
#pragma unroll
                for (int k = 0; k < 8; ++k) o[k] = rbf(ev[k] - mb) * rs;
                *(LAS v4u*)(L_XN + lane * 16) = v4u{pk_bf16(o[0], o[1]), pk_bf16(o[2], o[3]), pk_bf16(o[4], o[5]), pk_bf16(o[6], o[7])};
                CBAR();
            }
            const unsigned tagA = ep.x + 1u, tagB = ep.y | 1u;
            {
                v4u bx[7], a[7];
#pragma unroll
                for (int s = 0; s < 7; ++s) bx[s] = lds16(a_bx + 64 * s);
                STAMP(1);
                wait_lds<0>(CW(C_PROG) + 4 * (lane & 1), (lane & 1) ? 3u : 4u);
                HI();
                STAMP(2);
#pragma unroll
                for (int s = 0; s < 7; ++s) a[s] = lds_tr<512>(tra + s * 4096);
                SB();
                f32x4 acc = mfma(bx[0], a[0], Z4), acc2 = mfma(bx[1], a[1], Z4);
                acc = mfma(bx[2], a[2], acc); acc2 = mfma(bx[3], a[3], acc2); acc = mfma(bx[4], a[4], acc); acc2 = mfma(bx[5], a[5], acc2); acc = mfma(bx[6], a[6], acc);
                const float qv = acc.x + acc2.x;
                st16(ws, o_h0, v4u{f2u(qv), tagA, f2u(ror<8>(qv)), tagB});
            }
            STAMP(3);
            __builtin_amdgcn_s_sleep(DLY0);
            int a_qw = L_Q + t * 128 + ((lane >> 3) & 3) * 32 + (lane & 7) * 2, a_qr = L_Q + t * 128 + q4 * 16;
            int a_k0 = (8 + 4 * ((t - i8) & 3)) * 4096 + row_a0, a_k1 = a_k0 ^ 64;
            int a_pw = L_S2P + t * 512 + lane * 4;
            int a_pr = L_S2P + l15 * 32;
            PIN(a_qw); PIN(a_qr); PIN(a_k0); PIN(a_k1); PIN(o_h1); PIN(a_pw); PIN(a_pr);
            HI();
            v4u g0 = ld16(ws, o_g0);
            v4u ak[16];
            WAITT(0, 6u + 2 * ((t - i8) & 3));
#pragma unroll
            for (int k = 0; k < 8; ++k) { ak[2 * k] = lds16(a_k0 + k * 2048); ak[2 * k + 1] = lds16(a_k1 + k * 2048); }
            sig(CW(C_RELQ + t), 1u);
#pragma unroll 1
            for (unsigned s = 0; s < SPIN_G; ++s) { PASSES(0, s);
                if (!any_nz((g0.y ^ tagA) | (g0.w ^ tagB))) break;
                __builtin_amdgcn_s_sleep(RETRY);
                CBAR();
                g0 = ld16(ws, o_g0);
            }
            sig(CW(C_QH + t), 1u);
            STAMP(4);
            {
                auto ra = __builtin_amdgcn_permlane32_swap(g0.x, g0.x, false, false);
                auto rb = __builtin_amdgcn_permlane32_swap(g0.z, g0.z, false, false);
                const unsigned qq = pk_bf16(u2f(ra[0]) + u2f(ra[1]), u2f(rb[0]) + u2f(rb[1]));
                *(LAS u16*)(a_qw) = (u16)qq; *(LAS u16*)(a_qw + 16) = (u16)(qq >> 16);
                CBAR();
                const v4u bq0 = lds16(a_qr), bq1 = lds16(a_qr + 64);
                f32x4 a[4], a2[4];
#pragma unroll
                for (int k = 0; k < 4; ++k) a[k] = mfma(bq0, ak[2 * k], Z4);
#pragma unroll
                for (int k = 0; k < 4; ++k) a[k] = mfma(bq1, ak[2 * k + 1], a[k]);
#pragma unroll
                for (int k = 0; k < 4; ++k) a2[k] = mfma(bq0, ak[8 + 2 * k], Z4);
#pragma unroll
                for (int k = 0; k < 4; ++k) a2[k] = mfma(bq1, ak[9 + 2 * k], a2[k]);
                *(LAS float*)(a_pw) = sel4(q4, a);
                *(LAS float*)(a_pw + 256) = sel4(q4, a2);
                sig(CW(C_QX + t), 1u);
            }
            STAMP(5);
            if (t == 0) {
                wait_lds<0>(lane < 4 ? CW(C_QX + lane) : CW(C_FLOK), 1u);
                f32x4 v0[4], v1[4];
#pragma unroll
                for (int k = 0; k < 4; ++k) { v0[k] = lds16f(a_pr + k * 512); v1[k] = lds16f(a_pr + k * 512 + 16); }
                SB();
                const float sc = 0.0625f * 1.4426950408889634f;
                const f32x4 x0 = ((v0[0] + v0[1]) + (v0[2] + v0[3])) * sc, x1 = ((v1[0] + v1[1]) + (v1[2] + v1[3])) * sc;
                st16(ws, o_h1, san4(v4u{pk_bf16(x0.x, x0.y), pk_bf16(x0.z, x0.w), pk_bf16(x1.x, x1.y), pk_bf16(x1.z, x1.w)}));
                sig(CW(C_PPUB), 1u);
            }
            STAMP_OUT();
            return;
        }
        const int pp = (wv - 7) & 1, kq = (wv - 7) >> 1;
        v4u ac[16];
        {
            const int rr = 4 * q4 + (l15 >> 2);
            const int sc_ = ((rr >> 1) & 1) | (((rr >> 3) & 1) << 1);
            const int tb = rr * 128 + (((l15 >> 1) & 1) << 4) + 8 * (l15 & 1);
            wait_lds<1>(lane & 1 ? CW(C_PROG + pp) : CW(C_FLOK), lane & 1 ? 20u + 4 * kq : 1u);
            STAMP(1);
#pragma unroll
            for (int u = 0; u < 4; ++u) {
                const int cbase = CSLOT(4 * kq + u, pp);
#pragma unroll
                for (int nt = 0; nt < 4; ++nt) ac[4 * u + nt] = lds_tr<2048>(cbase + tb + ((nt ^ sc_) << 5));
            }
            if (kq == 0) sig(CW(C_RELC + pp), 1u);
        }
        int o_p = WS_P + hh * 16384 + lane * 16;
        int o_s = WS_PS + (hh * 16 + RHO(4 * kq + q4)) * 128 + l15 * 4;
        int a_sw = L_ST4 + kq * 128 + l15 * 8;
        int a_p[4];
#pragma unroll
        for (int nt = 0; nt < 4; ++nt) { a_p[nt] = CSLOT(4 * kq + 3, pp) + nt * 1024 + (q4 * 16 + ((l15 + 4 * nt + (q4 & 2)) & 15)) * 16; PIN(a_p[nt]); }
        const int hd = 8 * (kq & 1) + (lane >> 3), ck = lane & 7;
        int o_h2 = WS_H2 + (hd * 2 + (i8 >> 2)) * 16384 + hh * 1024 + (2 * (i8 & 3) + 1 - pp) * 128 + ck * 16;
        int o_h2s = lane < 32 ? WS_H2S + (hh * 2 + (i8 >> 2)) * 128 + lane * 4 : OOB;
        int a_rd = pp * 4096 + (ck >> 1) * 1024 + ((ck & 1) * 32 + ((hd + 2 * ck) & 15)) * 16;
        int a_sr = L_ST4 + (kq < 2 ? hd : l15) * 8;
        PIN(o_p); PIN(o_s); PIN(a_sw); PIN(o_h2); PIN(o_h2s); PIN(a_rd); PIN(a_sr);
        WAITT(1, 32u);
        STAMP(7);
        __builtin_amdgcn_s_sleep(DLY2);
        HI();
        v4u bp[4]; unsigned pr = SENT, pl = SENT;
#pragma unroll
        for (int u = 0; u < 4; ++u) bp[u] = S4;
#pragma unroll 1
        for (unsigned s = 0; s < SPIN_G; ++s) { PASSES(6, s);
            CBAR();
            pr = ld4(ws, o_s); pl = ld4(ws, o_s + 64);
#pragma unroll
            for (int u = 0; u < 4; ++u)
                if (any_sent(umax4(bp[u]))) bp[u] = __builtin_amdgcn_raw_buffer_load_b128(ws, o_p, RHO(4 * kq + u) * 1024, AUX_SC1);
            unsigned m = umax(pr, pl);
#pragma unroll
            for (int u = 0; u < 4; ++u) m = umax(m, umax4(bp[u]));
            if (!any_sent(m)) break;
            __builtin_amdgcn_s_sleep(RETRY);
        }
        sig(CW(C_CG + wv - 7), 1u);
        STAMP(8);
        {
            const float rw = xrow_max(u2f(pr));
            const float dl = rw - u2f(pr);
            const unsigned dd = (unsigned)__builtin_amdgcn_fmed3f(dl, 0.f, 255.f) * 0x00800080u;
            auto x = __builtin_amdgcn_permlane32_swap(dd, dd, false, false);
            auto y = __builtin_amdgcn_permlane16_swap(x[0], x[0], false, false);
            auto z = __builtin_amdgcn_permlane16_swap(x[1], x[1], false, false);
            const unsigned du[4] = {y[0], y[1], z[0], z[1]};
            f32x4 c[4] = {Z4, Z4, Z4, Z4};
#pragma unroll
            for (int u = 0; u < 4; ++u) {
                const u16x2 d2 = __builtin_bit_cast(u16x2, du[u]);
                const unsigned x0 = bp[u].x, x1 = bp[u].y, x2 = bp[u].z, x3 = bp[u].w;
                const v4u bs = {__builtin_bit_cast(unsigned, __builtin_elementwise_sub_sat(__builtin_bit_cast(u16x2, x0), d2)),
                                __builtin_bit_cast(unsigned, __builtin_elementwise_sub_sat(__builtin_bit_cast(u16x2, x1), d2)),
                                __builtin_bit_cast(unsigned, __builtin_elementwise_sub_sat(__builtin_bit_cast(u16x2, x2), d2)),
                                __builtin_bit_cast(unsigned, __builtin_elementwise_sub_sat(__builtin_bit_cast(u16x2, x3), d2))};
#pragma unroll
                for (int nt = 0; nt < 4; ++nt) c[nt] = mfma(ac[4 * u + nt], bs, c[nt]);
            }
            const float lw = xrow_sum(__builtin_amdgcn_exp2f(-dl) * u2f(pl));
            *(LAS f32x2*)(a_sw) = f32x2{rw, lw};
#pragma unroll
            for (int nt = 0; nt < 4; ++nt) *(LAS f32x4*)(a_p[nt]) = c[nt];
            sig(CW(C_P4 + pp * 4 + kq), 1u);
        }
        STAMP(9);
        if (kq < 2) {
            WAITF(0, C_P4 + pp * 4, 4);
            STAMP(10);
            f32x4 v0[4], v1[4]; f32x2 sx[4];
#pragma unroll
            for (int k = 0; k < 4; ++k) sx[k] = *(LAS f32x2*)(a_sr + k * 128);
#pragma unroll
            for (int k = 0; k < 4; ++k) { v0[k] = lds16f(a_rd + CSLOT(4 * k + 3, 0)); v1[k] = lds16f(a_rd + CSLOT(4 * k + 3, 0) + 256); }
            SB();
            const float R = fmx(fmx(sx[0].x, sx[1].x), fmx(sx[2].x, sx[3].x));
            float sc[4];
#pragma unroll
            for (int k = 0; k < 4; ++k) sc[k] = __builtin_amdgcn_exp2f(sx[k].x - R);
            const f32x4 a0 = (v0[0] * sc[0] + v0[1] * sc[1]) + (v0[2] * sc[2] + v0[3] * sc[3]);
            const f32x4 a1 = (v1[0] * sc[0] + v1[1] * sc[1]) + (v1[2] * sc[2] + v1[3] * sc[3]);
            st16(ws, o_h2, san4(v4u{pk_bf16(a0.x, a0.y), pk_bf16(a0.z, a0.w), pk_bf16(a1.x, a1.y), pk_bf16(a1.z, a1.w)}));
            sig(CW(C_S4 + 2 * kq + pp), 1u);
        } else if (kq == 2 && pp == 0 && (i8 & 3) == 0) {
            WAITF(0, C_P4, 4);
            f32x2 sx[4];
#pragma unroll
            for (int k = 0; k < 4; ++k) sx[k] = *(LAS f32x2*)(a_sr + k * 128);
            const float R = fmx(fmx(sx[0].x, sx[1].x), fmx(sx[2].x, sx[3].x));
            const float lg = (sx[0].y * __builtin_amdgcn_exp2f(sx[0].x - R) + sx[1].y * __builtin_amdgcn_exp2f(sx[1].x - R))
                           + (sx[2].y * __builtin_amdgcn_exp2f(sx[2].x - R) + sx[3].y * __builtin_amdgcn_exp2f(sx[3].x - R));
            st4(ws, o_h2s, san(f2u(q4 == 0 ? R : lg)));
        }
        STAMP_OUT();
        return;
    }

    if (wv >= 11) {
        if (wv >= 13) { STAMP_OUT(); return; }
        const int rb = wv - 11;
        int o_p = WS_P + (hh * 16 + 2 * i8 + rb) * 1024 + lane * 16;
        int o_ps = lane < 32 ? WS_PS + (hh * 16 + 2 * i8 + rb) * 128 + lane * 4 : OOB;
        int a_pr = L_PART + rb * 2048 + lane * 16;
        int a_f = CW(C_P3 + 4 * rb) + 4 * (lane < 3 ? lane : 0);
        PIN(o_p); PIN(o_ps); PIN(a_pr); PIN(a_f);
        WAITT(2, G1START);
        HI();
        f32x4 s0, s1;
        {
            f32x4 v0[3], v1[3];
            wait_lds<0>(a_f, 1u);
#pragma unroll
            for (int k = 0; k < 3; ++k) { v0[k] = lds16f(a_pr + k * 4096); v1[k] = lds16f(a_pr + k * 4096 + 1024); }
            WAITP(0, C_P3 + 4 * rb + 3, 1u);
            STAMP(4);
            const f32x4 x0 = lds16f(a_pr + 3 * 4096), x1 = lds16f(a_pr + 3 * 4096 + 1024);
            SB();
            s0 = ((v0[0] + v0[1]) + v0[2]) + x0;
            s1 = ((v1[0] + v1[1]) + v1[2]) + x1;
        }
        {
            const float mx = xrow_max(fmx(fmx(fmx(s0.x, s0.y), fmx(s0.z, s0.w)), fmx(fmx(s1.x, s1.y), fmx(s1.z, s1.w))));
            const float r = __builtin_ceilf(mx);
            const float p0 = __builtin_amdgcn_exp2f(s0.x - r), p1 = __builtin_amdgcn_exp2f(s0.y - r), p2 = __builtin_amdgcn_exp2f(s0.z - r), p3 = __builtin_amdgcn_exp2f(s0.w - r);
            const float p4 = __builtin_amdgcn_exp2f(s1.x - r), p5 = __builtin_amdgcn_exp2f(s1.y - r), p6 = __builtin_amdgcn_exp2f(s1.z - r), p7 = __builtin_amdgcn_exp2f(s1.w - r);
            st16(ws, o_p, san4(v4u{pk_bf16(p0, p1), pk_bf16(p2, p3), pk_bf16(p4, p5), pk_bf16(p6, p7)}));
            const float l = xrow_sum(((p0 + p1) + (p2 + p3)) + ((p4 + p5) + (p6 + p7)));
            st4(ws, o_ps, san(f2u(q4 == 0 ? r : l)));
            sig(CW(C_PPUB + rb), 1u);
        }
        STAMP_OUT();
        return;
    }

    const int gw = wv < 6 ? wv - 2 : wv - 3;
#define H2_SETUP() int o_h2 = WS_H2 + (hh * 2 + hf) * 16384 + gw * 2048 + lane * 16; \
        int a_w0 = L_PART + (lane >> 3) * 2048 + gw * 256 + (((lane & 7) ^ (((gw & 1) | ((gw >> 2) << 1)) << 1)) << 4); PIN(o_h2); PIN(a_w0);

    if (wv < 6) {
        const int t = wv - 2;
        v4u ak[16];
        WAITT(2, 24u);
        {
            const int tra = TR_A(t);
#pragma unroll
            for (int s = 0; s < 16; ++s) ak[s] = lds_tr<512>(tra + s * 4096);
        }
        H2_SETUP();
        int o_st = lane < 8 ? WS_H2S + ((4 * t + (lane >> 1)) * 2 + hf) * 128 + (lane & 1) * 64 + hh * 4 : OOB;
        int a_sw = lane < 8 ? L_ST5 + (lane & 1) * 64 + (4 * t + (lane >> 1)) * 4 : L_X + lane * 4;
        int a_r0 = L_PART + row_a0, a_r1 = L_PART + row_a1, a_sr = L_ST5 + q4 * 16;
        int a_cw = L_CTX + (16 * t + l15) * 2;
        int a_g2 = CW(C_G2) + 4 * (lane & 7);
        PIN(o_st); PIN(a_sw); PIN(a_r0); PIN(a_r1); PIN(a_sr); PIN(a_cw); PIN(a_g2);
        STAMP(8);
        WAITF(1, C_PPUB, 2);
        __builtin_amdgcn_s_sleep(DLY3V);
        HI();
        STAMP(9);
        {
            v4u g2[2] = {S4, S4}; unsigned st;
#pragma unroll 1
            for (unsigned s = 0; s < SPIN_G; ++s) { PASSES(1, s);
                CBAR();
                st = ld4(ws, o_st);
                if (any_sent(umax4(g2[0]))) g2[0] = ld16(ws, o_h2);
                if (any_sent(umax4(g2[1]))) g2[1] = ld16(ws, o_h2 + 1024);
                if (!any_sent(umax(st, umax(umax4(g2[0]), umax4(g2[1]))))) break;
                __builtin_amdgcn_s_sleep(RETRY);
            }
            *(LAS v4u*)(a_w0) = g2[0];
            *(LAS v4u*)(a_w0 + 128) = g2[1];
            *(LAS unsigned*)(a_sw) = st;
            sig(CW(C_G2 + gw), 1u);
        }
        STAMP(10);
        wait_lds<0>(a_g2, 1u);
        STAMP(11);
        {
            const f32x4 rg = lds16f(a_sr), lg = lds16f(a_sr + 64);
            f32x4 acc = Z4, acc2 = Z4;
#pragma unroll
            for (int hb = 0; hb < 2; ++hb) {
                v4u bb[8];
#pragma unroll
                for (int k = 0; k < 4; ++k) { bb[2 * k] = lds16(a_r0 + (4 * hb + k) * 2048); bb[2 * k + 1] = lds16(a_r1 + (4 * hb + k) * 2048); }
#pragma unroll
                for (int k = 0; k < 4; ++k) { acc = mfma(bb[2 * k], ak[8 * hb + 2 * k], acc); acc2 = mfma(bb[2 * k + 1], ak[8 * hb + 2 * k + 1], acc2); }
            }
            const float R = xrow_max(fmx(fmx(rg.x, rg.y), fmx(rg.z, rg.w)));
            const f32x4 sc = {__builtin_amdgcn_exp2f(rg.x - R), __builtin_amdgcn_exp2f(rg.y - R), __builtin_amdgcn_exp2f(rg.z - R), __builtin_amdgcn_exp2f(rg.w - R)};
            const float il = __builtin_amdgcn_rcpf(xrow_sum((sc.x * lg.x + sc.y * lg.y) + (sc.z * lg.z + sc.w * lg.w)));
            acc = (acc + acc2) * sc;
            const float cx = xrow_sum((acc.x + acc.y) + (acc.z + acc.w)) * il;
            *(LAS u16*)(a_cw) = (u16)pk_bf16(cx, cx);
            sig(CW(C_CTX + t), 1u);
        }
        STAMP_OUT();
        return;
    }

    {
        const int v = wv - 7;
        v4u ar[16], g[8];
#pragma unroll
        for (int k = 0; k < 16; ++k) ar[k] = v4u{0u, 0u, 0u, 0u};
#pragma unroll
        for (int k = 0; k < 8; ++k) g[k] = S4;
        int a_r0 = 4 * v * 4096 + row_a0, a_r1 = a_r0 ^ 64;
        const int pw = L_PART + v * 4096;
        int o_q = WS_H1 + lane * 16;
        int a_qw = pw + (lane >> 3) * 128 + (((lane & 7) ^ (q4 & 1) << 1) << 4), a_qw1 = (a_qw ^ 64) + 1024;
        int a_q0 = pw + row_a0, a_q1 = pw + row_a1, a_pw = pw + lane * 16;
        int a_poll = lane < 2 ? CW(C_PROG + lane) : CW(C_FLOK);
        PIN(a_r0); PIN(a_r1); PIN(o_q); PIN(a_qw); PIN(a_qw1); PIN(a_q0); PIN(a_q1); PIN(a_pw); PIN(a_poll);
#define PULL_R(rb, k) do { _Pragma("unroll") for (int z = 2 * (k); z < 2 * (k) + 2; ++z) { \
            ar[4 * z] = lds16(a_r0 + (rb) * 65536 + z * 4096); ar[4 * z + 1] = lds16(a_r0 + (rb) * 65536 + z * 4096 + 2048); \
            ar[4 * z + 2] = lds16(a_r1 + (rb) * 65536 + z * 4096); ar[4 * z + 3] = lds16(a_r1 + (rb) * 65536 + z * 4096 + 2048); } } while (0)
#define MM_R(k) do { _Pragma("unroll") for (int z = 2 * (k); z < 2 * (k) + 2; ++z) { \
            s0 = mfma(ar[4 * z], g[2 * z], s0); s1 = mfma(ar[4 * z + 1], g[2 * z], s1); s0 = mfma(ar[4 * z + 2], g[2 * z + 1], s0); s1 = mfma(ar[4 * z + 3], g[2 * z + 1], s1); } } while (0)
        STAMP(1);
        bool pulled = false, first = true;
#pragma unroll 1
        for (unsigned s = 0; s < (1u << 19); ++s) {
            CBAR();
            const unsigned pv = *(volatile LAS unsigned*)a_poll;
            const int p0 = __builtin_amdgcn_readlane(pv, 0), p1 = __builtin_amdgcn_readlane(pv, 1), pm = p0 < p1 ? p0 : p1;
            const unsigned ok = __builtin_amdgcn_readlane(pv, 2);
            if (!pulled && pm >= 2 * v + 2) { PULL_R(0, 0); PULL_R(0, 1); sig(CW(C_RELR + v), 1u); pulled = true; }
            if (ok && pm >= G1START) {
                if (!first) __builtin_amdgcn_s_sleep(RETRY1);
                first = false;
                HI();
                CBAR();
#pragma unroll
                for (int k = 0; k < 8; ++k)
                    if (any_sent(umax4(g[k]))) g[k] = __builtin_amdgcn_raw_buffer_load_b128(ws, o_q + (k & 1) * 1024, ((2 * i8 + 4 * v + (k >> 1)) & 15) * 2048, AUX_SC1);
                s += 64;
                unsigned m = 0;
#pragma unroll
                for (int k = 0; k < 8; ++k) m = umax(m, umax4(g[k]));
                if (!any_sent(m)) break;
            } else __builtin_amdgcn_s_sleep(1);
        }
        sig(CW(C_QH + v), 1u);
        STAMP(2);
#pragma unroll
        for (int k = 0; k < 2; ++k) {
            *(LAS v4u*)(a_qw) = g[4 * k]; *(LAS v4u*)(a_qw1) = g[4 * k + 1]; *(LAS v4u*)(a_qw + 2048) = g[4 * k + 2]; *(LAS v4u*)(a_qw1 + 2048) = g[4 * k + 3];
            CBAR();
            g[4 * k] = lds16(a_q0); g[4 * k + 1] = lds16(a_q1); g[4 * k + 2] = lds16(a_q0 + 2048); g[4 * k + 3] = lds16(a_q1 + 2048);
            CBAR();
        }
        if (!pulled) { WAITT(0, 2u * v + 2); PULL_R(0, 0); PULL_R(0, 1); sig(CW(C_RELR + v), 1u); }
        STAMP(3);
        {
            f32x4 s0 = Z4, s1 = Z4;
            MM_R(0); MM_R(1);
            *(LAS f32x4*)(a_pw) = s0; *(LAS f32x4*)(a_pw + 1024) = s1;
            sig(CW(C_P3 + v), 1u);
            STAMP(4);
            s0 = Z4; s1 = Z4;
            WAITT(0, 2u * v + 9); PULL_R(1, 0);
            WAITT(0, 2u * v + 10); PULL_R(1, 1);
            SB();
            MM_R(0); MM_R(1);
            *(LAS f32x4*)(a_pw + 2048) = s0; *(LAS f32x4*)(a_pw + 3072) = s1;
            sig(CW(C_P3 + 4 + v), 1u);
        }
        STAMP(5);
        int cb0 = 2 * v + 4 * hf; if (cb0 >= 7) cb0 -= 7;
        int cb1 = cb0 + 1; if (cb1 >= 7) cb1 -= 7;
        int o_h40 = WS_H4 + (4 * cb0 + q4) * 8192 + e * 64 + l15 * 4;
        int o_h41 = v < 3 ? WS_H4 + (4 * cb1 + q4) * 8192 + e * 64 + l15 * 4 : OOB;
        H2_SETUP();
        int a_c = L_CTX + q4 * 16, a_cf = CW(C_CTX) + 4 * (lane & 3);
        PIN(a_c); PIN(a_cf); PIN(o_h40); PIN(o_h41);
        WAITF(1, C_PPUB, 2);
        __builtin_amdgcn_s_sleep(DLY3);
        bool po = false, gd = false;
        g[0] = S4; g[1] = S4;
        const int ob0 = (v == 0 ? 32 : 14 + 4 * v) * 4096, ob2 = (v < 3 ? 16 + 4 * v : 26) * 4096;
#pragma unroll 1
        for (unsigned s = 0; s < (1u << 19); ++s) { PASSES(1, s);
            CBAR();
            if (!gd) {
                if (any_sent(umax4(g[0]))) g[0] = ld16(ws, o_h2);
                if (any_sent(umax4(g[1]))) g[1] = ld16(ws, o_h2 + 1024);
            }
            if (!po) {
                const unsigned pv = *(volatile LAS unsigned*)a_poll;
                const int p0 = __builtin_amdgcn_readlane(pv, 0), p1 = __builtin_amdgcn_readlane(pv, 1), pm = p0 < p1 ? p0 : p1;
                if (pm >= (v < 3 ? 26 + 2 * v : 31)) {
#pragma unroll
                    for (int nt = 0; nt < 4; ++nt) {
                        ar[nt] = lds_tr<512>(ob0 + TR_A(nt)); ar[4 + nt] = lds_tr<512>(ob0 + 4096 + TR_A(nt));
                        ar[8 + nt] = lds_tr<512>(ob2 + TR_A(nt)); ar[12 + nt] = lds_tr<512>(ob2 + 4096 + TR_A(nt));
                    }
                    po = true;
                }
            }
            if (!gd) {
                s += 16;
                if (!any_sent(umax(umax4(g[0]), umax4(g[1])))) {
                    *(LAS v4u*)(a_w0) = g[0];
                    *(LAS v4u*)(a_w0 + 128) = g[1];
                    sig(CW(C_G2 + gw), 1u);
                    gd = true;
                    STAMP(10);
                } else __builtin_amdgcn_s_sleep(RETRY);
            }
            if (gd && po) break;
        }
        STAMP(12);
        wait_lds<0>(a_cf, 1u);
        STAMP(13);
        {
            const v4u bc0 = lds16(a_c), bc1 = lds16(a_c + 64);
            f32x4 a[4], a2[4];
#pragma unroll
            for (int nt = 0; nt < 4; ++nt) a[nt] = mfma(bc0, ar[nt], Z4);
#pragma unroll
            for (int nt = 0; nt < 4; ++nt) a[nt] = mfma(bc1, ar[4 + nt], a[nt]);
#pragma unroll
            for (int nt = 0; nt < 4; ++nt) a2[nt] = mfma(bc0, ar[8 + nt], Z4);
#pragma unroll
            for (int nt = 0; nt < 4; ++nt) a2[nt] = mfma(bc1, ar[12 + nt], a2[nt]);
            st4(ws, o_h40, san(f2u(sel4(q4, a))));
            st4(ws, o_h41, san(f2u(sel4(q4, a2))));
        }
        STAMP_OUT();
    }
}

extern "C" void kernel_launch(void* const* d_in, const int* in_sizes, int n_in,
                              void* d_out, int out_size, void* d_ws, size_t ws_size,
                              hipStream_t stream) {
    Params p;
    p.x = d_in[0]; p.ckv = d_in[1]; p.wq = d_in[2]; p.wuk = d_in[3]; p.wuv = d_in[4]; p.wo = d_in[5];
    p.w1 = d_in[7]; p.b1 = d_in[8]; p.w2 = d_in[9]; p.b2 = d_in[10]; p.out = d_out; p.ws = (char*)d_ws + (size_t)WSOFF;
    (void)hipFuncSetAttribute((const void*)fused_decode, hipFuncAttributeMaxDynamicSharedMemorySize, L_END);
    fused_decode<<<256, NWAVE * 64, L_END, stream>>>(p);
}
```

```cpp
#define NOPF
#define FWD
#define RGATE 16
#define G1START 13
#define NSLOW 2
#define NT_W 1
#define PECU 1
#define PWAVE 2
#define PKEY 3
#include <hip/hip_runtime.h>
#include <stdint.h>

typedef unsigned v4u __attribute__((ext_vector_type(4)));
typedef unsigned v2u __attribute__((ext_vector_type(2)));
typedef float f32x4 __attribute__((ext_vector_type(4)));
typedef float f32x2 __attribute__((ext_vector_type(2)));
typedef __bf16 bf16x8 __attribute__((ext_vector_type(8)));
typedef __bf16 bf16x2 __attribute__((ext_vector_type(2)));
typedef short s16x4 __attribute__((ext_vector_type(4)));
typedef unsigned short u16x2 __attribute__((ext_vector_type(2)));
typedef unsigned short u16;
#define LAS __attribute__((address_space(3)))
#define GAS __attribute__((address_space(1)))
typedef __amdgpu_buffer_rsrc_t rsrc_t;

#define LATE 1
#define EARLY 1
#define WSOFF 0
#define DLY0 16
#define DLYF 40
#define FSTART 7u
#define DLY2 0
#define DLY3 40
#define DLY3V 0
#define DLY4 50
#define RETRY 1
#define RETRY1 4
#define WF 4
#define WS 0
#define PRIO 2
#define HIDSTART 24u
#define EGATE 1
#define WV 2
#ifndef NV
#define NV 20
#endif
#define GISSUE 0
#define HSHIFT 3

constexpr int WS_EPOCH = 0;
constexpr int WS_FLAG = 4096;
constexpr int WS_H0 = WS_FLAG + 16384;
constexpr int WS_H1 = WS_H0 + 65536;
constexpr int WS_P = WS_H1 + 32768;
constexpr int WS_PS = WS_P + 262144;
constexpr int WS_H2 = WS_PS + 32768;
constexpr int WS_H2S = WS_H2 + 524288;
constexpr int WS_H4 = WS_H2S + 4096;
constexpr int WS_HID = WS_H4 + 28 * 8192;
constexpr int WS_GP = WS_HID + 98 * 512;
constexpr int WS_END = WS_GP + 14 * 1792;
constexpr int WS_DIAG = 4 << 20;
static_assert(WS_END - WS_H1 <= 128 * 9 * 1024, "nine 1 KB pieces per filling workgroup");

constexpr int L_PART = 34 * 4096;
constexpr int L_XN = L_PART + 16384;
constexpr int L_Q = L_XN + 1024;
constexpr int L_CTX = L_Q + 512;
constexpr int L_HID = L_CTX + 128;
constexpr int L_ST4 = L_HID + 256;
constexpr int L_ST5 = L_ST4 + 512;
constexpr int L_X = L_ST5 + 128;
constexpr int L_E = L_X + 1024;
constexpr int L_CTL = L_E + 16;
constexpr int L_END = L_CTL + 512;
constexpr int L_S2P = L_PART;
enum { C_PROG = 0  , C_X = 2, C_FLOK = 3, C_PPUB = 4  , C_FILL = 8  , C_QX = 20  , C_QH = 24  , C_RELQ = 28  ,
       C_RELR = 32  , C_RELC = 36  , C_P3 = 40  , C_P4 = 48  , C_CG = 56  , C_G2 = 64  , C_CTX = 72  , C_S4 = 76  , C_NUM = 80 };

constexpr int NWAVE = 15;
constexpr unsigned SENT = 0xFFFFFFFFu;
constexpr int AUX_SC1 = 16;
constexpr unsigned SPIN_G = 1u << 15;

struct Params {
    const void* x; const void* ckv; const void* wq; const void* wuk; const void* wuv; const void* wo;
    const void* w1; const void* b1; const void* w2; const void* b2; void* out; void* ws;
};

__device__ __forceinline__ float u2f(unsigned u) { return __builtin_bit_cast(float, u); }
__device__ __forceinline__ unsigned f2u(float f) { return __builtin_bit_cast(unsigned, f); }
__device__ __forceinline__ unsigned pk_bf16(float a, float b) { f32x2 v = {a, b}; bf16x2 r = __builtin_convertvector(v, bf16x2); return __builtin_bit_cast(unsigned, r); }
__device__ __forceinline__ float rbf(float a) { return u2f(pk_bf16(a, a) << 16); }
__device__ __forceinline__ float bflo(unsigned u) { return u2f(u << 16); }
__device__ __forceinline__ float bfhi(unsigned u) { return u2f(u & 0xFFFF0000u); }
__device__ __forceinline__ unsigned san(unsigned u) { return u < 0xFFFFFFFEu ? u : 0xFFFFFFFEu; }
__device__ __forceinline__ v4u san4(v4u v) { return v4u{san(v.x), san(v.y), san(v.z), san(v.w)}; }
__device__ __forceinline__ v4u f4u(f32x4 v) { return __builtin_bit_cast(v4u, v); }
__device__ __forceinline__ f32x4 u4f(v4u v) { return __builtin_bit_cast(f32x4, v); }
__device__ __forceinline__ unsigned umax(unsigned a, unsigned b) { return a > b ? a : b; }
__device__ __forceinline__ unsigned umax4(v4u v) { return umax(umax(v.x, v.y), umax(v.z, v.w)); }
__device__ __forceinline__ unsigned umax4a(unsigned m, v4u v) { return umax(umax(umax(umax(m, v.x), v.y), v.z), v.w); }
__device__ __forceinline__ float fmx(float a, float b) { return __builtin_amdgcn_fmed3f(a, b, __builtin_inff()); }
__device__ __forceinline__ f32x4 mfma(v4u a, v4u b, f32x4 c) {
    return __builtin_amdgcn_mfma_f32_16x16x32_bf16(__builtin_bit_cast(bf16x8, a), __builtin_bit_cast(bf16x8, b), c, 0, 0, 0);
}
template <int N> __device__ __forceinline__ float ror(float v) { return u2f((unsigned)__builtin_amdgcn_update_dpp(0, (int)f2u(v), 0x120 + N, 0xf, 0xf, false)); }
__device__ __forceinline__ float row_sum(float v) { v += ror<8>(v); v += ror<4>(v); v += ror<2>(v); v += ror<1>(v); return v; }
__device__ __forceinline__ float row_max(float v) { v = fmx(v, ror<8>(v)); v = fmx(v, ror<4>(v)); v = fmx(v, ror<2>(v)); v = fmx(v, ror<1>(v)); return v; }
__device__ __forceinline__ float xrow_sum(float v) {
    auto a = __builtin_amdgcn_permlane32_swap(f2u(v), f2u(v), false, false); v = u2f(a[0]) + u2f(a[1]);
    auto b = __builtin_amdgcn_permlane16_swap(f2u(v), f2u(v), false, false); return u2f(b[0]) + u2f(b[1]);
}
__device__ __forceinline__ float xrow_max(float v) {
    auto a = __builtin_amdgcn_permlane32_swap(f2u(v), f2u(v), false, false); v = fmx(u2f(a[0]), u2f(a[1]));
    auto b = __builtin_amdgcn_permlane16_swap(f2u(v), f2u(v), false, false); return fmx(u2f(b[0]), u2f(b[1]));
}
__device__ __forceinline__ float wave_sum(float v) { return xrow_sum(row_sum(v)); }
__device__ __forceinline__ float wave_sum4(f32x4 v) {
    auto a = __builtin_amdgcn_permlane32_swap(f2u(v.x), f2u(v.y), false, false); const float xy = u2f(a[0]) + u2f(a[1]);
    auto b = __builtin_amdgcn_permlane32_swap(f2u(v.z), f2u(v.w), false, false); const float zw = u2f(b[0]) + u2f(b[1]);
    auto c = __builtin_amdgcn_permlane16_swap(f2u(xy), f2u(zw), false, false);
    return row_sum(u2f(c[0]) + u2f(c[1]));
}

__device__ __forceinline__ float sel4(int q, const f32x4 (&a)[4]) { const float x0 = a[0].x, x1 = a[1].x, x2 = a[2].x, x3 = a[3].x; return q == 0 ? x0 : q == 1 ? x1 : q == 2 ? x2 : x3; }
__device__ __forceinline__ float quad_sum4(f32x4 v) {
    auto a = __builtin_amdgcn_permlane32_swap(f2u(v.x), f2u(v.y), false, false); const float xy = u2f(a[0]) + u2f(a[1]);
    auto b = __builtin_amdgcn_permlane32_swap(f2u(v.z), f2u(v.w), false, false); const float zw = u2f(b[0]) + u2f(b[1]);
    auto c = __builtin_amdgcn_permlane16_swap(f2u(xy), f2u(zw), false, false);
    float r = u2f(c[0]) + u2f(c[1]); r += ror<8>(r); r += ror<4>(r); return r;
}
__device__ __forceinline__ rsrc_t mk_rsrc(const void* p, int bytes) { return __builtin_amdgcn_make_buffer_rsrc((void*)p, 0, bytes, 0x00020000); }
__device__ __forceinline__ v4u ld16(rsrc_t r, int off) { return __builtin_amdgcn_raw_buffer_load_b128(r, off, 0, AUX_SC1); }
__device__ __forceinline__ v2u ld8(rsrc_t r, int off) { return __builtin_amdgcn_raw_buffer_load_b64(r, off, 0, AUX_SC1); }
__device__ __forceinline__ unsigned ld4(rsrc_t r, int off) { return __builtin_amdgcn_raw_buffer_load_b32(r, off, 0, AUX_SC1); }
__device__ __forceinline__ void st16(rsrc_t r, int off, v4u v) { __builtin_amdgcn_raw_buffer_store_b128(v, r, off, 0, AUX_SC1); }
__device__ __forceinline__ void st4(rsrc_t r, int off, unsigned v) { __builtin_amdgcn_raw_buffer_store_b32(v, r, off, 0, AUX_SC1); }
__device__ __forceinline__ void st8(rsrc_t r, int off, v2u v) { __builtin_amdgcn_raw_buffer_store_b64(v, r, off, 0, AUX_SC1); }
constexpr int OOB = 0x40000000;
#define CBAR() asm volatile("" ::: "memory")
#define SB() __builtin_amdgcn_sched_barrier(0)
#define HI() __builtin_amdgcn_s_setprio(3)
#define PIN(x) asm volatile("" : "+v"(x))
#define DRAIN() asm volatile("s_waitcnt vmcnt(0)" ::: "memory")
#define LWAIT() asm volatile("s_waitcnt lgkmcnt(0)" ::: "memory")

typedef LAS unsigned char* lds_t;
__device__ __forceinline__ v4u lds16(int off) { return *(LAS v4u*)off; }
__device__ __forceinline__ f32x4 lds16f(int off) { return *(LAS f32x4*)off; }
template <int D2> __device__ __forceinline__ v4u lds_tr(int off) {
    s16x4 a = __builtin_amdgcn_ds_read_tr16_b64_v4i16((LAS s16x4*)off);
    s16x4 b = __builtin_amdgcn_ds_read_tr16_b64_v4i16((LAS s16x4*)(off + D2));
    v2u a2 = __builtin_bit_cast(v2u, a), b2 = __builtin_bit_cast(v2u, b);
    return v4u{a2.x, a2.y, b2.x, b2.y};
}
#define MINSLEEP 0
template <int SLEEP0> __device__ __forceinline__ void wait_lds(int addr, unsigned want) {
    constexpr int SLEEP = SLEEP0 > MINSLEEP ? SLEEP0 : MINSLEEP;
    unsigned v, c;
    if (SLEEP)
        asm volatile("s_mov_b32 %1, 0x40000\n1:\n\tds_read_b32 %0, %2\n\ts_sub_u32 %1, %1, 1\n\ts_waitcnt lgkmcnt(0)\n\tv_cmp_gt_u32 vcc, %3, %0\n\t"
                     "s_cbranch_scc1 2f\n\ts_cbranch_vccz 2f\n\ts_sleep %4\n\ts_branch 1b\n2:"
                     : "=&v"(v), "=&s"(c) : "v"(addr), "v"(want), "i"(SLEEP) : "memory", "vcc", "scc");
    else
        asm volatile("s_mov_b32 %1, 0x80000\n1:\n\tds_read_b32 %0, %2\n\ts_sub_u32 %1, %1, 1\n\ts_waitcnt lgkmcnt(0)\n\tv_cmp_gt_u32 vcc, %3, %0\n\t"
                     "s_cbranch_scc1 2f\n\ts_cbranch_vccnz 1b\n2:"
                     : "=&v"(v), "=&s"(c) : "v"(addr), "v"(want) : "memory", "vcc", "scc");
}
#define CW(word) (L_CTL + 4 * (word))
#define WAITP(S, word, want) wait_lds<S>(CW(word), (want))
#define WAITF(S, word, n) wait_lds<S>(CW(word) + 4 * (lane & ((n) - 1)), 1u)
__device__ __forceinline__ void sig(int addr, unsigned v) { asm volatile("ds_write_b32 %0, %1" :: "v"(addr), "v"(v) : "memory"); }
__device__ __forceinline__ bool any_sent(unsigned m) { return __builtin_amdgcn_ballot_w64(m == SENT) != 0ull; }
__device__ __forceinline__ bool any_nz(unsigned m) { return __builtin_amdgcn_ballot_w64(m != 0u) != 0ull; }

#define STAMP(k) do { if ((k) == PKEY && wv == PWAVE && (int)ecu == PECU) pd_ = (unsigned)__builtin_amdgcn_s_memrealtime() - pt0_; } while (0)
#define STAMP_DECL const unsigned pt0_ = (unsigned)__builtin_amdgcn_s_memrealtime(); unsigned pd_ = 0;
#define LSTAMP(n)
#define PASSES(k, s)
#define STAMP_OUT() do { STAMP(15); pd_ = pd_ < 5000u ? pd_ : 5000u; while ((unsigned)__builtin_amdgcn_s_memrealtime() - pt0_ < 10u * pd_) __builtin_amdgcn_s_sleep(8); } while (0)

__device__ __forceinline__ void wait_vm(int n) {
    switch (n) {
#define VMC(x) case x: asm volatile("s_waitcnt vmcnt(" #x ")" ::: "memory"); break;
        VMC(0) VMC(4) VMC(8) VMC(12) VMC(16) VMC(20) VMC(24) VMC(28) VMC(32) VMC(36) VMC(40)
    }
}
__device__ __forceinline__ void dma_tile(rsrc_t r, unsigned soff, unsigned lds, unsigned v0, unsigned v1, unsigned v2, unsigned v3) {
#define POL " nt"
    asm volatile("s_mov_b32 m0, %6\n\ts_nop 0\n\t"
                 "buffer_load_dwordx4 %0, %4, %5 offen" POL " lds\n\t"
                 "buffer_load_dwordx4 %1, %4, %5 offen offset:1024" POL " lds\n\t"
                 "buffer_load_dwordx4 %2, %4, %5 offen offset:2048" POL " lds\n\t"
                 "buffer_load_dwordx4 %3, %4, %5 offen offset:3072" POL " lds"
                 :: "v"(v0), "v"(v1), "v"(v2), "v"(v3), "s"(r), "s"(soff), "s"(lds) : "memory");
}

template <int W>
__device__ __forceinline__ void dma_seg(rsrc_t r, int& n, int end, unsigned ts, unsigned tl, int& vp, int prog, unsigned v0, unsigned v1, unsigned v2, unsigned v3) {
    unsigned so, sl; int t;
    asm volatile("s_nop 1\n\tv_readlane_b32 %[so], %[ts], %[n]\n\tv_readlane_b32 %[sl], %[tl], %[n]\n\ts_nop 3\n"
                 "1:\n\ts_mov_b32 m0, %[sl]\n\ts_add_u32 %[n], %[n], 1\n\t"
                 "buffer_load_dwordx4 %[v0], %[r], %[so] offen" POL " lds\n\t"
                 "buffer_load_dwordx4 %[v1], %[r], %[so] offen offset:1024" POL " lds\n\t"
                 "buffer_load_dwordx4 %[v2], %[r], %[so] offen offset:2048" POL " lds\n\t"
                 "buffer_load_dwordx4 %[v3], %[r], %[so] offen offset:3072" POL " lds\n\t"
                 "v_readlane_b32 %[sl], %[tl], %[n]\n\tv_readlane_b32 %[so], %[ts], %[n]\n\t"
                 "s_sub_u32 %[t], %[n], %[w]\n\tv_max_i32 %[vp], %[t], %[vp]\n\t"
                 "s_waitcnt vmcnt(%[w4])\n\tds_write_b32 %[pa], %[vp]\n\t"
                 "s_cmp_lt_i32 %[n], %[e]\n\ts_cbranch_scc1 1b"
                 : [n] "+s"(n), [vp] "+v"(vp), [so] "=&s"(so), [sl] "=&s"(sl), [t] "=&s"(t)
                 : [r] "s"(r), [e] "s"(end), [ts] "v"(ts), [tl] "v"(tl), [pa] "v"(prog), [v0] "v"(v0), [v1] "v"(v1), [v2] "v"(v2), [v3] "v"(v3), [w] "i"(W), [w4] "i"(4 * W)
                 : "memory", "scc");
}

__global__ void __launch_bounds__(NWAVE * 64, 4) fused_decode(Params p) {
    extern __shared__ __attribute__((aligned(16))) unsigned char smem[];
    const int lane = threadIdx.x & 63, wv = __builtin_amdgcn_readfirstlane(threadIdx.x >> 6);
    const int b = blockIdx.x, jj = (b >> 3) & 15;
    const bool ecu = b < 128;
    const int hh = 2 * (b & 7) + (jj >> 3);
    const int i8 = jj & 7, dq = i8 >> 1, hf = i8 & 1;
    const int e = (b & 7) * 16 + jj;
    const int l15 = lane & 15, q4 = lane >> 4;
#define CSLOT(mm, pq) (((mm) < 12 ? 2 * (mm) + (pq) : (mm) == 12 ? 32 + (pq) : 2 * (mm) - 26 + (pq)) * 4096)
#define KROT ((hh & 1) ? i8 : 3)
#define RHO(mm) (2 * ((i8 - (mm)) & 7) + ((mm) >> 3))
    STAMP_DECL
    if (wv == 2) { *(LAS v2u*)(L_CTL + lane * 8) = v2u{0u, 0u}; LWAIT(); }
    if (wv >= 2) __builtin_amdgcn_s_barrier();
    asm volatile("" :: "s"(p.x), "s"(p.ws), "s"(p.wq) : "memory");

    if (__builtin_expect(wv < 2, 1)) {
        const int pp = wv;
        const unsigned r = lane >> 3, pc = lane & 7, s0 = (r >> 1) & 1;
        const unsigned ce = (pc ^ (s0 << 1)) << 4, co = (pc ^ ((s0 | 2) << 1)) << 4;
#define VOFFS(st) (r * (st) + ce), ((r + 8) * (st) + co - 1024), ((r + 16) * (st) + ce - 2048), ((r + 24) * (st) + co - 3072)
        int n = 0, vp = 0;
        const int prog = CW(C_PROG + pp);
        __builtin_amdgcn_s_setprio(1);
#define DOWN(W) do { wait_vm(4 * (W)); vp = vp > n - (W) ? vp : n - (W); sig(prog, vp); } while (0)
#define BLOCK(addr) do { const int a_ = (addr); _Pragma("unroll") for (int x_ = WF - 1; x_ >= 0; --x_) { if (__builtin_amdgcn_ballot_w64(*(volatile LAS unsigned*)a_ == 0u) == 0ull) break; DOWN(x_); } wait_lds<0>(a_, 1u); } while (0)
#define DRAINW(W, T) do { _Pragma("unroll") for (int x_ = (W) - 1; x_ >= (T); --x_) DOWN(x_); } while (0)
        if (pp == 0)
            asm volatile("s_mov_b32 m0, %3\n\ts_nop 0\n\tbuffer_load_dwordx4 %0, %1, 0 offen lds\n\t"
                         "s_mov_b32 m0, %4\n\ts_mov_b64 exec, 1\n\tbuffer_load_dwordx4 %0, %2, 0 offen lds\n\ts_mov_b64 exec, -1"
                         :: "v"(lane * 16), "s"(mk_rsrc(p.x, ecu ? 896 : 0)), "s"(mk_rsrc(p.ws, 16)), "s"(L_X), "s"(L_E) : "memory");
        if (__builtin_expect(ecu, 1)) {
            const rsrc_t rq = mk_rsrc(p.wq, 448 * 8192);
            const unsigned so = (224 * hf + 32 * pp) * 8192 + (((hh + HSHIFT) & 15) * 256 + dq * 64) * 2;
            dma_tile(rq, so, pp * 4096, VOFFS(8192));
            dma_tile(rq, so + 64 * 8192, (2 + pp) * 4096, VOFFS(8192));
            dma_tile(rq, so + 128 * 8192, (4 + pp) * 4096, VOFFS(8192));
            dma_tile(mk_rsrc(p.wq, pp ? 0 : 448 * 8192), so + 192 * 8192, (6 + pp) * 4096, VOFFS(8192));
            __builtin_amdgcn_s_barrier();
            SB();
            const int e2 = e - 30;
            const int k = lane - 4, mm = lane - 16;
            unsigned ts = (128 * i8 + 32 * pp + (k & 1) * 64) * 8192 + hh * 512 + (((k >> 1) + KROT) & 3) * 128, tl = (2 * lane + pp) * 4096;
            if (lane >= 12) ts = e < 98 ? (64 * (e % 7) + 32 * pp) * 3584 + (e / 7) * 256 + (lane & 1) * 128 : 0;
            if (lane >= 14) ts = e2 >= 0 ? (128 * (e2 / 7) + 32 * pp + (lane & 1) * 64) * 896 + (e2 % 7) * 128 : 0;
            if (lane >= 16) { ts = (512 * hh + 32 * RHO(mm)) * 2048 + (2 * i8 + 1 - pp) * 128; tl = CSLOT(mm, pp); }
            if (pp == 0) { asm volatile("s_waitcnt vmcnt(16)" ::: "memory"); sig(CW(C_X), 1u); }
            n = 4;
            dma_seg<WF>(mk_rsrc(p.wuk, EARLY * 1024 * 8192), n, 12, ts, tl, vp, prog, VOFFS(8192));
            STAMP(1);
            BLOCK(EGATE ? CW(C_PPUB) : CW(C_QH) + 4 * (lane & 7));
            STAMP(2);
            dma_seg<WF>(mk_rsrc(p.w1, e < 98 ? LATE * 448 * 3584 : 0), n, 14, ts, tl, vp, prog, VOFFS(3584));
            dma_seg<WF>(mk_rsrc(p.w2, e2 >= 0 ? LATE * 1792 * 896 : 0), n, 16, ts, tl, vp, prog, VOFFS(896));
            const rsrc_t rc = mk_rsrc(p.ckv, EARLY * 8192 * 2048);
            dma_seg<WF>(rc, n, 29, ts, tl, vp, prog, VOFFS(2048));
            STAMP(3);
            WAITF(0, C_RELC, 2);
            dma_seg<WF>(rc, n, 32, ts, tl, vp, prog, VOFFS(2048));
        } else {
            __builtin_amdgcn_s_barrier();
            int cb = lane - 24 + 4 * hf; if (cb >= 7) cb -= 7;
            unsigned ts = (512 * hh + 64 * i8 + (lane >> 3) * 32) * 2048 + ((2 * i8 + 2 * lane + pp) & 15) * 128, tl = 2 * lane + pp;
            if (lane >= 16) { ts = (512 * hf + 32 * pp + (lane - 16) * 64) * 8192 + (hh * 256 + dq * 64) * 2; tl = 2 * lane + pp - 32; }
            if (lane >= 24) { ts = (hh * 256 + dq * 64 + 32 * pp) * 896 + cb * 128; tl = lane == 24 ? 32 + pp : 2 * lane + pp - 34; }
            tl *= 4096;
            const rsrc_t rc = mk_rsrc(p.ckv, EARLY * 8192 * 2048);
            if (pp == 0) { DRAIN(); sig(CW(C_X), 1u); }
            wait_lds<0>(CW(C_FILL) + 4 * (lane < 9 ? lane : 0), 1u);
            if (NSLOW) dma_seg<WS>(rc, n, NSLOW, ts, tl, vp, prog, VOFFS(2048));
            if (RGATE) { dma_seg<WF>(rc, n, RGATE, ts, tl, vp, prog, VOFFS(2048)); STAMP(5); BLOCK(CW(C_QH) + 4 * (lane & 3)); STAMP(6); }
            if (RGATE < 16) dma_seg<WF>(rc, n, 16, ts, tl, vp, prog, VOFFS(2048));
            STAMP(1);
            BLOCK(CW(C_RELR) + 4 * (lane & 3));
            dma_seg<WV>(mk_rsrc(p.wuv, LATE * 1024 * 8192), n, NV, ts, tl, vp, prog, VOFFS(8192));
            dma_seg<WF>(mk_rsrc(p.wuv, LATE * 1024 * 8192), n, 24, ts, tl, vp, prog, VOFFS(8192));
            STAMP(2);
            const rsrc_t ro = mk_rsrc(p.wo, LATE * 4096 * 896);
            dma_seg<WF>(ro, n, 25, ts, tl, vp, prog, VOFFS(896));
            BLOCK(CW(C_P3 + 4) + 4 * (lane & 3));
            dma_seg<WF>(ro, n, 31, ts, tl, vp, prog, VOFFS(896));
        }
        STAMP(4);
        DRAINW(WF, 0);
        STAMP_OUT();
        return;
    }

    const rsrc_t ws = mk_rsrc(p.ws, WS_END);
    const v4u S4 = {SENT, SENT, SENT, SENT};
    const f32x4 Z4 = {0.f, 0.f, 0.f, 0.f};
    const int swr = ((l15 >> 1) & 1) | (((l15 >> 3) & 1) << 1);
    const int row_a0 = l15 * 128 + ((q4 ^ (swr << 1)) << 4), row_a1 = row_a0 ^ 64;
    const int swt = ((l15 >> 3) & 1) | ((q4 & 1) << 1);
    const int tr_b = (8 * q4 + (l15 >> 2)) * 128 + (((l15 >> 1) & 1) << 4) + 8 * (l15 & 1);
#define TR_A(nt) (tr_b + ((((nt) ^ swt)) << 5))
#define WAITT(S, k) wait_lds<S>(CW(C_PROG) + 4 * (lane & 1), (k))

    if (!ecu && wv < 11) st16(ws, WS_H1 + ((wv - 2) * 128 + e) * 1024 + lane * 16, S4);
    if (!ecu && wv < 11) { DRAIN(); sig(CW(C_FILL + wv - 2), 1u); }

    if (wv == 6) {
        unsigned tv = 0;
        {
            const int e2 = e - 30;
            const GAS char* a = (const GAS char*)p.ckv + (512 * hh + 64 * i8 + (ecu ? 63 : 0)) * 2048 + 2 * i8 * 128;
            if (ecu) {
                if (lane == 1 && e < 98) a = (const GAS char*)p.w1 + 64 * (e % 7) * 3584 + (e / 7) * 256;
                if (lane == 2 && e2 >= 0) a = (const GAS char*)p.w2 + 128 * (e2 / 7) * 896 + (e2 % 7) * 128;
            } else {
                if (lane == 1 || lane == 2) a = (const GAS char*)p.wuv + (512 * hf + 256 * (lane - 1)) * 8192 + (hh * 256 + dq * 64) * 2;
                if (lane == 3) a = (const GAS char*)p.wuv + (512 * hf + 511) * 8192 + (hh * 256 + dq * 64) * 2;
                if (lane == 4) a = (const GAS char*)p.wo + (hh * 256 + dq * 64) * 896 + 4 * hf * 128;
            }
            if (lane < 5) tv = __builtin_nontemporal_load((const GAS unsigned*)a);
        }
        wait_lds<0>((!ecu && lane < 9) ? CW(C_FILL + lane) : CW(C_X), 1u);
        const v2u ep = *(LAS v2u*)(L_E);
        const unsigned tagA = ep.x + 1u, tagB = ep.y | 1u;
        if (!ecu) st16(ws, lane < 8 ? WS_FLAG + e * 128 + lane * 16 : OOB, v4u{tagA, tagB, tagA, tagB});
        unsigned tb = 0;
        if (!ecu && jj < 2) {
            const GAS char* a = lane < 28 ? (const GAS char*)p.b1 + lane * 128 : lane < 35 ? (const GAS char*)p.b2 + (lane - 28) * 128 : (const GAS char*)p.out;
            if (lane < 36) tb = *(const GAS unsigned*)a;
        }
        const int u = e < 28 ? e : -1;
        const int col = 16 * (u >= 0 ? u : 0) + 4 * (lane & 3) + 2 * (q4 & 1) + (q4 >> 1);
        const unsigned xv = *(LAS u16*)(L_X + col * 2);
        STAMP(1);
        if (ecu) WAITT(2, FSTART); else __builtin_amdgcn_s_sleep(DLYF);
        HI();
#pragma unroll 1
        for (unsigned s = 0; s < SPIN_G; ++s) { PASSES(7, s);
            CBAR();
            unsigned m = 0;
#pragma unroll
            for (int k = 0; k < 4; ++k) m |= ld4(ws, WS_FLAG + k * 4096 + (lane >> 1) * 128 + (lane & 1) * 4) ^ ((lane & 1) ? tagB : tagA);
            if (!any_nz(m)) break;
            __builtin_amdgcn_s_sleep(4);
        }
        asm volatile("" :: "v"(tv), "v"(tb));
        sig(CW(C_FLOK), 1u);
        STAMP(2);
        if (ecu) {
            const int e2 = e - 30, fs2 = e2 / 7, mt2 = e2 % 7;
            unsigned b1a = 0, b1b = 0, b2v = 0;
            WAITF(2, C_RELQ, 4);
            if (e2 >= 0) { b1a = ((const GAS u16*)p.b1)[128 * fs2 + lane]; b1b = ((const GAS u16*)p.b1)[128 * fs2 + 64 + lane]; }
            if (u >= 0) b2v = ((const GAS u16*)p.b2)[col];
            if (e < 98) {
                const int ms1 = e % 7;
                const v4u bx0 = lds16(L_XN + (64 * ms1 + 8 * q4) * 2), bx1 = lds16(L_XN + (64 * ms1 + 32 + 8 * q4) * 2);
                WAITT(2, 14u);
#pragma unroll
                for (int cb = 0; cb < 2; ++cb) {
                    f32x4 a[4];
#pragma unroll
                    for (int nt = 0; nt < 4; ++nt) {
                        a[nt] = mfma(bx0, lds_tr<512>((24 + 2 * cb) * 4096 + TR_A(nt)), Z4);
                        a[nt] = mfma(bx1, lds_tr<512>((25 + 2 * cb) * 4096 + TR_A(nt)), a[nt]);
                    }
                    st4(ws, WS_HID + e * 512 + cb * 256 + lane * 4, san(f2u(sel4(q4, a))));
                }
            }
            STAMP(3);
            if (e2 >= 0) {
                unsigned hp[14];
                WAITT(2, HIDSTART);
#pragma unroll 1
                for (unsigned s = 0; s < SPIN_G; ++s) { PASSES(2, s);
                    CBAR();
                    unsigned m = 0;
#pragma unroll
                    for (int k = 0; k < 14; ++k) hp[k] = ld4(ws, WS_HID + fs2 * 3584 + k * 256 + lane * 4);
#pragma unroll
                    for (int k = 0; k < 14; ++k) m = umax(m, hp[k]);
                    if (!any_sent(m)) break;
                    __builtin_amdgcn_s_sleep(4);
                }
                float ha = 0.f, hb = 0.f;
#pragma unroll
                for (int k = 0; k < 7; ++k) { ha += u2f(hp[2 * k]); hb += u2f(hp[2 * k + 1]); }
                ha = rbf(rbf(ha) + bflo(b1a)); hb = rbf(rbf(hb) + bflo(b1b));
                ha = ha > 0.f ? ha : 0.f; hb = hb > 0.f ? hb : 0.f;
                const unsigned hq = pk_bf16(ha * ha, hb * hb);
                *(LAS u16*)(L_HID + lane * 2) = (u16)hq; *(LAS u16*)(L_HID + 128 + lane * 2) = (u16)(hq >> 16);
                CBAR();
                STAMP(4);
                f32x4 a[4] = {Z4, Z4, Z4, Z4};
#pragma unroll
                for (int k = 0; k < 4; ++k) {
                    const v4u bh = lds16(L_HID + k * 64 + q4 * 16);
#pragma unroll
                    for (int nt = 0; nt < 4; ++nt) a[nt] = mfma(bh, lds_tr<512>((28 + k) * 4096 + TR_A(nt)), a[nt]);
                }
                st4(ws, WS_GP + fs2 * 1792 + (64 * mt2 + lane) * 4, san(f2u(sel4(q4, a))));
            }
            STAMP(5);
            if (u >= 0) {
                v4u gv;
                WAITF(2, C_CG, 8);
#pragma unroll 1
                for (unsigned s = 0; s < SPIN_G; ++s) { PASSES(3, s);
                    CBAR();
                    gv = ld16(ws, lane < 56 ? WS_GP + (lane >> 2) * 1792 + u * 64 + (lane & 3) * 16 : OOB);
                    if (!any_sent(umax4(gv))) break;
                    __builtin_amdgcn_s_sleep(4);
                }
                float xf = bflo(xv);
                const float ga = rbf(rbf(quad_sum4(u4f(gv))) + bflo(b2v));
                float g = rbf(ga / (1.f + __expf(-ga)));
                int o_s = WS_H4 + u * 8192 + lane * 16;
                int o_o = l15 < 4 ? col * 2 : OOB;
                const rsrc_t ro = mk_rsrc(p.out, 896);
                PIN(g); PIN(xf); PIN(o_s); PIN(o_o);
                v4u sv[8];
                STAMP(6);
                WAITF(2, C_S4, 4);
                __builtin_amdgcn_s_sleep(DLY4);
#pragma unroll 1
                for (unsigned s = 0; s < SPIN_G; ++s) { PASSES(4, s);
                    CBAR();
#pragma unroll
                    for (int k = 0; k < 8; ++k) sv[k] = __builtin_amdgcn_raw_buffer_load_b128(ws, o_s + (k & 3) * 1024, (k >> 2) * 4096, AUX_SC1);
                    unsigned m = 0, m2 = 0;
#pragma unroll
                    for (int k = 0; k < 4; ++k) { m = umax4a(m, sv[2 * k]); m2 = umax4a(m2, sv[2 * k + 1]); }
                    if (!any_sent(umax(m, m2))) break;
                }
                STAMP(8);
                const float s = quad_sum4(((u4f(sv[0]) + u4f(sv[1])) + (u4f(sv[2]) + u4f(sv[3]))) + ((u4f(sv[4]) + u4f(sv[5])) + (u4f(sv[6]) + u4f(sv[7]))));
                const float o = xf + rbf(rbf(s) * g);
                __builtin_amdgcn_raw_buffer_store_b16((short)pk_bf16(o, o), ro, o_o, 0, 0);
                if (u == 0) st8(ws, lane == 0 ? WS_EPOCH : OOB, v2u{tagA, ep.y});
            }
        }
        STAMP_OUT();
        return;
    }

    if (ecu) {
        if (wv < 6) {
            const int t = wv - 2;
            int o_h0 = lane < 8 ? WS_H0 + ((((hh + HSHIFT) & 15) * 4 + dq) * 2 + hf) * 512 + t * 128 + lane * 16 : OOB;
            int o_h1 = lane < 16 ? WS_H1 + ((2 * i8 + (lane >> 3)) * 16 + hh) * 128 + (lane & 7) * 16 : OOB;
            st16(ws, o_h0, v4u{0u, 0u, 0u, 0u});
            int tra = TR_A(t);
            int a_bx = L_XN + (224 * hf + 8 * q4) * 2;
            int o_g0 = WS_H0 + (hh * 4 + t) * 1024 + lane * 16;
            PIN(tra); PIN(a_bx); PIN(o_h0); PIN(o_g0);
            WAITP(0, C_X, 1u);
            const v2u ep = *(LAS v2u*)(L_E);
            {
                const v4u xv = lds16(L_X + lane * 16);
                float ev[8] = {bflo(xv.x), bfhi(xv.x), bflo(xv.y), bfhi(xv.y), bflo(xv.z), bfhi(xv.z), bflo(xv.w), bfhi(xv.w)};
                const float s = ((ev[0] + ev[1]) + (ev[2] + ev[3])) + ((ev[4] + ev[5]) + (ev[6] + ev[7]));
                const float mean = wave_sum(s) * (1.0f / 448.0f);
                float v = 0.f;
#pragma unroll
                for (int k = 0; k < 8; ++k) { const float d = ev[k] - mean; v += d * d; }
                v = lane < 56 ? v : 0.f;
                const float var = wave_sum(v) * (1.0f / 448.0f);
                const float mb = rbf(mean);
                const float rs = 1.0f / rbf(__builtin_amdgcn_sqrtf(rbf(rbf(var) + 1e-5f)));
                float o[8];
#pragma unroll
                for (int k = 0; k < 8; ++k) o[k] = rbf(ev[k] - mb) * rs;
                *(LAS v4u*)(L_XN + lane * 16) = v4u{pk_bf16(o[0], o[1]), pk_bf16(o[2], o[3]), pk_bf16(o[4], o[5]), pk_bf16(o[6], o[7])};
                CBAR();
            }
            const unsigned tagA = ep.x + 1u, tagB = ep.y | 1u;
            {
                v4u bx[7], a[7];
#pragma unroll
                for (int s = 0; s < 7; ++s) bx[s] = lds16(a_bx + 64 * s);
                STAMP(1);
                wait_lds<0>(CW(C_PROG) + 4 * (lane & 1), (lane & 1) ? 3u : 4u);
                HI();
                STAMP(2);
#pragma unroll
                for (int s = 0; s < 7; ++s) a[s] = lds_tr<512>(tra + s * 4096);
                SB();
                f32x4 acc = mfma(bx[0], a[0], Z4), acc2 = mfma(bx[1], a[1], Z4);
                acc = mfma(bx[2], a[2], acc); acc2 = mfma(bx[3], a[3], acc2); acc = mfma(bx[4], a[4], acc); acc2 = mfma(bx[5], a[5], acc2); acc = mfma(bx[6], a[6], acc);
                const float qv = acc.x + acc2.x;
                st16(ws, o_h0, v4u{f2u(qv), tagA, f2u(ror<8>(qv)), tagB});
            }
            STAMP(3);
            __builtin_amdgcn_s_sleep(DLY0);
            int a_qw = L_Q + t * 128 + ((lane >> 3) & 3) * 32 + (lane & 7) * 2, a_qr = L_Q + t * 128 + q4 * 16;
            int a_k0 = (8 + 4 * ((t - KROT) & 3)) * 4096 + row_a0, a_k1 = a_k0 ^ 64;
            int a_pw = L_S2P + t * 512 + lane * 4;
            int a_pr = L_S2P + l15 * 32;
            PIN(a_qw); PIN(a_qr); PIN(a_k0); PIN(a_k1); PIN(o_h1); PIN(a_pw); PIN(a_pr);
            HI();
            v4u g0 = ld16(ws, o_g0);
            v4u ak[16];
            WAITT(0, 6u + 2 * ((t - KROT) & 3));
#pragma unroll
            for (int k = 0; k < 8; ++k) { ak[2 * k] = lds16(a_k0 + k * 2048); ak[2 * k + 1] = lds16(a_k1 + k * 2048); }
            sig(CW(C_RELQ + t), 1u);
#pragma unroll 1
            for (unsigned s = 0; s < SPIN_G; ++s) { PASSES(0, s);
                if (!any_nz((g0.y ^ tagA) | (g0.w ^ tagB))) break;
                CBAR();
                g0 = ld16(ws, o_g0);
            }
            sig(CW(C_QH + t), 1u);
            STAMP(4);
            {
                auto ra = __builtin_amdgcn_permlane32_swap(g0.x, g0.x, false, false);
                auto rb = __builtin_amdgcn_permlane32_swap(g0.z, g0.z, false, false);
                const unsigned qq = pk_bf16(u2f(ra[0]) + u2f(ra[1]), u2f(rb[0]) + u2f(rb[1]));
                *(LAS u16*)(a_qw) = (u16)qq; *(LAS u16*)(a_qw + 16) = (u16)(qq >> 16);
                CBAR();
                const v4u bq0 = lds16(a_qr), bq1 = lds16(a_qr + 64);
                f32x4 a[4], a2[4];
#pragma unroll
                for (int k = 0; k < 4; ++k) a[k] = mfma(bq0, ak[2 * k], Z4);
#pragma unroll
                for (int k = 0; k < 4; ++k) a[k] = mfma(bq1, ak[2 * k + 1], a[k]);
#pragma unroll
                for (int k = 0; k < 4; ++k) a2[k] = mfma(bq0, ak[8 + 2 * k], Z4);
#pragma unroll
                for (int k = 0; k < 4; ++k) a2[k] = mfma(bq1, ak[9 + 2 * k], a2[k]);
                *(LAS float*)(a_pw) = sel4(q4, a);
                *(LAS float*)(a_pw + 256) = sel4(q4, a2);
                sig(CW(C_QX + t), 1u);
            }
            STAMP(5);
            if (t == 0) {
                wait_lds<0>(lane < 4 ? CW(C_QX + lane) : CW(C_FLOK), 1u);
                f32x4 v0[4], v1[4];
#pragma unroll
                for (int k = 0; k < 4; ++k) { v0[k] = lds16f(a_pr + k * 512); v1[k] = lds16f(a_pr + k * 512 + 16); }
                SB();
                const float sc = 0.0625f * 1.4426950408889634f;
                const f32x4 x0 = ((v0[0] + v0[1]) + (v0[2] + v0[3])) * sc, x1 = ((v1[0] + v1[1]) + (v1[2] + v1[3])) * sc;
                st16(ws, o_h1, san4(v4u{pk_bf16(x0.x, x0.y), pk_bf16(x0.z, x0.w), pk_bf16(x1.x, x1.y), pk_bf16(x1.z, x1.w)}));
                sig(CW(C_PPUB), 1u);
            }
            STAMP_OUT();
            return;
        }
        const int pp = (wv - 7) & 1, kq = (wv - 7) >> 1;
        v4u ac[16];
        {
            const int rr = 4 * q4 + (l15 >> 2);
            const int sc_ = ((rr >> 1) & 1) | (((rr >> 3) & 1) << 1);
            const int tb = rr * 128 + (((l15 >> 1) & 1) << 4) + 8 * (l15 & 1);
            wait_lds<1>(lane & 1 ? CW(C_PROG + pp) : CW(C_FLOK), lane & 1 ? 20u + 4 * kq : 1u);
            STAMP(1);
#pragma unroll
            for (int u = 0; u < 4; ++u) {
                const int cbase = CSLOT(4 * kq + u, pp);
#pragma unroll
                for (int nt = 0; nt < 4; ++nt) ac[4 * u + nt] = lds_tr<2048>(cbase + tb + ((nt ^ sc_) << 5));
            }
            if (kq == 0) sig(CW(C_RELC + pp), 1u);
        }
        int o_p = WS_P + hh * 16384 + lane * 16;
        int o_s = WS_PS + (hh * 16 + RHO(4 * kq + q4)) * 128 + l15 * 4;
        int a_sw = L_ST4 + kq * 128 + l15 * 8;
        int a_p[4];
#pragma unroll
        for (int nt = 0; nt < 4; ++nt) { a_p[nt] = CSLOT(4 * kq + 3, pp) + nt * 1024 + (q4 * 16 + ((l15 + 4 * nt + (q4 & 2)) & 15)) * 16; PIN(a_p[nt]); }
        const int hd = 8 * (kq & 1) + (lane >> 3), ck = lane & 7;
        int o_h2 = WS_H2 + (hd * 2 + (i8 >> 2)) * 16384 + hh * 1024 + (2 * (i8 & 3) + 1 - pp) * 128 + ck * 16;
        int o_h2s = lane < 32 ? WS_H2S + (hh * 2 + (i8 >> 2)) * 128 + lane * 4 : OOB;
        int a_rd = pp * 4096 + (ck >> 1) * 1024 + ((ck & 1) * 32 + ((hd + 2 * ck) & 15)) * 16;
        int a_sr = L_ST4 + (kq < 2 ? hd : l15) * 8;
        PIN(o_p); PIN(o_s); PIN(a_sw); PIN(o_h2); PIN(o_h2s); PIN(a_rd); PIN(a_sr);
        WAITT(1, 32u);
        STAMP(7);
        __builtin_amdgcn_s_sleep(DLY2);
        HI();
        v4u bp[4]; unsigned pr, pl;
        const int so0 = RHO(4 * kq) * 1024, so1 = RHO(4 * kq + 1) * 1024, so2 = RHO(4 * kq + 2) * 1024, so3 = RHO(4 * kq + 3) * 1024;
#pragma unroll 1
        for (unsigned s = 0; s < SPIN_G; ++s) { PASSES(6, s);
            CBAR();
            bp[0] = __builtin_amdgcn_raw_buffer_load_b128(ws, o_p, so0, AUX_SC1); bp[1] = __builtin_amdgcn_raw_buffer_load_b128(ws, o_p, so1, AUX_SC1);
            bp[2] = __builtin_amdgcn_raw_buffer_load_b128(ws, o_p, so2, AUX_SC1); bp[3] = __builtin_amdgcn_raw_buffer_load_b128(ws, o_p, so3, AUX_SC1);
            pr = ld4(ws, o_s); pl = ld4(ws, o_s + 64);
            if (!any_sent(umax(umax4a(umax4a(pr, bp[0]), bp[1]), umax4a(umax4a(pl, bp[2]), bp[3])))) break;
        }
        sig(CW(C_CG + wv - 7), 1u);
        STAMP(8);
        {
            const float rw = xrow_max(u2f(pr));
            const float dl = rw - u2f(pr);
            const unsigned dd = (unsigned)__builtin_amdgcn_fmed3f(dl, 0.f, 255.f) * 0x00800080u;
            auto x = __builtin_amdgcn_permlane32_swap(dd, dd, false, false);
            auto y = __builtin_amdgcn_permlane16_swap(x[0], x[0], false, false);
            auto z = __builtin_amdgcn_permlane16_swap(x[1], x[1], false, false);
            const unsigned du[4] = {y[0], y[1], z[0], z[1]};
            f32x4 c[4] = {Z4, Z4, Z4, Z4};
#pragma unroll
            for (int u = 0; u < 4; ++u) {
                const u16x2 d2 = __builtin_bit_cast(u16x2, du[u]);
                const unsigned x0 = bp[u].x, x1 = bp[u].y, x2 = bp[u].z, x3 = bp[u].w;
                const v4u bs = {__builtin_bit_cast(unsigned, __builtin_elementwise_sub_sat(__builtin_bit_cast(u16x2, x0), d2)),
                                __builtin_bit_cast(unsigned, __builtin_elementwise_sub_sat(__builtin_bit_cast(u16x2, x1), d2)),
                                __builtin_bit_cast(unsigned, __builtin_elementwise_sub_sat(__builtin_bit_cast(u16x2, x2), d2)),
                                __builtin_bit_cast(unsigned, __builtin_elementwise_sub_sat(__builtin_bit_cast(u16x2, x3), d2))};
#pragma unroll
                for (int nt = 0; nt < 4; ++nt) c[nt] = mfma(ac[4 * u + nt], bs, c[nt]);
            }
            const float lw = xrow_sum(__builtin_amdgcn_exp2f(-dl) * u2f(pl));
            *(LAS f32x2*)(a_sw) = f32x2{rw, lw};
#pragma unroll
            for (int nt = 0; nt < 4; ++nt) *(LAS f32x4*)(a_p[nt]) = c[nt];
            sig(CW(C_P4 + pp * 4 + kq), 1u);
        }
        STAMP(9);
        if (kq < 2) {
            WAITF(0, C_P4 + pp * 4, 4);
            STAMP(10);
            f32x4 v0[4], v1[4]; f32x2 sx[4];
#pragma unroll
            for (int k = 0; k < 4; ++k) sx[k] = *(LAS f32x2*)(a_sr + k * 128);
#pragma unroll
            for (int k = 0; k < 4; ++k) { v0[k] = lds16f(a_rd + CSLOT(4 * k + 3, 0)); v1[k] = lds16f(a_rd + CSLOT(4 * k + 3, 0) + 256); }
            SB();
            const float R = fmx(fmx(sx[0].x, sx[1].x), fmx(sx[2].x, sx[3].x));
            float sc[4];
#pragma unroll
            for (int k = 0; k < 4; ++k) sc[k] = __builtin_amdgcn_exp2f(sx[k].x - R);
            const f32x4 a0 = (v0[0] * sc[0] + v0[1] * sc[1]) + (v0[2] * sc[2] + v0[3] * sc[3]);
            const f32x4 a1 = (v1[0] * sc[0] + v1[1] * sc[1]) + (v1[2] * sc[2] + v1[3] * sc[3]);
            st16(ws, o_h2, san4(v4u{pk_bf16(a0.x, a0.y), pk_bf16(a0.z, a0.w), pk_bf16(a1.x, a1.y), pk_bf16(a1.z, a1.w)}));
            sig(CW(C_S4 + 2 * kq + pp), 1u);
        } else if (kq == 2 && pp == 0 && (i8 & 3) == 0) {
            WAITF(0, C_P4, 4);
            f32x2 sx[4];
#pragma unroll
            for (int k = 0; k < 4; ++k) sx[k] = *(LAS f32x2*)(a_sr + k * 128);
            const float R = fmx(fmx(sx[0].x, sx[1].x), fmx(sx[2].x, sx[3].x));
            const float lg = (sx[0].y * __builtin_amdgcn_exp2f(sx[0].x - R) + sx[1].y * __builtin_amdgcn_exp2f(sx[1].x - R))
                           + (sx[2].y * __builtin_amdgcn_exp2f(sx[2].x - R) + sx[3].y * __builtin_amdgcn_exp2f(sx[3].x - R));
            st4(ws, o_h2s, san(f2u(q4 == 0 ? R : lg)));
        }
        STAMP_OUT();
        return;
    }

    const int gw = wv < 6 ? wv - 2 : wv - 7;
#define H2_SETUP() int o_h2 = WS_H2 + (hh * 2 + hf) * 16384 + gw * 2048 + lane * 16; \
        int a_w0 = L_PART + (lane >> 3) * 2048 + gw * 256 + (((lane & 7) ^ (((gw & 1) | ((gw >> 2) << 1)) << 1)) << 4); PIN(o_h2); PIN(a_w0);

    if (wv >= 11) {
        if (wv < 13) {
        const int rb = wv - 11;
        int o_p = WS_P + (hh * 16 + 2 * i8 + rb) * 1024 + lane * 16;
        int o_ps = lane < 32 ? WS_PS + (hh * 16 + 2 * i8 + rb) * 128 + lane * 4 : OOB;
        int a_pr = L_PART + rb * 2048 + lane * 16;
        int a_f = CW(C_P3 + 4 * rb) + 4 * (lane < 3 ? lane : 0);
        PIN(o_p); PIN(o_ps); PIN(a_pr); PIN(a_f);
        WAITT(2, G1START);
        HI();
        f32x4 s0, s1;
        {
            f32x4 v0[3], v1[3];
            wait_lds<0>(a_f, 1u);
#pragma unroll
            for (int k = 0; k < 3; ++k) { v0[k] = lds16f(a_pr + k * 4096); v1[k] = lds16f(a_pr + k * 4096 + 1024); }
            WAITP(0, C_P3 + 4 * rb + 3, 1u);
            STAMP(4);
            const f32x4 x0 = lds16f(a_pr + 3 * 4096), x1 = lds16f(a_pr + 3 * 4096 + 1024);
            SB();
            s0 = ((v0[0] + v0[1]) + v0[2]) + x0;
            s1 = ((v1[0] + v1[1]) + v1[2]) + x1;
        }
        {
            const float mx = xrow_max(fmx(fmx(fmx(s0.x, s0.y), fmx(s0.z, s0.w)), fmx(fmx(s1.x, s1.y), fmx(s1.z, s1.w))));
            const float r = __builtin_ceilf(mx);
            const float p0 = __builtin_amdgcn_exp2f(s0.x - r), p1 = __builtin_amdgcn_exp2f(s0.y - r), p2 = __builtin_amdgcn_exp2f(s0.z - r), p3 = __builtin_amdgcn_exp2f(s0.w - r);
            const float p4 = __builtin_amdgcn_exp2f(s1.x - r), p5 = __builtin_amdgcn_exp2f(s1.y - r), p6 = __builtin_amdgcn_exp2f(s1.z - r), p7 = __builtin_amdgcn_exp2f(s1.w - r);
            st16(ws, o_p, san4(v4u{pk_bf16(p0, p1), pk_bf16(p2, p3), pk_bf16(p4, p5), pk_bf16(p6, p7)}));
            const float l = xrow_sum(((p0 + p1) + (p2 + p3)) + ((p4 + p5) + (p6 + p7)));
            st4(ws, o_ps, san(f2u(q4 == 0 ? r : l)));
            sig(CW(C_PPUB + rb), 1u);
        }
        STAMP(5);
        __builtin_amdgcn_s_setprio(0);
        }
        H2_SETUP();
        WAITF(2, C_PPUB, 2);
        __builtin_amdgcn_s_sleep(DLY3);
        HI();
        v4u g2[2];
#pragma unroll 1
        for (unsigned s = 0; s < SPIN_G; ++s) { PASSES(1, s);
            CBAR();
            g2[0] = ld16(ws, o_h2); g2[1] = ld16(ws, o_h2 + 1024);
            if (!any_sent(umax4a(umax4(g2[0]), g2[1]))) break;
        }
        *(LAS v4u*)(a_w0) = g2[0];
        *(LAS v4u*)(a_w0 + 128) = g2[1];
        sig(CW(C_G2 + gw), 1u);
        STAMP_OUT();
        return;
    }

    if (wv < 6) {
        const int t = wv - 2;
        v4u ak[16];
        WAITT(2, 24u);
        {
            const int tra = TR_A(t);
#pragma unroll
            for (int s = 0; s < 16; ++s) ak[s] = lds_tr<512>(tra + s * 4096);
        }
        H2_SETUP();
        int o_st = lane < 8 ? WS_H2S + ((4 * t + (lane >> 1)) * 2 + hf) * 128 + (lane & 1) * 64 + hh * 4 : OOB;
        int a_sw = lane < 8 ? L_ST5 + (lane & 1) * 64 + (4 * t + (lane >> 1)) * 4 : L_X + lane * 4;
        int a_r0 = L_PART + row_a0, a_r1 = L_PART + row_a1, a_sr = L_ST5 + q4 * 16;
        int a_cw = L_CTX + (16 * t + l15) * 2;
        int a_g2 = CW(C_G2) + 4 * (lane & 7);
        PIN(o_st); PIN(a_sw); PIN(a_r0); PIN(a_r1); PIN(a_sr); PIN(a_cw); PIN(a_g2);
        STAMP(8);
        WAITF(1, C_PPUB, 2);
        __builtin_amdgcn_s_sleep(DLY3V);
        HI();
        STAMP(9);
        {
            v4u g2[2]; unsigned st;
#pragma unroll 1
            for (unsigned s = 0; s < SPIN_G; ++s) { PASSES(1, s);
                CBAR();
                g2[0] = ld16(ws, o_h2); g2[1] = ld16(ws, o_h2 + 1024); st = ld4(ws, o_st);
                if (!any_sent(umax4a(umax4a(st, g2[0]), g2[1]))) break;
            }
            *(LAS v4u*)(a_w0) = g2[0];
            *(LAS v4u*)(a_w0 + 128) = g2[1];
            *(LAS unsigned*)(a_sw) = st;
            sig(CW(C_G2 + gw), 1u);
        }
        STAMP(10);
        wait_lds<0>(a_g2, 1u);
        STAMP(11);
        {
            const f32x4 rg = lds16f(a_sr), lg = lds16f(a_sr + 64);
            f32x4 acc = Z4, acc2 = Z4;
#pragma unroll
            for (int hb = 0; hb < 2; ++hb) {
                v4u bb[8];
#pragma unroll
                for (int k = 0; k < 4; ++k) { bb[2 * k] = lds16(a_r0 + (4 * hb + k) * 2048); bb[2 * k + 1] = lds16(a_r1 + (4 * hb + k) * 2048); }
#pragma unroll
                for (int k = 0; k < 4; ++k) { acc = mfma(bb[2 * k], ak[8 * hb + 2 * k], acc); acc2 = mfma(bb[2 * k + 1], ak[8 * hb + 2 * k + 1], acc2); }
            }
            const float R = xrow_max(fmx(fmx(rg.x, rg.y), fmx(rg.z, rg.w)));
            const f32x4 sc = {__builtin_amdgcn_exp2f(rg.x - R), __builtin_amdgcn_exp2f(rg.y - R), __builtin_amdgcn_exp2f(rg.z - R), __builtin_amdgcn_exp2f(rg.w - R)};
            const float il = __builtin_amdgcn_rcpf(xrow_sum((sc.x * lg.x + sc.y * lg.y) + (sc.z * lg.z + sc.w * lg.w)));
            acc = (acc + acc2) * sc;
            const float cx = xrow_sum((acc.x + acc.y) + (acc.z + acc.w)) * il;
            *(LAS u16*)(a_cw) = (u16)pk_bf16(cx, cx);
            sig(CW(C_CTX + t), 1u);
        }
        STAMP_OUT();
        return;
    }

    {
        const int v = wv - 7;
        v4u ar[16], g[8];
        int a_r0 = 4 * v * 4096 + row_a0, a_r1 = a_r0 ^ 64;
        const int pw = L_PART + v * 4096;
        int o_q = WS_H1 + lane * 16;
        int a_qw = pw + (lane >> 3) * 128 + (((lane & 7) ^ (q4 & 1) << 1) << 4), a_qw1 = (a_qw ^ 64) + 1024;
        int a_q0 = pw + row_a0, a_q1 = pw + row_a1, a_pw = pw + lane * 16;
        int a_poll = lane < 2 ? CW(C_PROG + lane) : CW(C_FLOK);
        PIN(a_r0); PIN(a_r1); PIN(o_q); PIN(a_qw); PIN(a_qw1); PIN(a_q0); PIN(a_q1); PIN(a_pw); PIN(a_poll);
#define PULL_R(rb, k) do { _Pragma("unroll") for (int z = 2 * (k); z < 2 * (k) + 2; ++z) { \
            ar[4 * z] = lds16(a_r0 + (rb) * 65536 + z * 4096); ar[4 * z + 1] = lds16(a_r0 + (rb) * 65536 + z * 4096 + 2048); \
            ar[4 * z + 2] = lds16(a_r1 + (rb) * 65536 + z * 4096); ar[4 * z + 3] = lds16(a_r1 + (rb) * 65536 + z * 4096 + 2048); } } while (0)
#define MM_R(k) do { _Pragma("unroll") for (int z = 2 * (k); z < 2 * (k) + 2; ++z) { \
            s0 = mfma(ar[4 * z], g[2 * z], s0); s1 = mfma(ar[4 * z + 1], g[2 * z], s1); s0 = mfma(ar[4 * z + 2], g[2 * z + 1], s0); s1 = mfma(ar[4 * z + 3], g[2 * z + 1], s1); } } while (0)
        STAMP(1);
        WAITT(1, 2u * v + 2); PULL_R(0, 0); PULL_R(0, 1); sig(CW(C_RELR + v), 1u);
        wait_lds<1>(a_poll, lane < 2 ? G1START : 1u);
        HI();
        {
            const int q0 = ((2 * i8 + 4 * v) & 15) * 2048, q2 = ((2 * i8 + 4 * v + 2) & 15) * 2048;
#pragma unroll 1
            for (unsigned s = 0; s < SPIN_G; ++s) { PASSES(5, s);
                CBAR();
#pragma unroll
                for (int k = 0; k < 8; ++k) g[k] = __builtin_amdgcn_raw_buffer_load_b128(ws, o_q + (k & 3) * 1024, k < 4 ? q0 : q2, AUX_SC1);
                unsigned m = 0, m2 = 0;
#pragma unroll
                for (int k = 0; k < 4; ++k) { m = umax4a(m, g[2 * k]); m2 = umax4a(m2, g[2 * k + 1]); }
                if (!any_sent(umax(m, m2))) break;
                __builtin_amdgcn_s_sleep(RETRY1);
            }
        }
        sig(CW(C_QH + v), 1u);
        STAMP(2);
#pragma unroll
        for (int k = 0; k < 2; ++k) {
            *(LAS v4u*)(a_qw) = g[4 * k]; *(LAS v4u*)(a_qw1) = g[4 * k + 1]; *(LAS v4u*)(a_qw + 2048) = g[4 * k + 2]; *(LAS v4u*)(a_qw1 + 2048) = g[4 * k + 3];
            CBAR();
            g[4 * k] = lds16(a_q0); g[4 * k + 1] = lds16(a_q1); g[4 * k + 2] = lds16(a_q0 + 2048); g[4 * k + 3] = lds16(a_q1 + 2048);
            CBAR();
        }
        STAMP(3);
        {
            f32x4 s0 = Z4, s1 = Z4;
            MM_R(0); MM_R(1);
            *(LAS f32x4*)(a_pw) = s0; *(LAS f32x4*)(a_pw + 1024) = s1;
            sig(CW(C_P3 + v), 1u);
            STAMP(4);
            s0 = Z4; s1 = Z4;
            WAITT(0, 2u * v + 9); PULL_R(1, 0);
            WAITT(0, 2u * v + 10); PULL_R(1, 1);
            SB();
            MM_R(0); MM_R(1);
            *(LAS f32x4*)(a_pw + 2048) = s0; *(LAS f32x4*)(a_pw + 3072) = s1;
            sig(CW(C_P3 + 4 + v), 1u);
        }
        STAMP(5);
        int cb0 = 2 * v + 4 * hf; if (cb0 >= 7) cb0 -= 7;
        int cb1 = cb0 + 1; if (cb1 >= 7) cb1 -= 7;
        int o_h40 = WS_H4 + (4 * cb0 + q4) * 8192 + e * 64 + l15 * 4;
        int o_h41 = v < 3 ? WS_H4 + (4 * cb1 + q4) * 8192 + e * 64 + l15 * 4 : OOB;
        int a_c = L_CTX + q4 * 16, a_cf = CW(C_CTX) + 4 * (lane & 3);
        PIN(a_c); PIN(a_cf); PIN(o_h40); PIN(o_h41);
        __builtin_amdgcn_s_setprio(0);
        WAITT(2, v < 3 ? 26u + 2 * v : 31u);
        {
            const int ob0 = (v == 0 ? 32 : 14 + 4 * v) * 4096, ob2 = (v < 3 ? 16 + 4 * v : 26) * 4096;
#pragma unroll
            for (int nt = 0; nt < 4; ++nt) {
                ar[nt] = lds_tr<512>(ob0 + TR_A(nt)); ar[4 + nt] = lds_tr<512>(ob0 + 4096 + TR_A(nt));
                ar[8 + nt] = lds_tr<512>(ob2 + TR_A(nt)); ar[12 + nt] = lds_tr<512>(ob2 + 4096 + TR_A(nt));
            }
        }
        HI();
        STAMP(12);
        wait_lds<0>(a_cf, 1u);
        STAMP(13);
        {
            const v4u bc0 = lds16(a_c), bc1 = lds16(a_c + 64);
            f32x4 a[4], a2[4];
#pragma unroll
            for (int nt = 0; nt < 4; ++nt) a[nt] = mfma(bc0, ar[nt], Z4);
#pragma unroll
            for (int nt = 0; nt < 4; ++nt) a[nt] = mfma(bc1, ar[4 + nt], a[nt]);
#pragma unroll
            for (int nt = 0; nt < 4; ++nt) a2[nt] = mfma(bc0, ar[8 + nt], Z4);
#pragma unroll
            for (int nt = 0; nt < 4; ++nt) a2[nt] = mfma(bc1, ar[12 + nt], a2[nt]);
            st4(ws, o_h40, san(f2u(sel4(q4, a))));
            st4(ws, o_h41, san(f2u(sel4(q4, a2))));
        }
        STAMP_OUT();
    }
}

extern "C" void kernel_launch(void* const* d_in, const int* in_sizes, int n_in,
                              void* d_out, int out_size, void* d_ws, size_t ws_size,
                              hipStream_t stream) {
    Params p;
    p.x = d_in[0]; p.ckv = d_in[1]; p.wq = d_in[2]; p.wuk = d_in[3]; p.wuv = d_in[4]; p.wo = d_in[5];
    p.w1 = d_in[7]; p.b1 = d_in[8]; p.w2 = d_in[9]; p.b2 = d_in[10]; p.out = d_out; p.ws = (char*)d_ws + (size_t)WSOFF;
    (void)hipFuncSetAttribute((const void*)fused_decode, hipFuncAttributeMaxDynamicSharedMemorySize, L_END);
    fused_decode<<<256, NWAVE * 64, L_END, stream>>>(p);
}
```

```cpp
#include <hip/hip_runtime.h>
#include <hip/hip_bf16.h>
#include <stdint.h>

typedef unsigned short u16;
__device__ __forceinline__ float bf2f(u16 b) { return __builtin_bit_cast(float, (unsigned)b << 16); }
__device__ __forceinline__ float rbf(float f) {
    unsigned u = __builtin_bit_cast(unsigned, f);
    u += 0x7FFFu + ((u >> 16) & 1u);
    return __builtin_bit_cast(float, u & 0xFFFF0000u);
}
__device__ __forceinline__ u16 f2bf(float f) {
    unsigned u = __builtin_bit_cast(unsigned, f);
    u += 0x7FFFu + ((u >> 16) & 1u);
    return (u16)(u >> 16);
}
constexpr int M = 448, H = 16, D = 256, DC = 1024, SK = 8192, F = 1792;

#define MIMIC 0

__device__ void layernorm_to_lds(const u16* x, float* xn, float* red) {
    int t = threadIdx.x;
    float a = 0.f;
    for (int i = t; i < M; i += 256) a += bf2f(x[i]);
    red[t] = a; __syncthreads();
    for (int s = 128; s > 0; s >>= 1) { if (t < s) red[t] += red[t + s]; __syncthreads(); }
    float mean = red[0] / M; __syncthreads();
    a = 0.f;
    for (int i = t; i < M; i += 256) { float d = bf2f(x[i]) - mean; a += d * d; }
    red[t] = a; __syncthreads();
    for (int s = 128; s > 0; s >>= 1) { if (t < s) red[t] += red[t + s]; __syncthreads(); }
    float var = red[0] / M; __syncthreads();
    float sd = sqrtf(var + 1e-5f);
    for (int i = t; i < M; i += 256) xn[i] = (bf2f(x[i]) - mean) / sd;
    __syncthreads();
}

__global__ void k1(const u16* x, const u16* wq, const u16* w1, const u16* b1, float* q, float* hid) {
    __shared__ float xn[M]; __shared__ float red[256];
    layernorm_to_lds(x, xn, red);
    int j = blockIdx.x * 256 + threadIdx.x;
    if (j < H * D) {
        float a = 0.f;
        for (int m = 0; m < M; ++m) a += xn[m] * bf2f(wq[m * (H * D) + j]);
        q[j] = MIMIC ? rbf(a) : a;
    } else {
        int f = j - H * D;
        if (f < F) {
            float a = 0.f;
            for (int m = 0; m < M; ++m) a += xn[m] * bf2f(w1[m * F + f]);
            if (MIMIC) a = rbf(rbf(a) + bf2f(b1[f])); else a = a + bf2f(b1[f]);
            a = a > 0.f ? a : 0.f;
            hid[f] = MIMIC ? rbf(a * a) : a * a;
        }
    }
}
__global__ void k2(const float* q, const u16* wuk, const float* hid, const u16* w2, const u16* b2, float* qabs, float* g) {
    int wave = (blockIdx.x * 256 + threadIdx.x) >> 6, lane = threadIdx.x & 63;
    if (wave < DC * H) {
        int c = wave / H, h = wave % H;
        float a = 0.f;
        for (int d = lane; d < D; d += 64) a += q[h * D + d] * bf2f(wuk[(size_t)c * H * D + h * D + d]);
        for (int s = 32; s > 0; s >>= 1) a += __shfl_xor(a, s);
        if (lane == 0) qabs[h * DC + c] = a * 0.0625f;
    } else {
        int m = wave - DC * H;
        if (m < M) {
            float a = 0.f;
            for (int f = lane; f < F; f += 64) a += hid[f] * bf2f(w2[f * M + m]);
            for (int s = 32; s > 0; s >>= 1) a += __shfl_xor(a, s);
            if (MIMIC) a = rbf(rbf(a) + bf2f(b2[m])); else a += bf2f(b2[m]);
            float sg = a / (1.f + __expf(-a));
            if (lane == 0) g[m] = MIMIC ? rbf(sg) : sg;
        }
    }
}
__global__ void __launch_bounds__(256) k3(const float* qabs, const u16* ckv, float* pctx, float* pm, float* pl) {
    __shared__ float sc[H][32];
    int b = blockIdx.x, t = threadIdx.x, wave = t >> 6, lane = t & 63;
    const u16* C = ckv + (size_t)b * 32 * DC;
    for (int idx = wave; idx < H * 32; idx += 4) {
        int h = idx / 32, k = idx % 32;
        float a = 0.f;
        for (int c = lane; c < DC; c += 64) a += qabs[h * DC + c] * bf2f(C[k * DC + c]);
        for (int s = 32; s > 0; s >>= 1) a += __shfl_xor(a, s);
        if (lane == 0) sc[h][k] = a;
    }
    __syncthreads();
    if (t < H) {
        float m = -1e30f;
        for (int k = 0; k < 32; ++k) m = fmaxf(m, sc[t][k]);
        float l = 0.f;
        for (int k = 0; k < 32; ++k) { float p = __expf(sc[t][k] - m); sc[t][k] = p; l += p; }
        pm[b * H + t] = m; pl[b * H + t] = l;
    }
    __syncthreads();
    for (int c = t; c < DC; c += 256) {
        float acc[H];
        for (int h = 0; h < H; ++h) acc[h] = 0.f;
        #pragma unroll 1
        for (int k = 0; k < 32; ++k) { float v = bf2f(C[k * DC + c]);
#pragma unroll
            for (int h = 0; h < H; ++h) acc[h] += sc[h][k] * v; }
        for (int h = 0; h < H; ++h) pctx[((size_t)b * H + h) * DC + c] = acc[h];
    }
}
__global__ void k4a(const float* pctx, const float* pm, const float* pl, float* ctxc) {
    int j = blockIdx.x * 256 + threadIdx.x;
    int h = j / DC;
    float m = -1e30f;
    for (int b = 0; b < 256; ++b) m = fmaxf(m, pm[b * H + h]);
    float l = 0.f, a = 0.f;
    for (int b = 0; b < 256; ++b) { float w = __expf(pm[b * H + h] - m); l += w * pl[b * H + h]; a += w * pctx[(size_t)b * H * DC + j]; }
    ctxc[j] = a / l;
}
__global__ void k4b(const float* ctxc, const u16* wuv, float* ctx) {
    int j = blockIdx.x * 256 + threadIdx.x;
    int h = j / D;
    float a = 0.f;
    for (int c = 0; c < DC; ++c) a += ctxc[h * DC + c] * bf2f(wuv[(size_t)c * H * D + j]);
    ctx[j] = MIMIC ? rbf(a) : a;
}
__global__ void k5(const float* ctx, const u16* wo, const float* g, const u16* x, u16* out) {
    int wave = (blockIdx.x * 256 + threadIdx.x) >> 6, lane = threadIdx.x & 63;
    if (wave < M) {
        float a = 0.f;
        for (int j = lane; j < H * D; j += 64) a += ctx[j] * bf2f(wo[(size_t)j * M + wave]);
        for (int s = 32; s > 0; s >>= 1) a += __shfl_xor(a, s);
        if (lane == 0) {
            float o = MIMIC ? bf2f(x[wave]) + rbf(rbf(a) * g[wave]) : bf2f(x[wave]) + a * g[wave];
            out[wave] = f2bf(o);
        }
    }
}

extern "C" void kernel_launch(void* const* d_in, const int* in_sizes, int n_in,
                              void* d_out, int out_size, void* d_ws, size_t ws_size,
                              hipStream_t stream) {
    const u16* x = (const u16*)d_in[0];
    const u16* ckv = (const u16*)d_in[1];
    const u16* wq = (const u16*)d_in[2];
    const u16* wuk = (const u16*)d_in[3];
    const u16* wuv = (const u16*)d_in[4];
    const u16* wo = (const u16*)d_in[5];
    const u16* w1 = (const u16*)d_in[7];
    const u16* b1 = (const u16*)d_in[8];
    const u16* w2 = (const u16*)d_in[9];
    const u16* b2 = (const u16*)d_in[10];
    float* ws = (float*)d_ws;
    float* q = ws; float* hid = q + 4096; float* qabs = hid + 2048; float* g = qabs + 16384;
    float* pm = g + 512; float* pl = pm + 4096; float* ctxc = pl + 4096; float* ctx = ctxc + 16384;
    float* pctx = ctx + 4096;
    k1<<<(H * D + F) / 256, 256, 0, stream>>>(x, wq, w1, b1, q, hid);
    k2<<<(DC * H + M) / 4, 256, 0, stream>>>(q, wuk, hid, w2, b2, qabs, g);
    k3<<<256, 256, 0, stream>>>(qabs, ckv, pctx, pm, pl);
    k4a<<<64, 256, 0, stream>>>(pctx, pm, pl, ctxc);
    k4b<<<16, 256, 0, stream>>>(ctxc, wuv, ctx);
    k5<<<M / 4, 256, 0, stream>>>(ctx, wo, g, x, (u16*)d_out);
}
```

```cpp
#define DLYG 48
#define HIDCG 2
#define PECU 1
#define PWAVE 2
#define PKEY 1
#include <hip/hip_runtime.h>
#include <stdint.h>

typedef unsigned v4u __attribute__((ext_vector_type(4)));
typedef unsigned v2u __attribute__((ext_vector_type(2)));
typedef float f32x4 __attribute__((ext_vector_type(4)));
typedef float f32x2 __attribute__((ext_vector_type(2)));
typedef __bf16 bf16x8 __attribute__((ext_vector_type(8)));
typedef __bf16 bf16x2 __attribute__((ext_vector_type(2)));
typedef short s16x4 __attribute__((ext_vector_type(4)));
typedef unsigned short u16x2 __attribute__((ext_vector_type(2)));
typedef unsigned short u16;
#define LAS __attribute__((address_space(3)))
#define GAS __attribute__((address_space(1)))
typedef __amdgpu_buffer_rsrc_t rsrc_t;

#define LATE 1
#define EARLY 1
#define NT_W 0
#define WSOFF 0
#define DLY0 16
#define DLYF 40
#define FSTART 7u
#define DLY2 0
#define DLY3 40
#define DLY3V 0
#define DLY4 0
#define RETRY 1
#define RETRY1 4
#define WF 4
#define WS 0
#define NSLOW 2
#define G1START 10
#define PRIO 2
#define HIDSTART 24u
#define RGATE 0
#define EGATE 1
#define WV 1
#ifndef NV
#define NV 20
#endif
#define GISSUE 0
#define PROBE1 0
#define DLY1 8
#define MLPPOS 2
#define PSTART 32u
#define HSHIFT 3

constexpr int WS_EPOCH = 0;
constexpr int WS_FLAG = 4096;
constexpr int WS_H0 = WS_FLAG + 16384;
constexpr int WS_H1 = WS_H0 + 65536;
constexpr int WS_P = WS_H1 + 32768;
constexpr int WS_PS = WS_P + 262144;
constexpr int WS_H2 = WS_PS + 32768;
constexpr int WS_H2S = WS_H2 + 524288;
constexpr int WS_H4 = WS_H2S + 4096;
constexpr int WS_HID = WS_H4 + 28 * 8192;
constexpr int WS_GP = WS_HID + 98 * 512;
constexpr int WS_END = WS_GP + 14 * 1792;
constexpr int WS_DIAG = 4 << 20;
static_assert(WS_END - WS_H1 <= 128 * 9 * 1024, "nine 1 KB pieces per filling workgroup");

constexpr int L_PART = 34 * 4096;
constexpr int L_XN = L_PART + 16384;
constexpr int L_Q = L_XN + 1024;
constexpr int L_CTX = L_Q + 512;
constexpr int L_HID = L_CTX + 128;
constexpr int L_ST4 = L_HID + 256;
constexpr int L_ST5 = L_ST4 + 512;
constexpr int L_X = L_ST5 + 128;
constexpr int L_E = L_X + 1024;
constexpr int L_CTL = L_E + 16;
constexpr int L_END = L_CTL + 512;
constexpr int L_S2P = L_PART;
enum { C_PROG = 0  , C_X = 2, C_FLOK = 3, C_PPUB = 4  , C_FILL = 8  , C_QX = 20  , C_QH = 24  , C_RELQ = 28  ,
       C_RELR = 32  , C_RELC = 40  , C_P3 = 80  , C_P4 = 48  , C_CG = 56  , C_G2 = 64  , C_CTX = 72  , C_S4 = 76  , C_NUM = 96 };

constexpr int NWAVE = 15;
constexpr unsigned SENT = 0xFFFFFFFFu;
constexpr int AUX_SC1 = 16;
constexpr unsigned SPIN_G = 1u << 15;

struct Params {
    const void* x; const void* ckv; const void* wq; const void* wuk; const void* wuv; const void* wo;
    const void* w1; const void* b1; const void* w2; const void* b2; void* out; void* ws;
};

__device__ __forceinline__ float u2f(unsigned u) { return __builtin_bit_cast(float, u); }
__device__ __forceinline__ unsigned f2u(float f) { return __builtin_bit_cast(unsigned, f); }
__device__ __forceinline__ unsigned pk_bf16(float a, float b) { f32x2 v = {a, b}; bf16x2 r = __builtin_convertvector(v, bf16x2); return __builtin_bit_cast(unsigned, r); }
__device__ __forceinline__ float rbf(float a) { return u2f(pk_bf16(a, a) << 16); }
__device__ __forceinline__ float bflo(unsigned u) { return u2f(u << 16); }
__device__ __forceinline__ float bfhi(unsigned u) { return u2f(u & 0xFFFF0000u); }
__device__ __forceinline__ unsigned san(unsigned u) { return u < 0xFFFFFFFEu ? u : 0xFFFFFFFEu; }
__device__ __forceinline__ v4u san4(v4u v) { return v4u{san(v.x), san(v.y), san(v.z), san(v.w)}; }
__device__ __forceinline__ v4u f4u(f32x4 v) { return __builtin_bit_cast(v4u, v); }
__device__ __forceinline__ f32x4 u4f(v4u v) { return __builtin_bit_cast(f32x4, v); }
__device__ __forceinline__ unsigned umax(unsigned a, unsigned b) { return a > b ? a : b; }
__device__ __forceinline__ unsigned umax4(v4u v) { return umax(umax(v.x, v.y), umax(v.z, v.w)); }
__device__ __forceinline__ unsigned umax4a(unsigned m, v4u v) { return umax(umax(umax(umax(m, v.x), v.y), v.z), v.w); }
__device__ __forceinline__ float fmx(float a, float b) { return __builtin_amdgcn_fmed3f(a, b, __builtin_inff()); }
__device__ __forceinline__ f32x4 mfma(v4u a, v4u b, f32x4 c) {
    return __builtin_amdgcn_mfma_f32_16x16x32_bf16(__builtin_bit_cast(bf16x8, a), __builtin_bit_cast(bf16x8, b), c, 0, 0, 0);
}
template <int N> __device__ __forceinline__ float ror(float v) { return u2f((unsigned)__builtin_amdgcn_update_dpp(0, (int)f2u(v), 0x120 + N, 0xf, 0xf, false)); }
__device__ __forceinline__ float row_sum(float v) { v += ror<8>(v); v += ror<4>(v); v += ror<2>(v); v += ror<1>(v); return v; }
__device__ __forceinline__ float row_max(float v) { v = fmx(v, ror<8>(v)); v = fmx(v, ror<4>(v)); v = fmx(v, ror<2>(v)); v = fmx(v, ror<1>(v)); return v; }
__device__ __forceinline__ float xrow_sum(float v) {
    auto a = __builtin_amdgcn_permlane32_swap(f2u(v), f2u(v), false, false); v = u2f(a[0]) + u2f(a[1]);
    auto b = __builtin_amdgcn_permlane16_swap(f2u(v), f2u(v), false, false); return u2f(b[0]) + u2f(b[1]);
}
__device__ __forceinline__ float xrow_max(float v) {
    auto a = __builtin_amdgcn_permlane32_swap(f2u(v), f2u(v), false, false); v = fmx(u2f(a[0]), u2f(a[1]));
    auto b = __builtin_amdgcn_permlane16_swap(f2u(v), f2u(v), false, false); return fmx(u2f(b[0]), u2f(b[1]));
}
__device__ __forceinline__ float wave_sum(float v) { return xrow_sum(row_sum(v)); }
__device__ __forceinline__ float wave_sum4(f32x4 v) {
    auto a = __builtin_amdgcn_permlane32_swap(f2u(v.x), f2u(v.y), false, false); const float xy = u2f(a[0]) + u2f(a[1]);
    auto b = __builtin_amdgcn_permlane32_swap(f2u(v.z), f2u(v.w), false, false); const float zw = u2f(b[0]) + u2f(b[1]);
    auto c = __builtin_amdgcn_permlane16_swap(f2u(xy), f2u(zw), false, false);
    return row_sum(u2f(c[0]) + u2f(c[1]));
}

__device__ __forceinline__ float sel4(int q, const f32x4 (&a)[4]) { const float x0 = a[0].x, x1 = a[1].x, x2 = a[2].x, x3 = a[3].x; return q == 0 ? x0 : q == 1 ? x1 : q == 2 ? x2 : x3; }
__device__ __forceinline__ float quad_sum4(f32x4 v) {
    auto a = __builtin_amdgcn_permlane32_swap(f2u(v.x), f2u(v.y), false, false); const float xy = u2f(a[0]) + u2f(a[1]);
    auto b = __builtin_amdgcn_permlane32_swap(f2u(v.z), f2u(v.w), false, false); const float zw = u2f(b[0]) + u2f(b[1]);
    auto c = __builtin_amdgcn_permlane16_swap(f2u(xy), f2u(zw), false, false);
    float r = u2f(c[0]) + u2f(c[1]); r += ror<8>(r); r += ror<4>(r); return r;
}
__device__ __forceinline__ rsrc_t mk_rsrc(const void* p, int bytes) { return __builtin_amdgcn_make_buffer_rsrc((void*)p, 0, bytes, 0x00020000); }
__device__ __forceinline__ v4u ld16(rsrc_t r, int off) { return __builtin_amdgcn_raw_buffer_load_b128(r, off, 0, AUX_SC1); }
__device__ __forceinline__ v2u ld8(rsrc_t r, int off) { return __builtin_amdgcn_raw_buffer_load_b64(r, off, 0, AUX_SC1); }
__device__ __forceinline__ unsigned ld4(rsrc_t r, int off) { return __builtin_amdgcn_raw_buffer_load_b32(r, off, 0, AUX_SC1); }
__device__ __forceinline__ void st16(rsrc_t r, int off, v4u v) { __builtin_amdgcn_raw_buffer_store_b128(v, r, off, 0, AUX_SC1); }
__device__ __forceinline__ void st4(rsrc_t r, int off, unsigned v) { __builtin_amdgcn_raw_buffer_store_b32(v, r, off, 0, AUX_SC1); }
__device__ __forceinline__ void st8(rsrc_t r, int off, v2u v) { __builtin_amdgcn_raw_buffer_store_b64(v, r, off, 0, AUX_SC1); }
constexpr int OOB = 0x40000000;
#define CBAR() asm volatile("" ::: "memory")
#define SB() __builtin_amdgcn_sched_barrier(0)
#define HI() __builtin_amdgcn_s_setprio(3)
#define PIN(x) asm volatile("" : "+v"(x))
#define DRAIN() asm volatile("s_waitcnt vmcnt(0)" ::: "memory")
#define LWAIT() asm volatile("s_waitcnt lgkmcnt(0)" ::: "memory")

typedef LAS unsigned char* lds_t;
__device__ __forceinline__ v4u lds16(int off) { return *(LAS v4u*)off; }
__device__ __forceinline__ f32x4 lds16f(int off) { return *(LAS f32x4*)off; }
template <int D2> __device__ __forceinline__ v4u lds_tr(int off) {
    s16x4 a = __builtin_amdgcn_ds_read_tr16_b64_v4i16((LAS s16x4*)off);
    s16x4 b = __builtin_amdgcn_ds_read_tr16_b64_v4i16((LAS s16x4*)(off + D2));
    v2u a2 = __builtin_bit_cast(v2u, a), b2 = __builtin_bit_cast(v2u, b);
    return v4u{a2.x, a2.y, b2.x, b2.y};
}
#define MINSLEEP 0
template <int SLEEP0> __device__ __forceinline__ void wait_lds(int addr, unsigned want) {
    constexpr int SLEEP = SLEEP0 > MINSLEEP ? SLEEP0 : MINSLEEP;
    unsigned v, c;
    if (SLEEP)
        asm volatile("s_mov_b32 %1, 0x40000\n1:\n\tds_read_b32 %0, %2\n\ts_sub_u32 %1, %1, 1\n\ts_waitcnt lgkmcnt(0)\n\tv_cmp_gt_u32 vcc, %3, %0\n\t"
                     "s_cbranch_scc1 2f\n\ts_cbranch_vccz 2f\n\ts_sleep %4\n\ts_branch 1b\n2:"
                     : "=&v"(v), "=&s"(c) : "v"(addr), "v"(want), "i"(SLEEP) : "memory", "vcc", "scc");
    else
        asm volatile("s_mov_b32 %1, 0x80000\n1:\n\tds_read_b32 %0, %2\n\ts_sub_u32 %1, %1, 1\n\ts_waitcnt lgkmcnt(0)\n\tv_cmp_gt_u32 vcc, %3, %0\n\t"
                     "s_cbranch_scc1 2f\n\ts_cbranch_vccnz 1b\n2:"
                     : "=&v"(v), "=&s"(c) : "v"(addr), "v"(want) : "memory", "vcc", "scc");
}
#define CW(word) (L_CTL + 4 * (word))
#define WAITP(S, word, want) wait_lds<S>(CW(word), (want))
#define WAITF(S, word, n) wait_lds<S>(CW(word) + 4 * (lane & ((n) - 1)), 1u)
__device__ __forceinline__ void sig(int addr, unsigned v) { asm volatile("ds_write_b32 %0, %1" :: "v"(addr), "v"(v) : "memory"); }
__device__ __forceinline__ bool any_sent(unsigned m) { return __builtin_amdgcn_ballot_w64(m == SENT) != 0ull; }
__device__ __forceinline__ bool any_nz(unsigned m) { return __builtin_amdgcn_ballot_w64(m != 0u) != 0ull; }

#define STAMP(k) do { if ((k) == PKEY && wv == PWAVE && (int)ecu == PECU) pd_ = (unsigned)__builtin_amdgcn_s_memrealtime() - pt0_; } while (0)
#define STAMP_DECL const unsigned pt0_ = (unsigned)__builtin_amdgcn_s_memrealtime(); unsigned pd_ = 0;
#define LSTAMP(n)
#define PASSES(k, s)
#define STAMP_OUT() do { STAMP(15); pd_ = pd_ < 5000u ? pd_ : 5000u; while ((unsigned)__builtin_amdgcn_s_memrealtime() - pt0_ < 20u * pd_) __builtin_amdgcn_s_sleep(8); } while (0)

__device__ __forceinline__ void wait_vm(int n) {
    switch (n) {
#define VMC(x) case x: asm volatile("s_waitcnt vmcnt(" #x ")" ::: "memory"); break;
        VMC(0) VMC(4) VMC(8) VMC(12) VMC(16) VMC(20) VMC(24) VMC(28) VMC(32) VMC(36) VMC(40)
    }
}
__device__ __forceinline__ void dma_tile(rsrc_t r, unsigned soff, unsigned lds, unsigned v0, unsigned v1, unsigned v2, unsigned v3) {
#define POL ""
    asm volatile("s_mov_b32 m0, %6\n\ts_nop 0\n\t"
                 "buffer_load_dwordx4 %0, %4, %5 offen" POL " lds\n\t"
                 "buffer_load_dwordx4 %1, %4, %5 offen offset:1024" POL " lds\n\t"
                 "buffer_load_dwordx4 %2, %4, %5 offen offset:2048" POL " lds\n\t"
                 "buffer_load_dwordx4 %3, %4, %5 offen offset:3072" POL " lds"
                 :: "v"(v0), "v"(v1), "v"(v2), "v"(v3), "s"(r), "s"(soff), "s"(lds) : "memory");
}

template <int W>
__device__ __forceinline__ void dma_seg(rsrc_t r, int& n, int end, unsigned ts, unsigned tl, int& vp, int prog, unsigned v0, unsigned v1, unsigned v2, unsigned v3) {
    unsigned so, sl; int t;
    asm volatile("s_nop 1\n\tv_readlane_b32 %[so], %[ts], %[n]\n\tv_readlane_b32 %[sl], %[tl], %[n]\n\ts_nop 3\n"
                 "1:\n\ts_mov_b32 m0, %[sl]\n\ts_add_u32 %[n], %[n], 1\n\t"
                 "buffer_load_dwordx4 %[v0], %[r], %[so] offen" POL " lds\n\t"
                 "buffer_load_dwordx4 %[v1], %[r], %[so] offen offset:1024" POL " lds\n\t"
                 "buffer_load_dwordx4 %[v2], %[r], %[so] offen offset:2048" POL " lds\n\t"
                 "buffer_load_dwordx4 %[v3], %[r], %[so] offen offset:3072" POL " lds\n\t"
                 "v_readlane_b32 %[sl], %[tl], %[n]\n\tv_readlane_b32 %[so], %[ts], %[n]\n\t"
                 "s_sub_u32 %[t], %[n], %[w]\n\tv_max_i32 %[vp], %[t], %[vp]\n\t"
                 "s_waitcnt vmcnt(%[w4])\n\tds_write_b32 %[pa], %[vp]\n\t"
                 "s_cmp_lt_i32 %[n], %[e]\n\ts_cbranch_scc1 1b"
                 : [n] "+s"(n), [vp] "+v"(vp), [so] "=&s"(so), [sl] "=&s"(sl), [t] "=&s"(t)
                 : [r] "s"(r), [e] "s"(end), [ts] "v"(ts), [tl] "v"(tl), [pa] "v"(prog), [v0] "v"(v0), [v1] "v"(v1), [v2] "v"(v2), [v3] "v"(v3), [w] "i"(W), [w4] "i"(4 * W)
                 : "memory", "scc");
}

__global__ void __launch_bounds__(NWAVE * 64, 4) fused_decode(Params p) {
    extern __shared__ __attribute__((aligned(16))) unsigned char smem[];
    const int lane = threadIdx.x & 63, wv = __builtin_amdgcn_readfirstlane(threadIdx.x >> 6);
    const int b = blockIdx.x, jj = (b >> 3) & 15;
    const bool ecu = b < 128;
    const int hh = 2 * (b & 7) + (jj >> 3);
    const int i8 = jj & 7, dq = i8 >> 1, hf = i8 & 1;
    const int e = (b & 7) * 16 + jj;
    const int l15 = lane & 15, q4 = lane >> 4;
#define CSLOT(mm, pq) (((mm) < 12 ? 2 * (mm) + (pq) : (mm) == 12 ? 32 + (pq) : 2 * (mm) - 26 + (pq)) * 4096)
#define KROT ((hh & 1) ? i8 : 3)
#define RHO(mm) (2 * ((i8 + 1 + (mm)) & 7) + 1 - ((mm) >> 3))
    STAMP_DECL
    if (wv == 2) { *(LAS v2u*)(L_CTL + lane * 8) = v2u{0u, 0u}; LWAIT(); }
    if (wv >= 2) __builtin_amdgcn_s_barrier();
    asm volatile("" :: "s"(p.x), "s"(p.ws), "s"(p.wq) : "memory");

    if (__builtin_expect(wv < 2, 1)) {
        const int pp = wv;
        const unsigned r = lane >> 3, pc = lane & 7, s0 = (r >> 1) & 1;
        const unsigned ce = (pc ^ (s0 << 1)) << 4, co = (pc ^ ((s0 | 2) << 1)) << 4;
#define VOFFS(st) (r * (st) + ce), ((r + 8) * (st) + co - 1024), ((r + 16) * (st) + ce - 2048), ((r + 24) * (st) + co - 3072)
        int n = 0, vp = 0;
        const int prog = CW(C_PROG + pp);
        __builtin_amdgcn_s_setprio(1);
#define DOWN(W) do { wait_vm(4 * (W)); vp = vp > n - (W) ? vp : n - (W); sig(prog, vp); } while (0)
#define BLOCK(addr) do { const int a_ = (addr); _Pragma("unroll") for (int x_ = WF - 1; x_ >= 0; --x_) { if (__builtin_amdgcn_ballot_w64(*(volatile LAS unsigned*)a_ == 0u) == 0ull) break; DOWN(x_); } wait_lds<0>(a_, 1u); } while (0)
#define DRAINW(W, T) do { _Pragma("unroll") for (int x_ = (W) - 1; x_ >= (T); --x_) DOWN(x_); } while (0)
        if (pp == 0)
            asm volatile("s_mov_b32 m0, %3\n\ts_nop 0\n\tbuffer_load_dwordx4 %0, %1, 0 offen lds\n\t"
                         "s_mov_b32 m0, %4\n\ts_mov_b64 exec, 1\n\tbuffer_load_dwordx4 %0, %2, 0 offen lds\n\ts_mov_b64 exec, -1"
                         :: "v"(lane * 16), "s"(mk_rsrc(p.x, ecu ? 896 : 0)), "s"(mk_rsrc(p.ws, 16)), "s"(L_X), "s"(L_E) : "memory");
        if (__builtin_expect(ecu, 1)) {
            const rsrc_t rq = mk_rsrc(p.wq, 448 * 8192);
            const unsigned so = (224 * hf + 32 * pp) * 8192 + (((hh + HSHIFT) & 15) * 256 + dq * 64) * 2;
            dma_tile(rq, so, pp * 4096, VOFFS(8192));
            dma_tile(rq, so + 64 * 8192, (2 + pp) * 4096, VOFFS(8192));
            dma_tile(rq, so + 128 * 8192, (4 + pp) * 4096, VOFFS(8192));
            dma_tile(mk_rsrc(p.wq, pp ? 0 : 448 * 8192), so + 192 * 8192, (6 + pp) * 4096, VOFFS(8192));
            __builtin_amdgcn_s_barrier();
            SB();
            const int e2 = e - 30;
            const int k = lane - 4, mm = lane - 16;
            unsigned ts = (128 * i8 + 32 * pp + (k & 1) * 64) * 8192 + hh * 512 + (((k >> 1) + KROT) & 3) * 128, tl = (2 * lane + pp) * 4096;
            if (lane >= 12) ts = e < 98 ? (64 * (e % 7) + 32 * pp) * 3584 + (e / 7) * 256 + (lane & 1) * 128 : 0;
            if (lane >= 14) ts = e2 >= 0 ? (128 * (e2 / 7) + 32 * pp + (lane & 1) * 64) * 896 + (e2 % 7) * 128 : 0;
            if (lane >= 16) { ts = (512 * hh + 32 * RHO(mm)) * 2048 + (2 * i8 + 1 - pp) * 128; tl = CSLOT(mm, pp); }
            if (pp == 0) { asm volatile("s_waitcnt vmcnt(16)" ::: "memory"); sig(CW(C_X), 1u); }
            n = 4;
            dma_seg<WF>(mk_rsrc(p.wuk, EARLY * 1024 * 8192), n, 12, ts, tl, vp, prog, VOFFS(8192));
            STAMP(1);
            if (MLPPOS == 1) BLOCK(CW(C_QH) + 4 * (lane & 3));
            if (MLPPOS == 2) BLOCK(CW(C_PPUB));
            dma_seg<WF>(mk_rsrc(p.w1, e < 98 ? LATE * 448 * 3584 : 0), n, 14, ts, tl, vp, prog, VOFFS(3584));
            dma_seg<WF>(mk_rsrc(p.w2, e2 >= 0 ? LATE * 1792 * 896 : 0), n, 16, ts, tl, vp, prog, VOFFS(896));
            if (MLPPOS < 2) BLOCK(CW(C_PPUB));
            STAMP(2);
            const rsrc_t rc = mk_rsrc(p.ckv, EARLY * 8192 * 2048);
            dma_seg<WF>(rc, n, 29, ts, tl, vp, prog, VOFFS(2048));
            STAMP(3);
            WAITF(0, C_RELC, 2);
            dma_seg<WF>(rc, n, 32, ts, tl, vp, prog, VOFFS(2048));
        } else {
            {
                const rsrc_t wsr = mk_rsrc(p.ws, WS_END);
#pragma unroll
                for (int k = 0; k < 5; ++k) __builtin_amdgcn_raw_buffer_store_b128(v4u{SENT, SENT, SENT, SENT}, wsr, lane * 16, WS_H1 + ((2 * k + pp) * 128 + e) * 1024, AUX_SC1);
            }
            __builtin_amdgcn_s_barrier();
            int cb = lane - 24 + 4 * hf; if (cb >= 7) cb -= 7;
            unsigned ts = (512 * hh + 64 * i8 + (lane >> 3) * 32) * 2048 + ((2 * i8 + 2 * lane + pp) & 15) * 128, tl = 2 * lane + pp;
            if (lane >= 16) { ts = (512 * hf + 32 * pp + (lane - 16) * 64) * 8192 + (hh * 256 + dq * 64) * 2; tl = 2 * lane + pp - 32; }
            if (lane >= 24) { ts = (hh * 256 + dq * 64 + 32 * pp) * 896 + cb * 128; tl = lane == 24 ? 32 + pp : 2 * lane + pp - 34; }
            tl *= 4096;
            const rsrc_t rc = mk_rsrc(p.ckv, EARLY * 8192 * 2048);
            DRAIN(); sig(CW(C_FILL + pp), 1u);
            if (NSLOW) dma_seg<WS>(rc, n, NSLOW, ts, tl, vp, prog, VOFFS(2048));
            dma_seg<WF>(rc, n, 16, ts, tl, vp, prog, VOFFS(2048));
            STAMP(1);
            BLOCK(CW(C_RELR) + 4 * (lane & 7));
            const rsrc_t rv = mk_rsrc(p.wuv, LATE * 1024 * 8192);
            DRAINW(WF, WV);
#pragma unroll 1
            while (n < 24 && __builtin_amdgcn_ballot_w64(*(volatile LAS unsigned*)(CW(C_PPUB) + 4 * (lane & 1)) == 0u) != 0ull)
                dma_seg<WV>(rv, n, n + 1, ts, tl, vp, prog, VOFFS(8192));
            if (n < 24) dma_seg<WF>(rv, n, 24, ts, tl, vp, prog, VOFFS(8192));
            STAMP(2);
            const rsrc_t ro = mk_rsrc(p.wo, LATE * 4096 * 896);
            dma_seg<WF>(ro, n, 25, ts, tl, vp, prog, VOFFS(896));
            BLOCK(CW(C_PPUB) + 4 * (lane & 1));
            dma_seg<WF>(ro, n, 31, ts, tl, vp, prog, VOFFS(896));
        }
        STAMP(4);
        DRAINW(WF, 0);
        STAMP_OUT();
        return;
    }

    const rsrc_t ws = mk_rsrc(p.ws, WS_END);
    const v4u S4 = {SENT, SENT, SENT, SENT};
    const f32x4 Z4 = {0.f, 0.f, 0.f, 0.f};
    const int swr = ((l15 >> 1) & 1) | (((l15 >> 3) & 1) << 1);
    const int row_a0 = l15 * 128 + ((q4 ^ (swr << 1)) << 4), row_a1 = row_a0 ^ 64;
    const int swt = ((l15 >> 3) & 1) | ((q4 & 1) << 1);
    const int tr_b = (8 * q4 + (l15 >> 2)) * 128 + (((l15 >> 1) & 1) << 4) + 8 * (l15 & 1);
#define TR_A(nt) (tr_b + ((((nt) ^ swt)) << 5))
#define WAITT(S, k) wait_lds<S>(CW(C_PROG) + 4 * (lane & 1), (k))

    if (wv == 6) {
        wait_lds<0>(ecu ? CW(C_X) : CW(C_FILL + (lane & 1)), 1u);
        const v2u ep = *(LAS v2u*)(L_E);
        const unsigned tagA = ep.x + 1u, tagB = ep.y | 1u;
        if (!ecu) st16(ws, lane < 8 ? WS_FLAG + e * 128 + lane * 16 : OOB, v4u{tagA, tagB, tagA, tagB});
        unsigned tv = 0;
        {
            const int e2 = e - 30;
            const GAS char* a = (const GAS char*)p.ckv + (512 * hh + 64 * i8 + (ecu ? 63 : 0)) * 2048 + 2 * i8 * 128;
            if (ecu) {
                if (lane == 1 && e < 98) a = (const GAS char*)p.w1 + 64 * (e % 7) * 3584 + (e / 7) * 256;
                if (lane == 2 && e2 >= 0) a = (const GAS char*)p.w2 + 128 * (e2 / 7) * 896 + (e2 % 7) * 128;
            } else {
                if (lane == 1 || lane == 2) a = (const GAS char*)p.wuv + (512 * hf + 256 * (lane - 1)) * 8192 + (hh * 256 + dq * 64) * 2;
                if (lane == 3) a = (const GAS char*)p.wuv + (512 * hf + 511) * 8192 + (hh * 256 + dq * 64) * 2;
                if (lane == 4) a = (const GAS char*)p.wo + (hh * 256 + dq * 64) * 896 + 4 * hf * 128;
            }
            if (lane < 5) tv = __builtin_nontemporal_load((const GAS unsigned*)a);
        }
        unsigned tb = 0;
        if (!ecu && jj < 2) {
            const GAS char* a = lane < 28 ? (const GAS char*)p.b1 + lane * 128 : lane < 35 ? (const GAS char*)p.b2 + (lane - 28) * 128 : (const GAS char*)p.out;
            if (lane < 36) tb = *(const GAS unsigned*)a;
        }
        const int u = e < 28 ? e : -1;
        const int col = 16 * (u >= 0 ? u : 0) + 4 * (lane & 3) + 2 * (q4 & 1) + (q4 >> 1);
        const unsigned xv = *(LAS u16*)(L_X + col * 2);
        STAMP(1);
        if (ecu) WAITT(2, FSTART); else __builtin_amdgcn_s_sleep(DLYF);
        HI();
#pragma unroll 1
        for (unsigned s = 0; s < SPIN_G; ++s) { PASSES(7, s);
            CBAR();
            unsigned m = 0;
#pragma unroll
            for (int k = 0; k < 4; ++k) m |= ld4(ws, WS_FLAG + k * 4096 + (lane >> 1) * 128 + (lane & 1) * 4) ^ ((lane & 1) ? tagB : tagA);
            if (!any_nz(m)) break;
            __builtin_amdgcn_s_sleep(4);
        }
        asm volatile("" :: "v"(tv), "v"(tb));
        sig(CW(C_FLOK), 1u);
        STAMP(2);
        if (ecu) {
            const int e2 = e - 30, fs2 = e2 / 7, mt2 = e2 % 7;
            unsigned b1a = 0, b1b = 0, b2v = 0;
            WAITF(2, C_RELQ, 4);
            if (e2 >= 0) { b1a = ((const GAS u16*)p.b1)[128 * fs2 + lane]; b1b = ((const GAS u16*)p.b1)[128 * fs2 + 64 + lane]; }
            if (u >= 0) b2v = ((const GAS u16*)p.b2)[col];
            if (e < 98) {
                const int ms1 = e % 7;
                const v4u bx0 = lds16(L_XN + (64 * ms1 + 8 * q4) * 2), bx1 = lds16(L_XN + (64 * ms1 + 32 + 8 * q4) * 2);
                WAITT(2, 14u);
#pragma unroll
                for (int cb = 0; cb < 2; ++cb) {
                    f32x4 a[4];
#pragma unroll
                    for (int nt = 0; nt < 4; ++nt) {
                        a[nt] = mfma(bx0, lds_tr<512>((24 + 2 * cb) * 4096 + TR_A(nt)), Z4);
                        a[nt] = mfma(bx1, lds_tr<512>((25 + 2 * cb) * 4096 + TR_A(nt)), a[nt]);
                    }
                    st4(ws, WS_HID + e * 512 + cb * 256 + lane * 4, san(f2u(sel4(q4, a))));
                }
            }
            STAMP(3);
            if (e2 >= 0) {
                unsigned hp[14];
                if (HIDCG == 1) WAITF(2, C_CG, 8); else if (HIDCG == 2) WAITF(2, C_S4, 4); else WAITT(2, HIDSTART);
#pragma unroll 1
                for (unsigned s = 0; s < SPIN_G; ++s) { PASSES(2, s);
                    CBAR();
                    unsigned m = 0;
#pragma unroll
                    for (int k = 0; k < 14; ++k) hp[k] = ld4(ws, WS_HID + fs2 * 3584 + k * 256 + lane * 4);
#pragma unroll
                    for (int k = 0; k < 14; ++k) m = umax(m, hp[k]);
                    if (!any_sent(m)) break;
                    __builtin_amdgcn_s_sleep(4);
                }
                float ha = 0.f, hb = 0.f;
#pragma unroll
                for (int k = 0; k < 7; ++k) { ha += u2f(hp[2 * k]); hb += u2f(hp[2 * k + 1]); }
                ha = rbf(rbf(ha) + bflo(b1a)); hb = rbf(rbf(hb) + bflo(b1b));
                ha = ha > 0.f ? ha : 0.f; hb = hb > 0.f ? hb : 0.f;
                const unsigned hq = pk_bf16(ha * ha, hb * hb);
                *(LAS u16*)(L_HID + lane * 2) = (u16)hq; *(LAS u16*)(L_HID + 128 + lane * 2) = (u16)(hq >> 16);
                CBAR();
                STAMP(4);
                f32x4 a[4] = {Z4, Z4, Z4, Z4};
#pragma unroll
                for (int k = 0; k < 4; ++k) {
                    const v4u bh = lds16(L_HID + k * 64 + q4 * 16);
#pragma unroll
                    for (int nt = 0; nt < 4; ++nt) a[nt] = mfma(bh, lds_tr<512>((28 + k) * 4096 + TR_A(nt)), a[nt]);
                }
                st4(ws, WS_GP + fs2 * 1792 + (64 * mt2 + lane) * 4, san(f2u(sel4(q4, a))));
            }
            STAMP(5);
            if (u >= 0) {
                v4u gv;
                WAITF(2, C_S4, 4);
                __builtin_amdgcn_s_sleep(DLYG);
#pragma unroll 1
                for (unsigned s = 0; s < SPIN_G; ++s) { PASSES(3, s);
                    CBAR();
                    gv = ld16(ws, lane < 56 ? WS_GP + (lane >> 2) * 1792 + u * 64 + (lane & 3) * 16 : OOB);
                    if (!any_sent(umax4(gv))) break;
                    __builtin_amdgcn_s_sleep(4);
                }
                float xf = bflo(xv);
                const float ga = rbf(rbf(quad_sum4(u4f(gv))) + bflo(b2v));
                float g = rbf(ga / (1.f + __expf(-ga)));
                int o_s = WS_H4 + u * 8192 + lane * 16;
                int o_o = l15 < 4 ? col * 2 : OOB;
                const rsrc_t ro = mk_rsrc(p.out, 896);
                PIN(g); PIN(xf); PIN(o_s); PIN(o_o);
                v4u sv[8];
                STAMP(6);
                __builtin_amdgcn_s_sleep(DLY4);
#pragma unroll 1
                for (unsigned s = 0; s < SPIN_G; ++s) { PASSES(4, s);
                    CBAR();
#pragma unroll
                    for (int k = 0; k < 8; ++k) sv[k] = __builtin_amdgcn_raw_buffer_load_b128(ws, o_s + (k & 3) * 1024, (k >> 2) * 4096, AUX_SC1);
                    unsigned m = 0, m2 = 0;
#pragma unroll
                    for (int k = 0; k < 4; ++k) { m = umax4a(m, sv[2 * k]); m2 = umax4a(m2, sv[2 * k + 1]); }
                    if (!any_sent(umax(m, m2))) break;
                }
                STAMP(8);
                const float s = quad_sum4(((u4f(sv[0]) + u4f(sv[1])) + (u4f(sv[2]) + u4f(sv[3]))) + ((u4f(sv[4]) + u4f(sv[5])) + (u4f(sv[6]) + u4f(sv[7]))));
                const float o = xf + rbf(rbf(s) * g);
                __builtin_amdgcn_raw_buffer_store_b16((short)pk_bf16(o, o), ro, o_o, 0, 0);
                if (u == 0) st8(ws, lane == 0 ? WS_EPOCH : OOB, v2u{tagA, ep.y});
            }
        }
        STAMP_OUT();
        return;
    }

    if (ecu) {
        if (wv < 6) {
            const int t = wv - 2;
            int o_h0 = lane < 8 ? WS_H0 + ((((hh + HSHIFT) & 15) * 4 + dq) * 2 + hf) * 512 + t * 128 + lane * 16 : OOB;
            int o_h1 = lane < 16 ? WS_H1 + ((2 * i8 + (lane >> 3)) * 16 + hh) * 128 + (lane & 7) * 16 : OOB;
            st16(ws, o_h0, v4u{0u, 0u, 0u, 0u});
            int tra = TR_A(t);
            int a_bx = L_XN + (224 * hf + 8 * q4) * 2;
            int o_g0 = WS_H0 + (hh * 4 + t) * 1024 + lane * 16;
            PIN(tra); PIN(a_bx); PIN(o_h0); PIN(o_g0);
            WAITP(0, C_X, 1u);
            const v2u ep = *(LAS v2u*)(L_E);
            {
                const v4u xv = lds16(L_X + lane * 16);
                float ev[8] = {bflo(xv.x), bfhi(xv.x), bflo(xv.y), bfhi(xv.y), bflo(xv.z), bfhi(xv.z), bflo(xv.w), bfhi(xv.w)};
                const float s = ((ev[0] + ev[1]) + (ev[2] + ev[3])) + ((ev[4] + ev[5]) + (ev[6] + ev[7]));
                const float mean = wave_sum(s) * (1.0f / 448.0f);
                float v = 0.f;
#pragma unroll
                for (int k = 0; k < 8; ++k) { const float d = ev[k] - mean; v += d * d; }
                v = lane < 56 ? v : 0.f;
                const float var = wave_sum(v) * (1.0f / 448.0f);
                const float mb = rbf(mean);
                const float rs = 1.0f / rbf(__builtin_amdgcn_sqrtf(rbf(rbf(var) + 1e-5f)));
                float o[8];
#pragma unroll
                for (int k = 0; k < 8; ++k) o[k] = rbf(ev[k] - mb) * rs;
                *(LAS v4u*)(L_XN + lane * 16) = v4u{pk_bf16(o[0], o[1]), pk_bf16(o[2], o[3]), pk_bf16(o[4], o[5]), pk_bf16(o[6], o[7])};
                CBAR();
            }
            const unsigned tagA = ep.x + 1u, tagB = ep.y | 1u;
            {
                v4u bx[7], a[7];
#pragma unroll
                for (int s = 0; s < 7; ++s) bx[s] = lds16(a_bx + 64 * s);
                STAMP(1);
                wait_lds<0>(CW(C_PROG) + 4 * (lane & 1), (lane & 1) ? 3u : 4u);
                HI();
                STAMP(2);
#pragma unroll
                for (int s = 0; s < 7; ++s) a[s] = lds_tr<512>(tra + s * 4096);
                SB();
                f32x4 acc = mfma(bx[0], a[0], Z4), acc2 = mfma(bx[1], a[1], Z4);
                acc = mfma(bx[2], a[2], acc); acc2 = mfma(bx[3], a[3], acc2); acc = mfma(bx[4], a[4], acc); acc2 = mfma(bx[5], a[5], acc2); acc = mfma(bx[6], a[6], acc);
                const float qv = acc.x + acc2.x;
                st16(ws, o_h0, v4u{f2u(qv), tagA, f2u(ror<8>(qv)), tagB});
            }
            STAMP(3);
            __builtin_amdgcn_s_sleep(DLY0);
            int a_qw = L_Q + t * 128 + ((lane >> 3) & 3) * 32 + (lane & 7) * 2, a_qr = L_Q + t * 128 + q4 * 16;
            int a_k0 = (8 + 4 * ((t - KROT) & 3)) * 4096 + row_a0, a_k1 = a_k0 ^ 64;
            int a_pw = L_S2P + t * 512 + lane * 4;
            int a_pr = L_S2P + l15 * 32;
            PIN(a_qw); PIN(a_qr); PIN(a_k0); PIN(a_k1); PIN(o_h1); PIN(a_pw); PIN(a_pr);
            HI();
            v4u g0 = ld16(ws, o_g0);
            v4u ak[16];
            WAITT(0, 6u + 2 * ((t - KROT) & 3));
#pragma unroll
            for (int k = 0; k < 8; ++k) { ak[2 * k] = lds16(a_k0 + k * 2048); ak[2 * k + 1] = lds16(a_k1 + k * 2048); }
            sig(CW(C_RELQ + t), 1u);
#pragma unroll 1
            for (unsigned s = 0; s < SPIN_G; ++s) { PASSES(0, s);
                if (!any_nz((g0.y ^ tagA) | (g0.w ^ tagB))) break;
                CBAR();
                g0 = ld16(ws, o_g0);
            }
            sig(CW(C_QH + t), 1u);
            STAMP(4);
            {
                auto ra = __builtin_amdgcn_permlane32_swap(g0.x, g0.x, false, false);
                auto rb = __builtin_amdgcn_permlane32_swap(g0.z, g0.z, false, false);
                const unsigned qq = pk_bf16(u2f(ra[0]) + u2f(ra[1]), u2f(rb[0]) + u2f(rb[1]));
                *(LAS u16*)(a_qw) = (u16)qq; *(LAS u16*)(a_qw + 16) = (u16)(qq >> 16);
                CBAR();
                const v4u bq0 = lds16(a_qr), bq1 = lds16(a_qr + 64);
                f32x4 a[4], a2[4];
#pragma unroll
                for (int k = 0; k < 4; ++k) a[k] = mfma(bq0, ak[2 * k], Z4);
#pragma unroll
                for (int k = 0; k < 4; ++k) a[k] = mfma(bq1, ak[2 * k + 1], a[k]);
#pragma unroll
                for (int k = 0; k < 4; ++k) a2[k] = mfma(bq0, ak[8 + 2 * k], Z4);
#pragma unroll
                for (int k = 0; k < 4; ++k) a2[k] = mfma(bq1, ak[9 + 2 * k], a2[k]);
                *(LAS float*)(a_pw) = sel4(q4, a);
                *(LAS float*)(a_pw + 256) = sel4(q4, a2);
                sig(CW(C_QX + t), 1u);
            }
            STAMP(5);
            if (t == 0) {
                wait_lds<0>(lane < 4 ? CW(C_QX + lane) : CW(C_FLOK), 1u);
                f32x4 v0[4], v1[4];
#pragma unroll
                for (int k = 0; k < 4; ++k) { v0[k] = lds16f(a_pr + k * 512); v1[k] = lds16f(a_pr + k * 512 + 16); }
                SB();
                const float sc = 0.0625f * 1.4426950408889634f;
                const f32x4 x0 = ((v0[0] + v0[1]) + (v0[2] + v0[3])) * sc, x1 = ((v1[0] + v1[1]) + (v1[2] + v1[3])) * sc;
                st16(ws, o_h1, san4(v4u{pk_bf16(x0.x, x0.y), pk_bf16(x0.z, x0.w), pk_bf16(x1.x, x1.y), pk_bf16(x1.z, x1.w)}));
                sig(CW(C_PPUB), 1u);
            }
            STAMP_OUT();
            return;
        }
        const int pp = (wv - 7) & 1, kq = (wv - 7) >> 1;
        int o_p = WS_P + hh * 16384 + lane * 16;
        int o_s = WS_PS + (hh * 16 + RHO(4 * kq + q4)) * 128 + l15 * 4;
        int a_sw = L_ST4 + kq * 128 + l15 * 8;
        int a_p[4];
#pragma unroll
        for (int nt = 0; nt < 4; ++nt) { a_p[nt] = CSLOT(4 * kq + 3, pp) + nt * 1024 + (q4 * 16 + ((l15 + 4 * nt + (q4 & 2)) & 15)) * 16; PIN(a_p[nt]); }
        const int hd = 8 * (kq & 1) + (lane >> 3), ck = lane & 7;
        int o_h2 = WS_H2 + (hd * 2 + (i8 >> 2)) * 16384 + hh * 1024 + (2 * (i8 & 3) + 1 - pp) * 128 + ck * 16;
        int o_h2s = lane < 32 ? WS_H2S + (hh * 2 + (i8 >> 2)) * 128 + lane * 4 : OOB;
        int a_rd = pp * 4096 + (ck >> 1) * 1024 + ((ck & 1) * 32 + ((hd + 2 * ck) & 15)) * 16;
        int a_sr = L_ST4 + (kq < 2 ? hd : l15) * 8;
        PIN(o_p); PIN(o_s); PIN(a_sw); PIN(o_h2); PIN(o_h2s); PIN(a_rd); PIN(a_sr);
        v4u ac[16];
        {
            const int rr = 4 * q4 + (l15 >> 2);
            const int sc_ = ((rr >> 1) & 1) | (((rr >> 3) & 1) << 1);
            const int tb = rr * 128 + (((l15 >> 1) & 1) << 4) + 8 * (l15 & 1);
            int ta[4];
#pragma unroll
            for (int nt = 0; nt < 4; ++nt) { ta[nt] = tb + ((nt ^ sc_) << 5); PIN(ta[nt]); }
#pragma unroll
            for (int u = 0; u < 4; ++u) {
                const int cbase = CSLOT(4 * kq + u, pp);
                wait_lds<1>(CW(C_PROG + pp), 17u + 4 * kq + u);
#pragma unroll
                for (int nt = 0; nt < 4; ++nt) ac[4 * u + nt] = lds_tr<2048>(cbase + ta[nt]);
            }
            if (kq == 0) sig(CW(C_RELC + pp), 1u);
            STAMP(1);
        }
        wait_lds<1>(lane < 2 ? CW(C_PROG + lane) : CW(C_FLOK), lane < 2 ? PSTART : 1u);
        STAMP(7);
        __builtin_amdgcn_s_sleep(DLY2);
        HI();
        v4u bp[4]; unsigned pr, pl;
        const int so0 = RHO(4 * kq) * 1024, so1 = RHO(4 * kq + 1) * 1024, so2 = RHO(4 * kq + 2) * 1024, so3 = RHO(4 * kq + 3) * 1024;
#pragma unroll 1
        for (unsigned s = 0; s < SPIN_G; ++s) { PASSES(6, s);
            CBAR();
            bp[0] = __builtin_amdgcn_raw_buffer_load_b128(ws, o_p, so0, AUX_SC1); bp[1] = __builtin_amdgcn_raw_buffer_load_b128(ws, o_p, so1, AUX_SC1);
            bp[2] = __builtin_amdgcn_raw_buffer_load_b128(ws, o_p, so2, AUX_SC1); bp[3] = __builtin_amdgcn_raw_buffer_load_b128(ws, o_p, so3, AUX_SC1);
            pr = ld4(ws, o_s); pl = ld4(ws, o_s + 64);
            if (!any_sent(umax(umax4a(umax4a(pr, bp[0]), bp[1]), umax4a(umax4a(pl, bp[2]), bp[3])))) break;
        }
        sig(CW(C_CG + wv - 7), 1u);
        STAMP(8);
        {
            const float rw = xrow_max(u2f(pr));
            const float dl = rw - u2f(pr);
            const unsigned dd = (unsigned)__builtin_amdgcn_fmed3f(dl, 0.f, 255.f) * 0x00800080u;
            auto x = __builtin_amdgcn_permlane32_swap(dd, dd, false, false);
            auto y = __builtin_amdgcn_permlane16_swap(x[0], x[0], false, false);
            auto z = __builtin_amdgcn_permlane16_swap(x[1], x[1], false, false);
            const unsigned du[4] = {y[0], y[1], z[0], z[1]};
            f32x4 c[4] = {Z4, Z4, Z4, Z4};
#pragma unroll
            for (int u = 0; u < 4; ++u) {
                const u16x2 d2 = __builtin_bit_cast(u16x2, du[u]);
                const unsigned x0 = bp[u].x, x1 = bp[u].y, x2 = bp[u].z, x3 = bp[u].w;
                const v4u bs = {__builtin_bit_cast(unsigned, __builtin_elementwise_sub_sat(__builtin_bit_cast(u16x2, x0), d2)),
                                __builtin_bit_cast(unsigned, __builtin_elementwise_sub_sat(__builtin_bit_cast(u16x2, x1), d2)),
                                __builtin_bit_cast(unsigned, __builtin_elementwise_sub_sat(__builtin_bit_cast(u16x2, x2), d2)),
                                __builtin_bit_cast(unsigned, __builtin_elementwise_sub_sat(__builtin_bit_cast(u16x2, x3), d2))};
#pragma unroll
                for (int nt = 0; nt < 4; ++nt) c[nt] = mfma(ac[4 * u + nt], bs, c[nt]);
            }
            const float lw = xrow_sum(__builtin_amdgcn_exp2f(-dl) * u2f(pl));
            *(LAS f32x2*)(a_sw) = f32x2{rw, lw};
#pragma unroll
            for (int nt = 0; nt < 4; ++nt) *(LAS f32x4*)(a_p[nt]) = c[nt];
            sig(CW(C_P4 + pp * 4 + kq), 1u);
        }
        STAMP(9);
        if (kq < 2) {
            WAITF(0, C_P4 + pp * 4, 4);
            STAMP(10);
            f32x4 v0[4], v1[4]; f32x2 sx[4];
#pragma unroll
            for (int k = 0; k < 4; ++k) sx[k] = *(LAS f32x2*)(a_sr + k * 128);
#pragma unroll
            for (int k = 0; k < 4; ++k) { v0[k] = lds16f(a_rd + CSLOT(4 * k + 3, 0)); v1[k] = lds16f(a_rd + CSLOT(4 * k + 3, 0) + 256); }
            SB();
            const float R = fmx(fmx(sx[0].x, sx[1].x), fmx(sx[2].x, sx[3].x));
            float sc[4];
#pragma unroll
            for (int k = 0; k < 4; ++k) sc[k] = __builtin_amdgcn_exp2f(sx[k].x - R);
            const f32x4 a0 = (v0[0] * sc[0] + v0[1] * sc[1]) + (v0[2] * sc[2] + v0[3] * sc[3]);
            const f32x4 a1 = (v1[0] * sc[0] + v1[1] * sc[1]) + (v1[2] * sc[2] + v1[3] * sc[3]);
            st16(ws, o_h2, san4(v4u{pk_bf16(a0.x, a0.y), pk_bf16(a0.z, a0.w), pk_bf16(a1.x, a1.y), pk_bf16(a1.z, a1.w)}));
            sig(CW(C_S4 + 2 * kq + pp), 1u);
        } else if (kq == 2 && pp == 0 && (i8 & 3) == 0) {
            WAITF(0, C_P4, 4);
            f32x2 sx[4];
#pragma unroll
            for (int k = 0; k < 4; ++k) sx[k] = *(LAS f32x2*)(a_sr + k * 128);
            const float R = fmx(fmx(sx[0].x, sx[1].x), fmx(sx[2].x, sx[3].x));
            const float lg = (sx[0].y * __builtin_amdgcn_exp2f(sx[0].x - R) + sx[1].y * __builtin_amdgcn_exp2f(sx[1].x - R))
                           + (sx[2].y * __builtin_amdgcn_exp2f(sx[2].x - R) + sx[3].y * __builtin_amdgcn_exp2f(sx[3].x - R));
            st4(ws, o_h2s, san(f2u(q4 == 0 ? R : lg)));
        }
        STAMP_OUT();
        return;
    }

    const int gw = wv < 6 ? wv - 2 : wv - 7;
#define H2_SETUP() int o_h2 = WS_H2 + (hh * 2 + hf) * 16384 + gw * 2048 + lane * 16; \
        int a_w0 = L_PART + (lane >> 3) * 2048 + gw * 256 + (((lane & 7) ^ (((gw & 1) | ((gw >> 2) << 1)) << 1)) << 4); PIN(o_h2); PIN(a_w0);

    if (wv >= 11) {
        if (wv < 13) {
        const int rb = wv - 11;
        int o_p = WS_P + (hh * 16 + 2 * i8 + rb) * 1024 + lane * 16;
        int o_ps = lane < 32 ? WS_PS + (hh * 16 + 2 * i8 + rb) * 128 + lane * 4 : OOB;
        int a_pr = 16 * 4096 + rb * 2048 + lane * 16;
        int a_f = CW(C_P3 + 8 * rb) + 4 * (lane & 7);
        PIN(o_p); PIN(o_ps); PIN(a_pr); PIN(a_f);
        WAITT(2, G1START);
        HI();
        f32x4 s0, s1;
        {
            f32x4 v0[8], v1[8];
            wait_lds<0>(a_f, 1u);
            STAMP(4);
#pragma unroll
            for (int k = 0; k < 8; ++k) { v0[k] = lds16f(a_pr + k * 8192); v1[k] = lds16f(a_pr + k * 8192 + 1024); }
            SB();
            s0 = ((v0[0] + v0[1]) + (v0[2] + v0[3])) + ((v0[4] + v0[5]) + (v0[6] + v0[7]));
            s1 = ((v1[0] + v1[1]) + (v1[2] + v1[3])) + ((v1[4] + v1[5]) + (v1[6] + v1[7]));
        }
        {
            const float mx = xrow_max(fmx(fmx(fmx(s0.x, s0.y), fmx(s0.z, s0.w)), fmx(fmx(s1.x, s1.y), fmx(s1.z, s1.w))));
            const float r = __builtin_ceilf(mx);
            const float p0 = __builtin_amdgcn_exp2f(s0.x - r), p1 = __builtin_amdgcn_exp2f(s0.y - r), p2 = __builtin_amdgcn_exp2f(s0.z - r), p3 = __builtin_amdgcn_exp2f(s0.w - r);
            const float p4 = __builtin_amdgcn_exp2f(s1.x - r), p5 = __builtin_amdgcn_exp2f(s1.y - r), p6 = __builtin_amdgcn_exp2f(s1.z - r), p7 = __builtin_amdgcn_exp2f(s1.w - r);
            st16(ws, o_p, san4(v4u{pk_bf16(p0, p1), pk_bf16(p2, p3), pk_bf16(p4, p5), pk_bf16(p6, p7)}));
            const float l = xrow_sum(((p0 + p1) + (p2 + p3)) + ((p4 + p5) + (p6 + p7)));
            st4(ws, o_ps, san(f2u(q4 == 0 ? r : l)));
            sig(CW(C_PPUB + rb), 1u);
        }
        STAMP(5);
        __builtin_amdgcn_s_setprio(0);
        }
        H2_SETUP();
        WAITF(2, C_PPUB, 2);
        __builtin_amdgcn_s_sleep(DLY3);
        HI();
        v4u g2[2];
#pragma unroll 1
        for (unsigned s = 0; s < SPIN_G; ++s) { PASSES(1, s);
            CBAR();
            g2[0] = ld16(ws, o_h2); g2[1] = ld16(ws, o_h2 + 1024);
            if (!any_sent(umax4a(umax4(g2[0]), g2[1]))) break;
        }
        *(LAS v4u*)(a_w0) = g2[0];
        *(LAS v4u*)(a_w0 + 128) = g2[1];
        sig(CW(C_G2 + gw), 1u);
        STAMP_OUT();
        return;
    }

    v4u ar[16];
    {
        const int w = wv < 6 ? wv - 2 : wv - 3;
        v4u g[4];
        int a_r0 = 2 * w * 4096 + row_a0, a_r1 = a_r0 ^ 64;
        const int pw = L_PART + w * 2048;
        int o_q = WS_H1 + ((2 * i8 + 2 * w) & 15) * 2048 + lane * 16;
        int a_qw = pw + (lane >> 3) * 128 + (((lane & 7) ^ (q4 & 1) << 1) << 4), a_qw1 = (a_qw ^ 64) + 1024;
        int a_q0 = pw + row_a0, a_q1 = pw + row_a1, a_pw = (16 + 2 * w) * 4096 + lane * 16;
        PIN(a_r0); PIN(a_r1); PIN(o_q); PIN(a_qw); PIN(a_qw1); PIN(a_q0); PIN(a_q1); PIN(a_pw);
#define PULL_R(rb) do { _Pragma("unroll") for (int z = 0; z < 2; ++z) { \
            ar[8 * (rb) + 4 * z] = lds16(a_r0 + (rb) * 65536 + z * 4096); ar[8 * (rb) + 4 * z + 1] = lds16(a_r0 + (rb) * 65536 + z * 4096 + 2048); \
            ar[8 * (rb) + 4 * z + 2] = lds16(a_r1 + (rb) * 65536 + z * 4096); ar[8 * (rb) + 4 * z + 3] = lds16(a_r1 + (rb) * 65536 + z * 4096 + 2048); } } while (0)
#define MM_R(rb) do { f32x4 s0 = Z4, s1 = Z4; _Pragma("unroll") for (int z = 0; z < 2; ++z) { \
            s0 = mfma(ar[8 * (rb) + 4 * z], g[2 * z], s0); s1 = mfma(ar[8 * (rb) + 4 * z + 1], g[2 * z], s1); \
            s0 = mfma(ar[8 * (rb) + 4 * z + 2], g[2 * z + 1], s0); s1 = mfma(ar[8 * (rb) + 4 * z + 3], g[2 * z + 1], s1); } \
            *(LAS f32x4*)(a_pw + (rb) * 2048) = s0; *(LAS f32x4*)(a_pw + (rb) * 2048 + 1024) = s1; sig(CW(C_P3 + 8 * (rb) + w), 1u); } while (0)
        WAITT(1, 1u + w); PULL_R(0); sig(CW(C_RELR + w), 1u);
        wait_lds<1>(lane < 2 ? CW(C_PROG + lane) : CW(C_FLOK), lane < 2 ? G1START : 1u);
        STAMP(1);
        HI();
#pragma unroll
        for (int k = 0; k < 4; ++k) g[k] = ld16(ws, o_q + k * 1024);
        WAITT(0, 9u + w); PULL_R(1);
#pragma unroll 1
        for (unsigned s = 0; s < SPIN_G; ++s) { PASSES(5, s);
            if (!any_sent(umax(umax4a(umax4(g[0]), g[1]), umax4a(umax4(g[2]), g[3])))) break;
            __builtin_amdgcn_s_sleep(RETRY1);
            CBAR();
#pragma unroll
            for (int k = 0; k < 4; ++k) g[k] = ld16(ws, o_q + k * 1024);
        }
        STAMP(2);
#pragma unroll
        for (int k = 0; k < 2; ++k) {
            *(LAS v4u*)(a_qw) = g[2 * k]; *(LAS v4u*)(a_qw1) = g[2 * k + 1];
            CBAR();
            g[2 * k] = lds16(a_q0); g[2 * k + 1] = lds16(a_q1);
            CBAR();
        }
        MM_R(0); MM_R(1);
        __builtin_amdgcn_s_setprio(0);
        STAMP(5);
    }

    if (wv < 6) {
        const int t = wv - 2;
        v4u (&ak)[16] = ar;
        WAITT(2, 24u);
        {
            const int tra = TR_A(t);
#pragma unroll
            for (int s = 0; s < 16; ++s) ak[s] = lds_tr<512>(tra + s * 4096);
        }
        H2_SETUP();
        int o_st = lane < 8 ? WS_H2S + ((4 * t + (lane >> 1)) * 2 + hf) * 128 + (lane & 1) * 64 + hh * 4 : OOB;
        int a_sw = lane < 8 ? L_ST5 + (lane & 1) * 64 + (4 * t + (lane >> 1)) * 4 : L_X + lane * 4;
        int a_r0 = L_PART + row_a0, a_r1 = L_PART + row_a1, a_sr = L_ST5 + q4 * 16;
        int a_cw = L_CTX + (16 * t + l15) * 2;
        int a_g2 = CW(C_G2) + 4 * (lane & 7);
        PIN(o_st); PIN(a_sw); PIN(a_r0); PIN(a_r1); PIN(a_sr); PIN(a_cw); PIN(a_g2);
        STAMP(8);
        WAITF(1, C_PPUB, 2);
        __builtin_amdgcn_s_sleep(DLY3V);
        HI();
        STAMP(9);
        {
            v4u g2[2]; unsigned st;
#pragma unroll 1
            for (unsigned s = 0; s < SPIN_G; ++s) { PASSES(1, s);
                CBAR();
                g2[0] = ld16(ws, o_h2); g2[1] = ld16(ws, o_h2 + 1024); st = ld4(ws, o_st);
                if (!any_sent(umax4a(umax4a(st, g2[0]), g2[1]))) break;
            }
            *(LAS v4u*)(a_w0) = g2[0];
            *(LAS v4u*)(a_w0 + 128) = g2[1];
            *(LAS unsigned*)(a_sw) = st;
            sig(CW(C_G2 + gw), 1u);
        }
        STAMP(10);
        wait_lds<0>(a_g2, 1u);
        STAMP(11);
        {
            const f32x4 rg = lds16f(a_sr), lg = lds16f(a_sr + 64);
            f32x4 acc = Z4, acc2 = Z4;
#pragma unroll
            for (int hb = 0; hb < 2; ++hb) {
                v4u bb[8];
#pragma unroll
                for (int k = 0; k < 4; ++k) { bb[2 * k] = lds16(a_r0 + (4 * hb + k) * 2048); bb[2 * k + 1] = lds16(a_r1 + (4 * hb + k) * 2048); }
#pragma unroll
                for (int k = 0; k < 4; ++k) { acc = mfma(bb[2 * k], ak[8 * hb + 2 * k], acc); acc2 = mfma(bb[2 * k + 1], ak[8 * hb + 2 * k + 1], acc2); }
            }
            const float R = xrow_max(fmx(fmx(rg.x, rg.y), fmx(rg.z, rg.w)));
            const f32x4 sc = {__builtin_amdgcn_exp2f(rg.x - R), __builtin_amdgcn_exp2f(rg.y - R), __builtin_amdgcn_exp2f(rg.z - R), __builtin_amdgcn_exp2f(rg.w - R)};
            const float il = __builtin_amdgcn_rcpf(xrow_sum((sc.x * lg.x + sc.y * lg.y) + (sc.z * lg.z + sc.w * lg.w)));
            acc = (acc + acc2) * sc;
            const float cx = xrow_sum((acc.x + acc.y) + (acc.z + acc.w)) * il;
            *(LAS u16*)(a_cw) = (u16)pk_bf16(cx, cx);
            sig(CW(C_CTX + t), 1u);
        }
        STAMP_OUT();
        return;
    }

    {
        const int v = wv - 7;
        int cb0 = 2 * v + 4 * hf; if (cb0 >= 7) cb0 -= 7;
        int cb1 = cb0 + 1; if (cb1 >= 7) cb1 -= 7;
        int o_h40 = WS_H4 + (4 * cb0 + q4) * 8192 + e * 64 + l15 * 4;
        int o_h41 = v < 3 ? WS_H4 + (4 * cb1 + q4) * 8192 + e * 64 + l15 * 4 : OOB;
        int a_c = L_CTX + q4 * 16, a_cf = CW(C_CTX) + 4 * (lane & 3);
        PIN(a_c); PIN(a_cf); PIN(o_h40); PIN(o_h41);
        WAITT(2, v < 3 ? 26u + 2 * v : 31u);
        {
            const int ob0 = (v == 0 ? 32 : 14 + 4 * v) * 4096, ob2 = (v < 3 ? 16 + 4 * v : 26) * 4096;
#pragma unroll
            for (int nt = 0; nt < 4; ++nt) {
                ar[nt] = lds_tr<512>(ob0 + TR_A(nt)); ar[4 + nt] = lds_tr<512>(ob0 + 4096 + TR_A(nt));
                ar[8 + nt] = lds_tr<512>(ob2 + TR_A(nt)); ar[12 + nt] = lds_tr<512>(ob2 + 4096 + TR_A(nt));
            }
        }
        HI();
        STAMP(12);
        wait_lds<0>(a_cf, 1u);
        STAMP(13);
        {
            const v4u bc0 = lds16(a_c), bc1 = lds16(a_c + 64);
            f32x4 a[4], a2[4];
#pragma unroll
            for (int nt = 0; nt < 4; ++nt) a[nt] = mfma(bc0, ar[nt], Z4);
#pragma unroll
            for (int nt = 0; nt < 4; ++nt) a[nt] = mfma(bc1, ar[4 + nt], a[nt]);
#pragma unroll
            for (int nt = 0; nt < 4; ++nt) a2[nt] = mfma(bc0, ar[8 + nt], Z4);
#pragma unroll
            for (int nt = 0; nt < 4; ++nt) a2[nt] = mfma(bc1, ar[12 + nt], a2[nt]);
            st4(ws, o_h40, san(f2u(sel4(q4, a))));
            st4(ws, o_h41, san(f2u(sel4(q4, a2))));
        }
        STAMP_OUT();
    }
}

extern "C" void kernel_launch(void* const* d_in, const int* in_sizes, int n_in,
                              void* d_out, int out_size, void* d_ws, size_t ws_size,
                              hipStream_t stream) {
    Params p;
    p.x = d_in[0]; p.ckv = d_in[1]; p.wq = d_in[2]; p.wuk = d_in[3]; p.wuv = d_in[4]; p.wo = d_in[5];
    p.w1 = d_in[7]; p.b1 = d_in[8]; p.w2 = d_in[9]; p.b2 = d_in[10]; p.out = d_out; p.ws = (char*)d_ws + (size_t)WSOFF;
    (void)hipFuncSetAttribute((const void*)fused_decode, hipFuncAttributeMaxDynamicSharedMemorySize, L_END);
    fused_decode<<<256, NWAVE * 64, L_END, stream>>>(p);
}
```

```cpp
#include <hip/hip_runtime.h>
#include <stdint.h>

typedef unsigned v4u __attribute__((ext_vector_type(4)));
typedef unsigned v2u __attribute__((ext_vector_type(2)));
typedef float f32x4 __attribute__((ext_vector_type(4)));
typedef float f32x2 __attribute__((ext_vector_type(2)));
typedef __bf16 bf16x8 __attribute__((ext_vector_type(8)));
typedef __bf16 bf16x2 __attribute__((ext_vector_type(2)));
typedef short s16x4 __attribute__((ext_vector_type(4)));
typedef unsigned short u16x2 __attribute__((ext_vector_type(2)));
typedef unsigned short u16;
#define LAS __attribute__((address_space(3)))
#define GAS __attribute__((address_space(1)))
typedef __amdgpu_buffer_rsrc_t rsrc_t;

constexpr int WF = 4;
constexpr int WS = 0;
constexpr int NSLOW = 2;
constexpr int WV = 1;
constexpr unsigned G1START = 11;
constexpr unsigned FSTART = 7;
constexpr int DLY0 = 22, DLYF = 40, DLY3 = 25, DLYG = 40;
constexpr int HSHIFT = 3;
constexpr int CODE_BYTES = 20480;

constexpr int WS_EPOCH = 0;
constexpr int WS_FLAG = 4096;
constexpr int WS_H0 = WS_FLAG + 16384;
constexpr int WS_H1 = WS_H0 + 65536;
constexpr int WS_P = WS_H1 + 32768;
constexpr int WS_PS = WS_P + 262144;
constexpr int WS_PL = WS_PS + 32768;
constexpr int WS_H2 = WS_PL + 32768;
constexpr int WS_H2S = WS_H2 + 524288;
constexpr int WS_H4 = WS_H2S + 4096;
constexpr int WS_HID = WS_H4 + 28 * 8192;
constexpr int WS_GP = WS_HID + 98 * 512;
constexpr int WS_END = WS_GP + 14 * 1792;
static_assert(WS_END - WS_H1 <= 128 * 10 * 1024, "ten 1 KB pieces per filling workgroup");

constexpr int L_PART = 34 * 4096;
constexpr int L_XN = L_PART + 16384;
constexpr int L_Q = L_XN + 1024;
constexpr int L_CTX = L_Q + 512;
constexpr int L_HID = L_CTX + 256;
constexpr int L_ST4 = L_HID + 256;
constexpr int L_ST5 = L_ST4 + 512;
constexpr int L_X = L_ST5 + 128;
constexpr int L_E = L_X + 1024;
constexpr int L_CTL = L_E + 16;
constexpr int L_END = L_CTL + 512;
constexpr int L_ZERO = L_CTL + 384;
constexpr int L_S2P = L_PART;
enum { C_PROG = 0  , C_X = 2, C_FLOK = 3, C_PPUB = 4  , C_FILL = 8  , C_QX = 20  , C_RELQ = 28  ,
       C_RELR = 32  , C_RELC = 40  , C_P3 = 80  , C_P4 = 48  , C_CG = 56  , C_G2 = 64  , C_CTX = 72  , C_S4 = 76  , C_NUM = 96 };

constexpr int NWAVE = 15;
constexpr unsigned SENT = 0xFFFFFFFFu;
constexpr int AUX_SC1 = 16;
constexpr unsigned SPIN_G = 1u << 15;

struct Params {
    const void* x; const void* ckv; const void* wq; const void* wuk; const void* wuv; const void* wo;
    const void* w1; const void* b1; const void* w2; const void* b2; void* out; void* ws;
};

__device__ __forceinline__ float u2f(unsigned u) { return __builtin_bit_cast(float, u); }
__device__ __forceinline__ unsigned f2u(float f) { return __builtin_bit_cast(unsigned, f); }
__device__ __forceinline__ unsigned pk_bf16(float a, float b) { f32x2 v = {a, b}; bf16x2 r = __builtin_convertvector(v, bf16x2); return __builtin_bit_cast(unsigned, r); }
__device__ __forceinline__ float rbf(float a) { return u2f(pk_bf16(a, a) << 16); }
__device__ __forceinline__ float bflo(unsigned u) { return u2f(u << 16); }
__device__ __forceinline__ float bfhi(unsigned u) { return u2f(u & 0xFFFF0000u); }
__device__ __forceinline__ unsigned san(unsigned u) { return u < 0xFFFFFFFEu ? u : 0xFFFFFFFEu; }
__device__ __forceinline__ v4u san4(v4u v) { return v4u{san(v.x), san(v.y), san(v.z), san(v.w)}; }
__device__ __forceinline__ f32x4 u4f(v4u v) { return __builtin_bit_cast(f32x4, v); }
__device__ __forceinline__ unsigned umax(unsigned a, unsigned b) { return a > b ? a : b; }
__device__ __forceinline__ unsigned umax4(v4u v) { return umax(umax(v.x, v.y), umax(v.z, v.w)); }
__device__ __forceinline__ unsigned umax4a(unsigned m, v4u v) { return umax(umax(umax(umax(m, v.x), v.y), v.z), v.w); }
__device__ __forceinline__ float fmx(float a, float b) { return __builtin_fmaxf(a, b); }
__device__ __forceinline__ float fmx3(float a, float b, float c) { return fmx(fmx(a, b), c); }
__device__ __forceinline__ f32x4 mfma(v4u a, v4u b, f32x4 c) {
    return __builtin_amdgcn_mfma_f32_16x16x32_bf16(__builtin_bit_cast(bf16x8, a), __builtin_bit_cast(bf16x8, b), c, 0, 0, 0);
}
template <int N> __device__ __forceinline__ float ror(float v) { return u2f((unsigned)__builtin_amdgcn_update_dpp(0, (int)f2u(v), 0x120 + N, 0xf, 0xf, false)); }
__device__ __forceinline__ float row_sum(float v) { v += ror<8>(v); v += ror<4>(v); v += ror<2>(v); v += ror<1>(v); return v; }
__device__ __forceinline__ float xrow_sum(float v) {
    auto a = __builtin_amdgcn_permlane32_swap(f2u(v), f2u(v), false, false); v = u2f(a[0]) + u2f(a[1]);
    auto b = __builtin_amdgcn_permlane16_swap(f2u(v), f2u(v), false, false); return u2f(b[0]) + u2f(b[1]);
}
__device__ __forceinline__ float xrow_max(float v) {
    auto a = __builtin_amdgcn_permlane32_swap(f2u(v), f2u(v), false, false); v = fmx(u2f(a[0]), u2f(a[1]));
    auto b = __builtin_amdgcn_permlane16_swap(f2u(v), f2u(v), false, false); return fmx(u2f(b[0]), u2f(b[1]));
}
__device__ __forceinline__ float wave_sum(float v) { return xrow_sum(row_sum(v)); }

__device__ __forceinline__ float sel4(int q, const f32x4 (&a)[4]) { const float x0 = a[0].x, x1 = a[1].x, x2 = a[2].x, x3 = a[3].x; return q == 0 ? x0 : q == 1 ? x1 : q == 2 ? x2 : x3; }
__device__ __forceinline__ float quad_sum4(f32x4 v) {
    auto a = __builtin_amdgcn_permlane32_swap(f2u(v.x), f2u(v.y), false, false); const float xy = u2f(a[0]) + u2f(a[1]);
    auto b = __builtin_amdgcn_permlane32_swap(f2u(v.z), f2u(v.w), false, false); const float zw = u2f(b[0]) + u2f(b[1]);
    auto c = __builtin_amdgcn_permlane16_swap(f2u(xy), f2u(zw), false, false);
    float r = u2f(c[0]) + u2f(c[1]); r += ror<8>(r); r += ror<4>(r); return r;
}
__device__ __forceinline__ rsrc_t mk_rsrc(const void* p, int bytes) { return __builtin_amdgcn_make_buffer_rsrc((void*)p, 0, bytes, 0x00020000); }
__device__ __forceinline__ v4u ld16(rsrc_t r, int off) { return __builtin_amdgcn_raw_buffer_load_b128(r, off, 0, AUX_SC1); }
__device__ __forceinline__ unsigned ld4(rsrc_t r, int off) { return __builtin_amdgcn_raw_buffer_load_b32(r, off, 0, AUX_SC1); }
__device__ __forceinline__ void st16(rsrc_t r, int off, v4u v) { __builtin_amdgcn_raw_buffer_store_b128(v, r, off, 0, AUX_SC1); }
__device__ __forceinline__ void st4(rsrc_t r, int off, unsigned v) { __builtin_amdgcn_raw_buffer_store_b32(v, r, off, 0, AUX_SC1); }
__device__ __forceinline__ void st8(rsrc_t r, int off, v2u v) { __builtin_amdgcn_raw_buffer_store_b64(v, r, off, 0, AUX_SC1); }
constexpr int OOB = 0x40000000;
#define CBAR() asm volatile("" ::: "memory")
#define SB() __builtin_amdgcn_sched_barrier(0)
#define HI() __builtin_amdgcn_s_setprio(3)
#define PIN(x) asm volatile("" : "+v"(x))
#define DRAIN() asm volatile("s_waitcnt vmcnt(0)" ::: "memory")
#define LWAIT() asm volatile("s_waitcnt lgkmcnt(0)" ::: "memory")
#define LDONE() __builtin_amdgcn_s_waitcnt(0xC07F)

__device__ __forceinline__ v4u lds16(int off) { return *(LAS v4u*)off; }
__device__ __forceinline__ f32x4 lds16f(int off) { return *(LAS f32x4*)off; }
template <int D2> __device__ __forceinline__ v4u lds_tr(int off) {
    s16x4 a = __builtin_amdgcn_ds_read_tr16_b64_v4i16((LAS s16x4*)off);
    s16x4 b = __builtin_amdgcn_ds_read_tr16_b64_v4i16((LAS s16x4*)(off + D2));
    v2u a2 = __builtin_bit_cast(v2u, a), b2 = __builtin_bit_cast(v2u, b);
    return v4u{a2.x, a2.y, b2.x, b2.y};
}
template <int SLEEP> __device__ __forceinline__ void wait_lds(int addr, unsigned want) {
    unsigned v, c;
    if (SLEEP)
        asm volatile("s_mov_b32 %1, 0x40000\n1:\n\tds_read_b32 %0, %2\n\ts_sub_u32 %1, %1, 1\n\ts_waitcnt lgkmcnt(0)\n\tv_cmp_gt_u32 vcc, %3, %0\n\t"
                     "s_cbranch_scc1 2f\n\ts_cbranch_vccz 2f\n\ts_sleep %4\n\ts_branch 1b\n2:"
                     : "=&v"(v), "=&s"(c) : "v"(addr), "v"(want), "i"(SLEEP) : "memory", "vcc", "scc");
    else
        asm volatile("s_mov_b32 %1, 0x80000\n1:\n\tds_read_b32 %0, %2\n\ts_sub_u32 %1, %1, 1\n\ts_waitcnt lgkmcnt(0)\n\tv_cmp_gt_u32 vcc, %3, %0\n\t"
                     "s_cbranch_scc1 2f\n\ts_cbranch_vccnz 1b\n2:"
                     : "=&v"(v), "=&s"(c) : "v"(addr), "v"(want) : "memory", "vcc", "scc");
}
#define CW(word) (L_CTL + 4 * (word))
#define WAITP(S, word, want) wait_lds<S>(CW(word), (want))
#define WAITF(S, word, n) wait_lds<S>(CW(word) + 4 * (lane & ((n) - 1)), 1u)
__device__ __forceinline__ void sig(int addr, unsigned v) { asm volatile("ds_write_b32 %0, %1" :: "v"(addr), "v"(v) : "memory"); }
__device__ __forceinline__ bool any_sent(unsigned m) { return __builtin_amdgcn_ballot_w64(m == SENT) != 0ull; }
__device__ __forceinline__ bool any_nz(unsigned m) { return __builtin_amdgcn_ballot_w64(m != 0u) != 0ull; }

__device__ __forceinline__ void wait_vm(int n) {
    switch (n) {
#define VMC(x) case x: asm volatile("s_waitcnt vmcnt(" #x ")" ::: "memory"); break;
        VMC(0) VMC(4) VMC(8) VMC(12) VMC(16) VMC(20) VMC(24) VMC(28) VMC(32) VMC(36) VMC(40)
    }
}
__device__ __forceinline__ void dma_tile(rsrc_t r, unsigned soff, unsigned lds, unsigned v0, unsigned v1, unsigned v2, unsigned v3) {
    asm volatile("s_mov_b32 m0, %6\n\ts_nop 0\n\t"
                 "buffer_load_dwordx4 %0, %4, %5 offen lds\n\t"
                 "buffer_load_dwordx4 %1, %4, %5 offen offset:1024 lds\n\t"
                 "buffer_load_dwordx4 %2, %4, %5 offen offset:2048 lds\n\t"
                 "buffer_load_dwordx4 %3, %4, %5 offen offset:3072 lds"
                 :: "v"(v0), "v"(v1), "v"(v2), "v"(v3), "s"(r), "s"(soff), "s"(lds) : "memory");
}

template <int W>
__device__ __forceinline__ void dma_seg(rsrc_t r, int& n, int end, unsigned ts, unsigned tl, int& vp, int prog, unsigned v0, unsigned v1, unsigned v2, unsigned v3) {
    unsigned so, sl; int t;
    asm volatile("s_nop 1\n\tv_readlane_b32 %[so], %[ts], %[n]\n\tv_readlane_b32 %[sl], %[tl], %[n]\n\ts_nop 3\n"
                 "1:\n\ts_mov_b32 m0, %[sl]\n\ts_add_u32 %[n], %[n], 1\n\t"
                 "buffer_load_dwordx4 %[v0], %[r], %[so] offen lds\n\t"
                 "buffer_load_dwordx4 %[v1], %[r], %[so] offen offset:1024 lds\n\t"
                 "buffer_load_dwordx4 %[v2], %[r], %[so] offen offset:2048 lds\n\t"
                 "buffer_load_dwordx4 %[v3], %[r], %[so] offen offset:3072 lds\n\t"
                 "v_readlane_b32 %[sl], %[tl], %[n]\n\tv_readlane_b32 %[so], %[ts], %[n]\n\t"
                 "s_sub_u32 %[t], %[n], %[w]\n\tv_max_i32 %[vp], %[t], %[vp]\n\t"
                 "s_waitcnt vmcnt(%[w4])\n\tds_write_b32 %[pa], %[vp]\n\t"
                 "s_cmp_lt_i32 %[n], %[e]\n\ts_cbranch_scc1 1b"
                 : [n] "+s"(n), [vp] "+v"(vp), [so] "=&s"(so), [sl] "=&s"(sl), [t] "=&s"(t)
                 : [r] "s"(r), [e] "s"(end), [ts] "v"(ts), [tl] "v"(tl), [pa] "v"(prog), [v0] "v"(v0), [v1] "v"(v1), [v2] "v"(v2), [v3] "v"(v3), [w] "i"(W), [w4] "i"(4 * W)
                 : "memory", "scc");
}

__global__ void __launch_bounds__(NWAVE * 64, 4) fused_decode(Params p) {
    extern __shared__ __attribute__((aligned(16))) unsigned char smem[];
    unsigned long long pc0;
    asm volatile("s_getpc_b64 %0" : "=s"(pc0));
    const int lane = threadIdx.x & 63, wv = __builtin_amdgcn_readfirstlane(threadIdx.x >> 6);
    const int b = blockIdx.x, jj = (b >> 3) & 15;
    const bool ecu = b < 128;
    const int hh = 2 * (b & 7) + (jj >> 3);
    const int i8 = jj & 7, dq = i8 >> 1, hf = i8 & 1;
    const int e = (b & 7) * 16 + jj;
    const int l15 = lane & 15, q4 = lane >> 4;
#define CSLOT(mm, pq) (((mm) < 12 ? 2 * (mm) + (pq) : (mm) == 12 ? 32 + (pq) : 2 * (mm) - 26 + (pq)) * 4096)
#define KROT ((hh & 1) ? i8 : 3)
#define RHO(mm) (2 * ((i8 + 1 + (mm)) & 7) + 1 - ((mm) >> 3))
    if (__builtin_expect(wv < 2, 1)) {
        const int pp = wv;
        asm volatile("s_mov_b32 m0, %2\n\ts_nop 0\n\tbuffer_load_dwordx4 %0, %1, 0 offen lds"
                     :: "v"((jj * 2 + pp) * 1024 + lane * 16), "s"(mk_rsrc((const void*)(pc0 & ~255ull), ecu ? CODE_BYTES : 0)), "s"(33 * 4096) : "memory");
        const unsigned r = lane >> 3, pc = lane & 7, s0 = (r >> 1) & 1;
        const unsigned ce = (pc ^ (s0 << 1)) << 4, co = (pc ^ ((s0 | 2) << 1)) << 4;
#define VOFFS(st) (r * (st) + ce), ((r + 8) * (st) + co - 1024), ((r + 16) * (st) + ce - 2048), ((r + 24) * (st) + co - 3072)
        int n = 0, vp = 0;
        const int prog = CW(C_PROG + pp);
        __builtin_amdgcn_s_setprio(1);
#define DOWN(W) do { wait_vm(4 * (W)); vp = vp > n - (W) ? vp : n - (W); sig(prog, vp); } while (0)
#define BLOCK(addr) do { const int a_ = (addr); _Pragma("unroll") for (int x_ = WF - 1; x_ >= 0; --x_) { if (__builtin_amdgcn_ballot_w64(*(volatile LAS unsigned*)a_ == 0u) == 0ull) break; DOWN(x_); } wait_lds<0>(a_, 1u); } while (0)
#define DRAINW(W, T) do { _Pragma("unroll") for (int x_ = (W) - 1; x_ >= (T); --x_) DOWN(x_); } while (0)
        if (pp == 0)
            asm volatile("s_mov_b32 m0, %3\n\ts_nop 0\n\tbuffer_load_dwordx4 %0, %1, 0 offen lds\n\t"
                         "s_mov_b32 m0, %4\n\ts_mov_b64 exec, 1\n\tbuffer_load_dwordx4 %0, %2, 0 offen sc1 lds\n\ts_mov_b64 exec, -1"
                         :: "v"(lane * 16), "s"(mk_rsrc(p.x, ecu ? 896 : 0)), "s"(mk_rsrc(p.ws, 16)), "s"(L_X), "s"(L_E) : "memory");
        if (__builtin_expect(ecu, 1)) {
            const rsrc_t rq = mk_rsrc(p.wq, 448 * 8192);
            const unsigned so = (224 * hf + 32 * pp) * 8192 + (((hh + HSHIFT) & 15) * 256 + dq * 64) * 2;
            dma_tile(rq, so, pp * 4096, VOFFS(8192));
            dma_tile(rq, so + 64 * 8192, (2 + pp) * 4096, VOFFS(8192));
            dma_tile(rq, so + 128 * 8192, (4 + pp) * 4096, VOFFS(8192));
            dma_tile(mk_rsrc(p.wq, pp ? 0 : 448 * 8192), so + 192 * 8192, (6 + pp) * 4096, VOFFS(8192));
            __builtin_amdgcn_s_barrier();
            SB();
            const int e2 = e - 30;
            const int k = lane - 4, mm = lane - 16;
            unsigned ts = (128 * i8 + 32 * pp + (k & 1) * 64) * 8192 + hh * 512 + (((k >> 1) + KROT) & 3) * 128, tl = (2 * lane + pp) * 4096;
            if (lane >= 12) ts = e < 98 ? (64 * (e % 7) + 32 * pp) * 3584 + (e / 7) * 256 + (lane & 1) * 128 : 0;
            if (lane >= 14) ts = e2 >= 0 ? (128 * (e2 / 7) + 32 * pp + (lane & 1) * 64) * 896 + (e2 % 7) * 128 : 0;
            if (lane >= 16) { ts = (512 * hh + 32 * RHO(mm)) * 2048 + (2 * i8 + 1 - pp) * 128; tl = CSLOT(mm, pp); }
            if (pp == 0) { asm volatile("s_waitcnt vmcnt(16)" ::: "memory"); sig(CW(C_X), 1u); }
            n = 4;
            dma_seg<WF>(mk_rsrc(p.wuk, 1024 * 8192), n, 12, ts, tl, vp, prog, VOFFS(8192));
            const rsrc_t r1 = mk_rsrc(p.w1, e < 98 ? 448 * 3584 : 0), r2 = mk_rsrc(p.w2, e2 >= 0 ? 1792 * 896 : 0);
            const rsrc_t rc = mk_rsrc(p.ckv, 8192 * 2048);
            BLOCK(CW(C_PPUB));
            dma_seg<WF>(r1, n, 14, ts, tl, vp, prog, VOFFS(3584));
            dma_seg<WF>(r2, n, 16, ts, tl, vp, prog, VOFFS(896));
            dma_seg<WF>(rc, n, 29, ts, tl, vp, prog, VOFFS(2048));
            WAITF(0, C_RELC, 2);
            dma_seg<WF>(rc, n, 32, ts, tl, vp, prog, VOFFS(2048));
        } else {
            {
                const rsrc_t wsr = mk_rsrc(p.ws, WS_END);
#pragma unroll
                for (int k = 0; k < 5; ++k) __builtin_amdgcn_raw_buffer_store_b128(v4u{SENT, SENT, SENT, SENT}, wsr, lane * 16, WS_H1 + ((2 * k + pp) * 128 + e) * 1024, AUX_SC1);
            }
            __builtin_amdgcn_s_barrier();
            int cb = lane - 24 + 4 * hf; if (cb >= 7) cb -= 7;
            unsigned ts = (512 * hh + 64 * i8 + (lane >> 3) * 32) * 2048 + ((2 * i8 + 2 * lane + pp) & 15) * 128, tl = 2 * lane + pp;
            if (lane >= 16) { ts = (512 * hf + 32 * pp + (lane - 16) * 64) * 8192 + (hh * 256 + dq * 64) * 2; tl = 2 * lane + pp - 32; }
            if (lane >= 24) { ts = (hh * 256 + dq * 64 + 32 * pp) * 896 + cb * 128; tl = lane == 24 ? 32 + pp : 2 * lane + pp - 34; }
            tl *= 4096;
            const rsrc_t rc = mk_rsrc(p.ckv, 8192 * 2048);
            DRAIN(); sig(CW(C_FILL + pp), 1u);
            dma_seg<WS>(rc, n, NSLOW, ts, tl, vp, prog, VOFFS(2048));
            dma_seg<WF>(rc, n, 16, ts, tl, vp, prog, VOFFS(2048));
            BLOCK(CW(C_RELR) + 4 * (lane & 7));
            const rsrc_t rv = mk_rsrc(p.wuv, 1024 * 8192);
            DRAINW(WF, WV);
#pragma unroll 1
            while (n < 24 && __builtin_amdgcn_ballot_w64(*(volatile LAS unsigned*)(CW(C_PPUB) + 4 * (lane & 1)) == 0u) != 0ull)
                dma_seg<WV>(rv, n, n + 1, ts, tl, vp, prog, VOFFS(8192));
            if (n < 24) dma_seg<WF>(rv, n, 24, ts, tl, vp, prog, VOFFS(8192));
            const rsrc_t ro = mk_rsrc(p.wo, 4096 * 896);
            dma_seg<WF>(ro, n, 25, ts, tl, vp, prog, VOFFS(896));
            BLOCK(CW(C_PPUB) + 4 * (lane & 1));
            dma_seg<WF>(ro, n, 31, ts, tl, vp, prog, VOFFS(896));
        }
        DRAINW(WF, 0);
        return;
    }

    if (wv == 2) { *(LAS v2u*)(L_CTL + lane * 8) = v2u{0u, 0u}; LWAIT(); }
    __builtin_amdgcn_s_barrier();
    const rsrc_t ws = mk_rsrc(p.ws, WS_END);
    const f32x4 Z4 = {0.f, 0.f, 0.f, 0.f};
    const int swr = ((l15 >> 1) & 1) | (((l15 >> 3) & 1) << 1);
    const int row_a0 = l15 * 128 + ((q4 ^ (swr << 1)) << 4), row_a1 = row_a0 ^ 64;
    const int swt = ((l15 >> 3) & 1) | ((q4 & 1) << 1);
    const int tr_b = (8 * q4 + (l15 >> 2)) * 128 + (((l15 >> 1) & 1) << 4) + 8 * (l15 & 1);
#define TR_A(nt) (tr_b + ((((nt) ^ swt)) << 5))
#define WAITT(S, k) wait_lds<S>(CW(C_PROG) + 4 * (lane & 1), (k))


    if (wv == 6) {
        wait_lds<0>(ecu ? CW(C_X) : CW(C_FILL + (lane & 1)), 1u);
        const v2u ep = *(LAS v2u*)(L_E);
        const unsigned tagA = ep.x + 1u, tagB = (ep.y + 0x9E3779B9u) | 1u;
        if (!ecu) st16(ws, lane < 8 ? WS_FLAG + e * 128 + lane * 16 : OOB, v4u{tagA, tagB, tagA, tagB});
        unsigned tv = 0;
        {
            const int e2 = e - 30;
            const GAS char* a = (const GAS char*)p.ckv + (512 * hh + 64 * i8 + (ecu ? 63 : 0)) * 2048 + 2 * i8 * 128;
            if (ecu) {
                if (lane == 1 && e < 98) a = (const GAS char*)p.w1 + 64 * (e % 7) * 3584 + (e / 7) * 256;
                if (lane == 2 && e2 >= 0) a = (const GAS char*)p.w2 + 128 * (e2 / 7) * 896 + (e2 % 7) * 128;
            } else {
                if (lane == 1 || lane == 2) a = (const GAS char*)p.wuv + (512 * hf + 256 * (lane - 1)) * 8192 + (hh * 256 + dq * 64) * 2;
                if (lane == 3) a = (const GAS char*)p.wuv + (512 * hf + 511) * 8192 + (hh * 256 + dq * 64) * 2;
                if (lane == 4) a = (const GAS char*)p.wo + (hh * 256 + dq * 64) * 896 + 4 * hf * 128;
            }
            if (lane < 5) tv = __builtin_nontemporal_load((const GAS unsigned*)a);
        }
        unsigned tb = 0;
        if (!ecu && jj < 2) {
            const GAS char* a = lane < 28 ? (const GAS char*)p.b1 + lane * 128 : (const GAS char*)p.b2 + (lane - 28) * 128;
            if (lane < 35) tb = *(const GAS unsigned*)a;
        }
        const int u = e < 28 ? e : -1;
        const int col = 16 * (u >= 0 ? u : 0) + 4 * (lane & 3) + 2 * (q4 & 1) + (q4 >> 1);
        const unsigned xv = *(LAS u16*)(L_X + col * 2);
        if (ecu) WAITT(2, FSTART); else __builtin_amdgcn_s_sleep(DLYF);
        HI();
#pragma unroll 1
        for (unsigned s = 0; s < SPIN_G; ++s) {
            CBAR();
            unsigned m = 0;
#pragma unroll
            for (int k = 0; k < 4; ++k) m |= ld4(ws, WS_FLAG + k * 4096 + (lane >> 1) * 128 + (lane & 1) * 4) ^ ((lane & 1) ? tagB : tagA);
            if (!any_nz(m)) break;
            __builtin_amdgcn_s_sleep(4);
        }
        asm volatile("" :: "v"(tv), "v"(tb));
        sig(CW(C_FLOK), 1u);
        if (ecu) {
            const int e2 = e - 30, fs2 = e2 / 7, mt2 = e2 % 7;
            unsigned b1a = 0, b1b = 0, b2v = 0;
            WAITF(2, C_RELQ, 4);
            if (e2 >= 0) { b1a = ((const GAS u16*)p.b1)[128 * fs2 + lane]; b1b = ((const GAS u16*)p.b1)[128 * fs2 + 64 + lane]; }
            if (u >= 0) b2v = ((const GAS u16*)p.b2)[col];
            if (e < 98) {
                const int ms1 = e % 7;
                const v4u bx0 = lds16(L_XN + (64 * ms1 + 8 * q4) * 2), bx1 = lds16(L_XN + (64 * ms1 + 32 + 8 * q4) * 2);
                WAITT(2, 14u);
#pragma unroll
                for (int cb = 0; cb < 2; ++cb) {
                    f32x4 a[4];
#pragma unroll
                    for (int nt = 0; nt < 4; ++nt) {
                        a[nt] = mfma(bx0, lds_tr<512>((24 + 2 * cb) * 4096 + TR_A(nt)), Z4);
                        a[nt] = mfma(bx1, lds_tr<512>((25 + 2 * cb) * 4096 + TR_A(nt)), a[nt]);
                    }
                    st4(ws, WS_HID + e * 512 + cb * 256 + lane * 4, san(f2u(sel4(q4, a))));
                }
            }
            if (e2 >= 0) {
                unsigned hp[14];
                WAITF(2, C_S4, 4);
#pragma unroll 1
                for (unsigned s = 0; s < SPIN_G; ++s) {
                    CBAR();
                    unsigned m = 0;
#pragma unroll
                    for (int k = 0; k < 14; ++k) hp[k] = ld4(ws, WS_HID + fs2 * 3584 + k * 256 + lane * 4);
#pragma unroll
                    for (int k = 0; k < 14; ++k) m = umax(m, hp[k]);
                    if (!any_sent(m)) break;
                    __builtin_amdgcn_s_sleep(4);
                }
                float ha = 0.f, hb = 0.f;
#pragma unroll
                for (int k = 0; k < 7; ++k) { ha += u2f(hp[2 * k]); hb += u2f(hp[2 * k + 1]); }
                ha = rbf(rbf(ha) + bflo(b1a)); hb = rbf(rbf(hb) + bflo(b1b));
                ha = ha > 0.f ? ha : 0.f; hb = hb > 0.f ? hb : 0.f;
                const unsigned hq = pk_bf16(ha * ha, hb * hb);
                *(LAS u16*)(L_HID + lane * 2) = (u16)hq; *(LAS u16*)(L_HID + 128 + lane * 2) = (u16)(hq >> 16);
                CBAR();
                WAITT(1, 16u);
                f32x4 a[4] = {Z4, Z4, Z4, Z4};
#pragma unroll
                for (int k = 0; k < 4; ++k) {
                    const v4u bh = lds16(L_HID + k * 64 + q4 * 16);
#pragma unroll
                    for (int nt = 0; nt < 4; ++nt) a[nt] = mfma(bh, lds_tr<512>((28 + k) * 4096 + TR_A(nt)), a[nt]);
                }
                st4(ws, WS_GP + fs2 * 1792 + (64 * mt2 + lane) * 4, san(f2u(sel4(q4, a))));
            }
            if (u >= 0) {
                v4u gv;
                WAITF(2, C_S4, 4);
                if (u == 0) st8(ws, lane == 0 ? WS_EPOCH : OOB, v2u{tagA, ep.y});
                int o_o = l15 < 4 ? col * 2 : OOB;
                const rsrc_t ro = mk_rsrc(p.out, 896);
                __builtin_amdgcn_raw_buffer_store_b16((short)0, ro, o_o, 0, 0);
                __builtin_amdgcn_s_sleep(DLYG);
#pragma unroll 1
                for (unsigned s = 0; s < SPIN_G; ++s) {
                    CBAR();
                    gv = ld16(ws, lane < 56 ? WS_GP + (lane >> 2) * 1792 + u * 64 + (lane & 3) * 16 : OOB);
                    if (!any_sent(umax4(gv))) break;
                    __builtin_amdgcn_s_sleep(4);
                }
                float xf = bflo(xv);
                const float ga = rbf(rbf(quad_sum4(u4f(gv))) + bflo(b2v));
                float g = rbf(ga / (1.f + __expf(-ga)));
                int o_s = WS_H4 + u * 8192 + lane * 16;
                PIN(g); PIN(xf); PIN(o_s); PIN(o_o);
                v4u sv[8];
#pragma unroll 1
                for (unsigned s = 0; s < SPIN_G; ++s) {
                    CBAR();
#pragma unroll
                    for (int k = 0; k < 8; ++k) sv[k] = __builtin_amdgcn_raw_buffer_load_b128(ws, o_s + (k & 3) * 1024, (k >> 2) * 4096, AUX_SC1);
                    unsigned m = 0, m2 = 0;
#pragma unroll
                    for (int k = 0; k < 4; ++k) { m = umax4a(m, sv[2 * k]); m2 = umax4a(m2, sv[2 * k + 1]); }
                    if (!any_sent(umax(m, m2))) break;
                }
                const float s = quad_sum4(((u4f(sv[0]) + u4f(sv[1])) + (u4f(sv[2]) + u4f(sv[3]))) + ((u4f(sv[4]) + u4f(sv[5])) + (u4f(sv[6]) + u4f(sv[7]))));
                const float o = xf + rbf(rbf(s) * g);
                __builtin_amdgcn_raw_buffer_store_b16((short)pk_bf16(o, o), ro, o_o, 0, 0);
            }
        }
        return;
    }

    if (ecu) {
        if (wv < 6) {
            const int t = wv - 2;
            int o_h0 = lane < 8 ? WS_H0 + ((((hh + HSHIFT) & 15) * 4 + dq) * 2 + hf) * 512 + t * 128 + lane * 16 : OOB;
            int o_h1 = lane < 16 ? WS_H1 + ((2 * i8 + (lane >> 3)) * 16 + hh) * 128 + (lane & 7) * 16 : OOB;
            st16(ws, o_h0, v4u{0u, 0u, 0u, 0u});
            int tra = TR_A(t);
            int a_bx = L_XN + (224 * hf + 8 * q4) * 2;
            int o_g0 = WS_H0 + (hh * 4 + t) * 1024 + lane * 16;
            PIN(tra); PIN(a_bx); PIN(o_h0); PIN(o_g0);
            WAITP(0, C_X, 1u);
            const v2u ep = *(LAS v2u*)(L_E);
            {
                const v4u xv = lds16(L_X + lane * 16);
                float ev[8] = {bflo(xv.x), bfhi(xv.x), bflo(xv.y), bfhi(xv.y), bflo(xv.z), bfhi(xv.z), bflo(xv.w), bfhi(xv.w)};
                const float s = ((ev[0] + ev[1]) + (ev[2] + ev[3])) + ((ev[4] + ev[5]) + (ev[6] + ev[7]));
                const float mean = wave_sum(s) * (1.0f / 448.0f);
                float v = 0.f;
#pragma unroll
                for (int k = 0; k < 8; ++k) { const float d = ev[k] - mean; v += d * d; }
                v = lane < 56 ? v : 0.f;
                const float var = wave_sum(v) * (1.0f / 448.0f);
                const float mb = rbf(mean);
                const float rs = 1.0f / rbf(__builtin_amdgcn_sqrtf(rbf(rbf(var) + 1e-5f)));
                float o[8];
#pragma unroll
                for (int k = 0; k < 8; ++k) o[k] = rbf(ev[k] - mb) * rs;
                *(LAS v4u*)(L_XN + lane * 16) = v4u{pk_bf16(o[0], o[1]), pk_bf16(o[2], o[3]), pk_bf16(o[4], o[5]), pk_bf16(o[6], o[7])};
                CBAR();
            }
            const unsigned tagA = ep.x + 1u, tagB = (ep.y + 0x9E3779B9u) | 1u;
            {
                v4u bx[7], a[7];
#pragma unroll
                for (int s = 0; s < 7; ++s) bx[s] = lds16(a_bx + 64 * s);
                wait_lds<0>(CW(C_PROG) + 4 * (lane & 1), (lane & 1) ? 3u : 4u);
                HI();
#pragma unroll
                for (int s = 0; s < 7; ++s) a[s] = lds_tr<512>(tra + s * 4096);
                SB();
                f32x4 acc = mfma(bx[0], a[0], Z4), acc2 = mfma(bx[1], a[1], Z4);
                acc = mfma(bx[2], a[2], acc); acc2 = mfma(bx[3], a[3], acc2); acc = mfma(bx[4], a[4], acc); acc2 = mfma(bx[5], a[5], acc2); acc = mfma(bx[6], a[6], acc);
                const float qv = acc.x + acc2.x;
                st16(ws, o_h0, v4u{f2u(qv), tagA, f2u(ror<8>(qv)), tagB});
            }
            __builtin_amdgcn_s_sleep(DLY0);
            int a_qw = L_Q + t * 128 + ((lane >> 3) & 3) * 32 + (lane & 7) * 2, a_qr[4];
#pragma unroll
            for (int k = 0; k < 4; ++k) { a_qr[k] = (l15 == 4 * k ? L_Q + t * 128 : L_ZERO) + q4 * 16; PIN(a_qr[k]); }
            int a_k0 = (8 + 4 * ((t - KROT) & 3)) * 4096 + row_a0, a_k1 = a_k0 ^ 64;
            int a_pw = L_S2P + t * 512 + lane * 4;
            int a_pr = L_S2P + l15 * 32;
            PIN(a_qw); PIN(a_k0); PIN(a_k1); PIN(o_h1); PIN(a_pw); PIN(a_pr);
            HI();
            v4u g0 = ld16(ws, o_g0);
            v4u ak[16];
            WAITT(0, 6u + 2 * ((t - KROT) & 3));
#pragma unroll
            for (int k = 0; k < 8; ++k) { ak[2 * k] = lds16(a_k0 + k * 2048); ak[2 * k + 1] = lds16(a_k1 + k * 2048); }
            sig(CW(C_RELQ + t), 1u);
            LDONE();
#pragma unroll 1
            for (unsigned s = 0; s < SPIN_G; ++s) {
                if (!any_nz((g0.y ^ tagA) | (g0.w ^ tagB))) break;
                CBAR();
                g0 = ld16(ws, o_g0);
            }
            {
                auto ra = __builtin_amdgcn_permlane32_swap(g0.x, g0.x, false, false);
                auto rb = __builtin_amdgcn_permlane32_swap(g0.z, g0.z, false, false);
                const unsigned qq = pk_bf16(u2f(ra[0]) + u2f(ra[1]), u2f(rb[0]) + u2f(rb[1]));
                *(LAS u16*)(a_qw) = (u16)qq; *(LAS u16*)(a_qw + 16) = (u16)(qq >> 16);
                CBAR();
                v4u q0[4], q1[4];
#pragma unroll
                for (int k = 0; k < 4; ++k) { q0[k] = lds16(a_qr[k]); q1[k] = lds16(a_qr[k] + 64); }
                SB();
                f32x4 a = Z4, a2 = Z4;
#pragma unroll
                for (int k = 0; k < 4; ++k) {
                    a = mfma(q0[k], ak[2 * k], a); a2 = mfma(q0[k], ak[8 + 2 * k], a2);
                    a = mfma(q1[k], ak[2 * k + 1], a); a2 = mfma(q1[k], ak[9 + 2 * k], a2);
                }
                *(LAS float*)(a_pw) = a.x;
                *(LAS float*)(a_pw + 256) = a2.x;
                sig(CW(C_QX + t), 1u);
            }
            if (t == 0) {
                wait_lds<0>(lane < 4 ? CW(C_QX + lane) : CW(C_FLOK), 1u);
                f32x4 v0[4], v1[4];
#pragma unroll
                for (int k = 0; k < 4; ++k) { v0[k] = lds16f(a_pr + k * 512); v1[k] = lds16f(a_pr + k * 512 + 16); }
                SB();
                const float sc = 0.0625f * 1.4426950408889634f;
                const f32x4 x0 = ((v0[0] + v0[1]) + (v0[2] + v0[3])) * sc, x1 = ((v1[0] + v1[1]) + (v1[2] + v1[3])) * sc;
                st16(ws, o_h1, san4(v4u{pk_bf16(x0.x, x0.y), pk_bf16(x0.z, x0.w), pk_bf16(x1.x, x1.y), pk_bf16(x1.z, x1.w)}));
                sig(CW(C_PPUB), 1u);
            }
            return;
        }
        const int pp = (wv - 7) & 1, kq = (wv - 7) >> 1;
        int o_p = WS_P + hh * 16384 + lane * 16;
        int o_s = WS_PL + (hh * 16 + RHO(4 * kq + q4)) * 128 + l15 * 4;
        int o_r = WS_PS + hh * 2048 + l15 * 4; PIN(o_r);
        int a_sw = L_ST4 + kq * 128 + l15 * 8;
        int a_p[4];
#pragma unroll
        for (int nt = 0; nt < 4; ++nt) { a_p[nt] = CSLOT(4 * kq + 3, pp) + nt * 1024 + (q4 * 16 + ((l15 + 4 * nt + (q4 & 2)) & 15)) * 16; PIN(a_p[nt]); }
        const int hd = 8 * (kq & 1) + (lane >> 3), ck = lane & 7;
        int o_h2 = WS_H2 + (hd * 2 + (i8 >> 2)) * 16384 + hh * 1024 + (2 * (i8 & 3) + 1 - pp) * 128 + ck * 16;
        int o_h2s = lane < 32 ? WS_H2S + (hh * 2 + (i8 >> 2)) * 128 + lane * 4 : OOB;
        int a_rd = pp * 4096 + (ck >> 1) * 1024 + ((ck & 1) * 32 + ((hd + 2 * ck) & 15)) * 16;
        int a_rd2 = a_rd + CSLOT(11, 0); PIN(a_rd2);
        int a_sr = L_ST4 + (kq < 2 ? hd : l15) * 8;
        int a_f4 = CW(C_P4 + pp * 4) + 4 * (lane & 3), a_s4 = CW(C_P4 + pp * 4 + kq); PIN(a_f4); PIN(a_s4);
        PIN(o_p); PIN(o_s); PIN(a_sw); PIN(o_h2); PIN(o_h2s); PIN(a_rd); PIN(a_sr);
        v4u ac[16];
        {
            const int rr = 4 * q4 + (l15 >> 2);
            const int sc_ = ((rr >> 1) & 1) | (((rr >> 3) & 1) << 1);
            const int tb = rr * 128 + (((l15 >> 1) & 1) << 4) + 8 * (l15 & 1);
            int ta[4];
#pragma unroll
            for (int nt = 0; nt < 4; ++nt) { ta[nt] = tb + ((nt ^ sc_) << 5); PIN(ta[nt]); }
#pragma unroll
            for (int u = 0; u < 4; ++u) {
                const int cbase = CSLOT(4 * kq + u, pp);
                wait_lds<1>(CW(C_PROG + pp), 17u + 4 * kq + u);
#pragma unroll
                for (int nt = 0; nt < 4; ++nt) ac[4 * u + nt] = lds_tr<2048>(cbase + ta[nt]);
            }
            if (kq == 0) sig(CW(C_RELC + pp), 1u);
            LDONE();
        }
        wait_lds<1>(lane < 2 ? CW(C_PROG + lane) : CW(C_FLOK), lane < 2 ? 32u : 1u);
        HI();
        v4u bp[4]; unsigned pr[4], pl;
        const int so[4] = {RHO(4 * kq) * 1024, RHO(4 * kq + 1) * 1024, RHO(4 * kq + 2) * 1024, RHO(4 * kq + 3) * 1024};
#pragma unroll 1
        for (unsigned s = 0; s < SPIN_G; ++s) {
            CBAR();
#pragma unroll
            for (int u = 0; u < 4; ++u) pr[u] = __builtin_amdgcn_raw_buffer_load_b32(ws, o_r, so[u] >> 3, AUX_SC1);
#pragma unroll
            for (int u = 0; u < 4; ++u) bp[u] = __builtin_amdgcn_raw_buffer_load_b128(ws, o_p, so[u], AUX_SC1);
            pl = ld4(ws, o_s);
            SB();
            if (!any_sent(umax(umax4a(umax4a(umax(umax(pr[0], pr[1]), pr[2]), bp[0]), bp[1]), umax4a(umax4a(umax(pr[3], pl), bp[2]), bp[3])))) break;
        }
        {
            const float rw = fmx(fmx3(u2f(pr[0]), u2f(pr[1]), u2f(pr[2])), u2f(pr[3]));
            f32x4 c[4] = {Z4, Z4, Z4, Z4};
#pragma unroll
            for (int u = 0; u < 4; ++u) {
                const unsigned dd = __umul24((unsigned)__builtin_amdgcn_fmed3f(rw - u2f(pr[u]), 0.f, 255.f), 0x00800080u);
                const u16x2 d2 = __builtin_bit_cast(u16x2, dd);
                const unsigned x0 = bp[u].x, x1 = bp[u].y, x2 = bp[u].z, x3 = bp[u].w;
                const v4u bs = {__builtin_bit_cast(unsigned, __builtin_elementwise_sub_sat(__builtin_bit_cast(u16x2, x0), d2)),
                                __builtin_bit_cast(unsigned, __builtin_elementwise_sub_sat(__builtin_bit_cast(u16x2, x1), d2)),
                                __builtin_bit_cast(unsigned, __builtin_elementwise_sub_sat(__builtin_bit_cast(u16x2, x2), d2)),
                                __builtin_bit_cast(unsigned, __builtin_elementwise_sub_sat(__builtin_bit_cast(u16x2, x3), d2))};
#pragma unroll
                for (int nt = 0; nt < 4; ++nt) c[nt] = mfma(ac[4 * u + nt], bs, c[nt]);
            }
            const float rq = u2f(q4 & 2 ? (q4 & 1 ? pr[3] : pr[2]) : (q4 & 1 ? pr[1] : pr[0]));
            const float lw = xrow_sum(__builtin_amdgcn_exp2f(rq - rw) * u2f(pl));
            *(LAS f32x2*)(a_sw) = f32x2{rw, lw};
#pragma unroll
            for (int nt = 0; nt < 4; ++nt) *(LAS f32x4*)(a_p[nt]) = c[nt];
            sig(a_s4, 1u);
            sig(CW(C_CG + wv - 7), 1u);
        }
        if (kq < 2) {
            wait_lds<0>(a_f4, 1u);
            f32x4 v0[4], v1[4]; f32x2 sx[4];
#pragma unroll
            for (int k = 0; k < 4; ++k) sx[k] = *(LAS f32x2*)(a_sr + k * 128);
#pragma unroll
            for (int k = 0; k < 4; ++k) { const int ad = k == 2 ? a_rd2 : a_rd + CSLOT(4 * k + 3, 0); v0[k] = lds16f(ad); v1[k] = lds16f(ad + 256); }
            SB();
            const float R = fmx(fmx3(sx[0].x, sx[1].x, sx[2].x), sx[3].x);
            float sc[4];
#pragma unroll
            for (int k = 0; k < 4; ++k) sc[k] = __builtin_amdgcn_exp2f(sx[k].x - R);
            const f32x4 a0 = (v0[0] * sc[0] + v0[1] * sc[1]) + (v0[2] * sc[2] + v0[3] * sc[3]);
            const f32x4 a1 = (v1[0] * sc[0] + v1[1] * sc[1]) + (v1[2] * sc[2] + v1[3] * sc[3]);
            st16(ws, o_h2, san4(v4u{pk_bf16(a0.x, a0.y), pk_bf16(a0.z, a0.w), pk_bf16(a1.x, a1.y), pk_bf16(a1.z, a1.w)}));
            sig(CW(C_S4 + 2 * kq + pp), 1u);
        } else if (kq == 2 && pp == 0 && (i8 & 3) == 0) {
            WAITF(0, C_P4, 4);
            f32x2 sx[4];
#pragma unroll
            for (int k = 0; k < 4; ++k) sx[k] = *(LAS f32x2*)(a_sr + k * 128);
            const float R = fmx(fmx(sx[0].x, sx[1].x), fmx(sx[2].x, sx[3].x));
            const float lg = (sx[0].y * __builtin_amdgcn_exp2f(sx[0].x - R) + sx[1].y * __builtin_amdgcn_exp2f(sx[1].x - R))
                           + (sx[2].y * __builtin_amdgcn_exp2f(sx[2].x - R) + sx[3].y * __builtin_amdgcn_exp2f(sx[3].x - R));
            st4(ws, o_h2s, san(f2u(q4 == 0 ? R : lg)));
        }
        return;
    }

    const int gw = wv < 6 ? wv - 2 : wv - 7;
#define H2_SETUP() int o_h2 = WS_H2 + (hh * 2 + hf) * 16384 + gw * 2048 + lane * 16; \
        int a_w0 = L_PART + (lane >> 3) * 2048 + gw * 256 + (((lane & 7) ^ (((gw & 1) | ((gw >> 2) << 1)) << 1)) << 4); PIN(o_h2); PIN(a_w0);

    if (wv >= 11) {
        if (wv < 13) {
        const int rb = wv - 11;
        int o_p = WS_P + (hh * 16 + 2 * i8 + rb) * 1024 + lane * 16;
        int o_ps = lane < 16 ? WS_PS + (hh * 16 + 2 * i8 + rb) * 128 + lane * 4 : OOB;
        int a_pr = 16 * 4096 + rb * 2048 + lane * 16;
        int a_f = CW(C_P3 + 8 * rb) + 4 * (lane & 7);
        PIN(o_p); PIN(o_ps); PIN(a_pr); PIN(a_f);
        WAITT(2, G1START);
        HI();
        f32x4 s0, s1;
        {
            f32x4 v0[8], v1[8];
            wait_lds<0>(a_f, 1u);
#pragma unroll
            for (int k = 0; k < 8; ++k) { v0[k] = lds16f(a_pr + k * 8192); v1[k] = lds16f(a_pr + k * 8192 + 1024); }
            SB();
            s0 = ((v0[0] + v0[1]) + (v0[2] + v0[3])) + ((v0[4] + v0[5]) + (v0[6] + v0[7]));
            s1 = ((v1[0] + v1[1]) + (v1[2] + v1[3])) + ((v1[4] + v1[5]) + (v1[6] + v1[7]));
        }
        {
            const float mx = xrow_max(fmx(fmx3(fmx3(s0.x, s0.y, s0.z), s0.w, s1.x), fmx3(s1.y, s1.z, s1.w)));
            const float r = __builtin_ceilf(mx);
            st4(ws, o_ps, san(f2u(r)));
            const float p0 = __builtin_amdgcn_exp2f(s0.x - r), p1 = __builtin_amdgcn_exp2f(s0.y - r), p2 = __builtin_amdgcn_exp2f(s0.z - r), p3 = __builtin_amdgcn_exp2f(s0.w - r);
            const float p4 = __builtin_amdgcn_exp2f(s1.x - r), p5 = __builtin_amdgcn_exp2f(s1.y - r), p6 = __builtin_amdgcn_exp2f(s1.z - r), p7 = __builtin_amdgcn_exp2f(s1.w - r);
            st16(ws, o_p, san4(v4u{pk_bf16(p0, p1), pk_bf16(p2, p3), pk_bf16(p4, p5), pk_bf16(p6, p7)}));
            const float l = xrow_sum(((p0 + p1) + (p2 + p3)) + ((p4 + p5) + (p6 + p7)));
            __builtin_amdgcn_raw_buffer_store_b32(san(f2u(l)), ws, o_ps, 32768, AUX_SC1);
            DRAIN();
            sig(CW(C_PPUB + rb), 1u);
        }
        __builtin_amdgcn_s_setprio(0);
        }
        H2_SETUP();
        WAITF(2, C_PPUB, 2);
        __builtin_amdgcn_s_sleep(DLY3);
        HI();
        v4u g2[2];
#pragma unroll 1
        for (unsigned s = 0; s < SPIN_G; ++s) {
            CBAR();
            g2[0] = ld16(ws, o_h2); g2[1] = ld16(ws, o_h2 + 1024);
            if (!any_sent(umax4a(umax4(g2[0]), g2[1]))) break;
        }
        *(LAS v4u*)(a_w0) = g2[0];
        *(LAS v4u*)(a_w0 + 128) = g2[1];
        sig(CW(C_G2 + gw), 1u);
        return;
    }

    v4u ar[16];
    {
        const int w = wv < 6 ? wv - 2 : wv - 3;
        v4u g[4];
        int a_r0 = 2 * w * 4096 + row_a0, a_r1 = a_r0 ^ 64;
        const int pw = L_PART + w * 2048;
        int o_q = WS_H1 + ((2 * i8 + 2 * w) & 15) * 2048 + lane * 16;
        int a_qw = pw + (lane >> 3) * 128 + (((lane & 7) ^ (q4 & 1) << 1) << 4), a_qw1 = (a_qw ^ 64) + 1024;
        int a_q0 = pw + row_a0, a_q1 = pw + row_a1, a_pw = (16 + 2 * w) * 4096 + lane * 16;
        PIN(a_r0); PIN(a_r1); PIN(o_q); PIN(a_qw); PIN(a_qw1); PIN(a_q0); PIN(a_q1); PIN(a_pw);
#define PULL_R(rb) do { _Pragma("unroll") for (int z = 0; z < 2; ++z) { \
            ar[8 * (rb) + 4 * z] = lds16(a_r0 + (rb) * 65536 + z * 4096); ar[8 * (rb) + 4 * z + 1] = lds16(a_r0 + (rb) * 65536 + z * 4096 + 2048); \
            ar[8 * (rb) + 4 * z + 2] = lds16(a_r1 + (rb) * 65536 + z * 4096); ar[8 * (rb) + 4 * z + 3] = lds16(a_r1 + (rb) * 65536 + z * 4096 + 2048); } } while (0)
#define MM_R(rb) do { f32x4 s0 = Z4, s1 = Z4; _Pragma("unroll") for (int z = 0; z < 2; ++z) { \
            s0 = mfma(ar[8 * (rb) + 4 * z], g[2 * z], s0); s1 = mfma(ar[8 * (rb) + 4 * z + 1], g[2 * z], s1); \
            s0 = mfma(ar[8 * (rb) + 4 * z + 2], g[2 * z + 1], s0); s1 = mfma(ar[8 * (rb) + 4 * z + 3], g[2 * z + 1], s1); } \
            *(LAS f32x4*)(a_pw + (rb) * 2048) = s0; *(LAS f32x4*)(a_pw + (rb) * 2048 + 1024) = s1; sig(CW(C_P3 + 8 * (rb) + w), 1u); } while (0)
        WAITT(1, 1u + w); PULL_R(0); sig(CW(C_RELR + w), 1u);
        wait_lds<1>(lane < 2 ? CW(C_PROG + lane) : CW(C_FLOK), lane < 2 ? G1START : 1u);
        HI();
#pragma unroll
        for (int k = 0; k < 4; ++k) g[k] = ld16(ws, o_q + k * 1024);
        WAITT(0, 9u + w); PULL_R(1);
#pragma unroll 1
        for (unsigned s = 0; s < SPIN_G; ++s) {
            if (!any_sent(umax(umax4a(umax4(g[0]), g[1]), umax4a(umax4(g[2]), g[3])))) break;
            __builtin_amdgcn_s_sleep(4);
            CBAR();
#pragma unroll
            for (int k = 0; k < 4; ++k) g[k] = ld16(ws, o_q + k * 1024);
        }
#pragma unroll
        for (int k = 0; k < 2; ++k) {
            *(LAS v4u*)(a_qw) = g[2 * k]; *(LAS v4u*)(a_qw1) = g[2 * k + 1];
            CBAR();
            g[2 * k] = lds16(a_q0); g[2 * k + 1] = lds16(a_q1);
            CBAR();
        }
        MM_R(0); MM_R(1);
        __builtin_amdgcn_s_setprio(0);
    }

    if (wv < 6) {
        const int t = wv - 2;
        v4u (&ak)[16] = ar;
        WAITT(2, 24u);
        {
            const int tra = TR_A(t);
#pragma unroll
            for (int s = 0; s < 16; ++s) ak[s] = lds_tr<512>(tra + s * 4096);
        }
        H2_SETUP();
        int o_st = lane < 8 ? WS_H2S + ((4 * t + (lane >> 1)) * 2 + hf) * 128 + (lane & 1) * 64 + hh * 4 : OOB;
        int a_sw = lane < 8 ? L_ST5 + (lane & 1) * 64 + (4 * t + (lane >> 1)) * 4 : L_X + lane * 4;
        int a_r0 = L_PART + row_a0, a_r1 = L_PART + row_a1, a_sr = L_ST5 + q4 * 16;
        int a_cw = L_CTX + (16 * t + l15) * 2;
        int a_g2 = CW(C_G2) + 4 * (lane & 7);
        PIN(o_st); PIN(a_sw); PIN(a_r0); PIN(a_r1); PIN(a_sr); PIN(a_cw); PIN(a_g2);
        LDONE();
        WAITF(1, C_PPUB, 2);
        HI();
        {
            v4u g2[2]; unsigned st;
#pragma unroll 1
            for (unsigned s = 0; s < SPIN_G; ++s) {
                CBAR();
                g2[0] = ld16(ws, o_h2); g2[1] = ld16(ws, o_h2 + 1024); st = ld4(ws, o_st);
                if (!any_sent(umax4a(umax4a(st, g2[0]), g2[1]))) break;
            }
            *(LAS v4u*)(a_w0) = g2[0];
            *(LAS v4u*)(a_w0 + 128) = g2[1];
            *(LAS unsigned*)(a_sw) = st;
            sig(CW(C_G2 + gw), 1u);
        }
        wait_lds<0>(a_g2, 1u);
        {
            const f32x4 rg = lds16f(a_sr), lg = lds16f(a_sr + 64);
            f32x4 acc = Z4, acc2 = Z4;
            v4u bb[8], bc[8];
#pragma unroll
            for (int k = 0; k < 4; ++k) { bb[2 * k] = lds16(a_r0 + k * 2048); bb[2 * k + 1] = lds16(a_r1 + k * 2048); }
            SB();
#pragma unroll
            for (int k = 0; k < 4; ++k) {
                acc = mfma(bb[2 * k], ak[2 * k], acc); acc2 = mfma(bb[2 * k + 1], ak[2 * k + 1], acc2);
                bc[2 * k] = lds16(a_r0 + (4 + k) * 2048); bc[2 * k + 1] = lds16(a_r1 + (4 + k) * 2048);
                SB();
            }
#pragma unroll
            for (int k = 0; k < 4; ++k) { acc = mfma(bc[2 * k], ak[8 + 2 * k], acc); acc2 = mfma(bc[2 * k + 1], ak[9 + 2 * k], acc2); }
            const float R = xrow_max(fmx(fmx3(rg.x, rg.y, rg.z), rg.w));
            const f32x4 sc = {__builtin_amdgcn_exp2f(rg.x - R), __builtin_amdgcn_exp2f(rg.y - R), __builtin_amdgcn_exp2f(rg.z - R), __builtin_amdgcn_exp2f(rg.w - R)};
            const float il = __builtin_amdgcn_rcpf(xrow_sum((sc.x * lg.x + sc.y * lg.y) + (sc.z * lg.z + sc.w * lg.w)));
            acc = (acc + acc2) * sc;
            const float cx = xrow_sum((acc.x + acc.y) + (acc.z + acc.w)) * il;
            const unsigned ch_ = pk_bf16(cx, cx) & 0xFFFFu;
            *(LAS u16*)(a_cw) = (u16)ch_; *(LAS u16*)(a_cw + 128) = (u16)pk_bf16(cx - bflo(ch_), 0.f);
            sig(CW(C_CTX + t), 1u);
        }
        return;
    }

    {
        const int v = wv - 7;
        int cb0 = 2 * v + 4 * hf; if (cb0 >= 7) cb0 -= 7;
        int cb1 = cb0 + 1; if (cb1 >= 7) cb1 -= 7;
        int o_h40 = WS_H4 + (4 * cb0 + q4) * 8192 + e * 64 + l15 * 4;
        int o_h41 = v < 3 ? WS_H4 + (4 * cb1 + q4) * 8192 + e * 64 + l15 * 4 : OOB;
        int a_c[4], a_cf = CW(C_CTX) + 4 * (lane & 3);
#pragma unroll
        for (int nt = 0; nt < 4; ++nt) { a_c[nt] = ((l15 >> 1) == 2 * nt ? L_CTX + (l15 & 1) * 128 : L_ZERO) + q4 * 16; PIN(a_c[nt]); }
        PIN(a_cf); PIN(o_h40); PIN(o_h41);
        WAITT(2, v < 3 ? 26u + 2 * v : 31u);
        {
            const int ob0 = (v == 0 ? 32 : 14 + 4 * v) * 4096, ob2 = (v < 3 ? 16 + 4 * v : 26) * 4096;
#pragma unroll
            for (int nt = 0; nt < 4; ++nt) {
                ar[nt] = lds_tr<512>(ob0 + TR_A(nt)); ar[4 + nt] = lds_tr<512>(ob0 + 4096 + TR_A(nt));
                ar[8 + nt] = lds_tr<512>(ob2 + TR_A(nt)); ar[12 + nt] = lds_tr<512>(ob2 + 4096 + TR_A(nt));
            }
        }
        LDONE();
        HI();
        wait_lds<0>(a_cf, 1u);
        {
            v4u c0[4], c1[4];
#pragma unroll
            for (int nt = 0; nt < 4; ++nt) c0[nt] = lds16(a_c[nt]);
            c1[0] = lds16(a_c[0] + 64); c1[1] = lds16(a_c[1] + 64);
            SB();
            f32x4 a = mfma(c0[0], ar[0], Z4), a2 = mfma(c0[0], ar[8], Z4);
            c1[2] = lds16(a_c[2] + 64); c1[3] = lds16(a_c[3] + 64);
            SB();
#pragma unroll
            for (int nt = 1; nt < 4; ++nt) { a = mfma(c0[nt], ar[nt], a); a2 = mfma(c0[nt], ar[8 + nt], a2); }
#pragma unroll
            for (int nt = 0; nt < 4; ++nt) { a = mfma(c1[nt], ar[4 + nt], a); a2 = mfma(c1[nt], ar[12 + nt], a2); }
            st4(ws, o_h40, san(f2u(a.x + a.y)));
            st4(ws, o_h41, san(f2u(a2.x + a2.y)));
        }
    }
}

extern "C" void kernel_launch(void* const* d_in, const int* in_sizes, int n_in,
                              void* d_out, int out_size, void* d_ws, size_t ws_size,
                              hipStream_t stream) {
    Params p;
    p.x = d_in[0]; p.ckv = d_in[1]; p.wq = d_in[2]; p.wuk = d_in[3]; p.wuv = d_in[4]; p.wo = d_in[5];
    p.w1 = d_in[7]; p.b1 = d_in[8]; p.w2 = d_in[9]; p.b2 = d_in[10]; p.out = d_out; p.ws = d_ws;
    (void)hipFuncSetAttribute((const void*)fused_decode, hipFuncAttributeMaxDynamicSharedMemorySize, L_END);
    fused_decode<<<256, NWAVE * 64, L_END, stream>>>(p);
}
```
